```python
import torch

def _softmax(x, axis=-1):
    return torch.softmax(x, dim=axis)
def _gelu(x):
    return torch.nn.functional.gelu(x, approximate="tanh")
def _silu(x):
    return torch.nn.functional.silu(x)
def _sigmoid(x):
    return torch.sigmoid(x)
def _layernorm(x, axis=-1, eps=1e-5):
    m = x.mean(dim=axis, keepdim=True)
    v = x.var(dim=axis, keepdim=True, unbiased=False)
    return (x - m) / torch.sqrt(v + eps)
def _scatter_add(tgt, idx, src, axis=0):
    return tgt.clone().index_add_(axis, idx, src)
def _segment_sum(src, seg, n):
    out = torch.zeros((n,) + src.shape[1:], dtype=src.dtype)
    return out.index_add_(0, seg, src)
def _pad(x, pw):
    flat = tuple(v for pair in reversed(pw) for v in pair)
    return torch.nn.functional.pad(x, flat)

kernel_name = 'recurrent_scan__mha_block_cached__dense_activation_relu__prompt'

def setup_inputs():

    _w = (lambda a: (lambda t: t.to(torch.bfloat16) if t.is_floating_point() else t)(torch.as_tensor(a)))
    def _A_setup():
        B, Sq, D = 1, 2048, 576
        H, Sk, Dh = 32, 2048, 128
        return {
            "x": _w((lambda *s: torch.randn(*s, dtype=torch.bfloat16) * 0.05)(B, Sq, D)),
            "k_cache": _w((lambda *s: torch.randn(*s, dtype=torch.bfloat16) * 0.8)(B, H, Sk, Dh)),
            "v_cache": _w((lambda *s: torch.randn(*s, dtype=torch.bfloat16) * 0.2)(B, H, Sk, Dh)),
            "wq": _w((lambda *s: torch.randn(*s, dtype=torch.bfloat16) * 0.8)(D, H * Dh) * (D ** -0.5)),
            "wo": _w((lambda *s: torch.randn(*s, dtype=torch.bfloat16) * 0.2)(H * Dh, D) * ((H * Dh) ** -0.5)),
        }
    _A_in = _A_setup()

    def _B_setup():
        B, S, D, Dff = 1, 2048, 576, 4608
        return {
            "x": _w((lambda *s: torch.randn(*s, dtype=torch.bfloat16) * 0.05)(B, S, D)),
            "w1": _w((lambda *s: torch.randn(*s, dtype=torch.bfloat16) * 0.1)(D, Dff)),
            "w2": _w((lambda *s: torch.randn(*s, dtype=torch.bfloat16) * 0.1)(Dff, D)),
        }
    _B_in = _B_setup()

    return {
        "A_x": _A_in["x"],
        "A_k_cache": _A_in["k_cache"],
        "A_v_cache": _A_in["v_cache"],
        "A_wq": _A_in["wq"],
        "A_wo": _A_in["wo"],
        "B_x": _B_in["x"],
        "B_w1": _B_in["w1"],
        "B_w2": _B_in["w2"],
    }

def reference(A_x, A_k_cache, A_v_cache, A_wq, A_wo, B_x, B_w1, B_w2):
    def _A_ref(x, k_cache, v_cache, wq, wo):
        B, Sq, D = x.shape
        H, Dh = 32, 128
        q = (lambda x, *d: x.permute(*d))((x @ wq).reshape(B, Sq, H, Dh), 0, 2, 1, 3)
        scores = torch.einsum("bhqd,bhkd->bhqk", q, k_cache) * (Dh ** -0.5)
        p = _softmax(scores, axis=-1)
        o = torch.einsum("bhqk,bhkd->bhqd", p, v_cache)
        return (lambda x, *d: x.permute(*d))(o, 0, 2, 1, 3).reshape(B, Sq, H * Dh) @ wo

    def _B_ref(x, w1, w2):
        h = torch.einsum("bsd,df->bsf", x, w1)
        zero = torch.zeros_like(h)
        h = torch.maximum(h, zero)
        return torch.einsum("bsf,fd->bsd", h, w2)

    x = A_x
    xn = _layernorm(x, axis=-1)
    s = _A_ref(xn, A_k_cache, A_v_cache, A_wq, A_wo)
    g = _silu(_B_ref(xn, B_w1, B_w2))
    return x + s * g
```

```cpp
#include <hip/hip_runtime.h>
#include <hip/hip_bf16.h>
#include <cstdio>
#include <stdint.h>

using bf16x8 = __attribute__((ext_vector_type(8))) short;
using s16x4  = __attribute__((ext_vector_type(4))) short;
using f32x16 = __attribute__((ext_vector_type(16))) float;
using f32x4  = __attribute__((ext_vector_type(4))) float;
using u32x4  = __attribute__((ext_vector_type(4))) unsigned;
typedef unsigned short u16;

constexpr int SQ = 2048, DM = 576, NH = 32, DH = 128, DFF = 4608, HD = NH * DH;

__device__ __forceinline__ float bf2f(u16 v) { return __uint_as_float(((unsigned)v) << 16); }
__device__ __forceinline__ unsigned cvtpk(float lo, float hi) {
  unsigned r; asm volatile("v_cvt_pk_bf16_f32 %0, %1, %2" : "=v"(r) : "v"(lo), "v"(hi)); return r;
}
__device__ __forceinline__ u16 f2bf(float f) { return (u16)(cvtpk(f, f) & 0xffff); }
__device__ __forceinline__ float rbf(float f) { return bf2f(f2bf(f)); }

__global__ __launch_bounds__(256) void ln_kernel(const u16* __restrict__ x, u16* __restrict__ xn) {
  int row = blockIdx.x * 4 + (threadIdx.x >> 6), lane = threadIdx.x & 63;
  float v[9]; float s = 0;
  for (int i = 0; i < 9; ++i) { v[i] = bf2f(x[row * DM + lane + 64 * i]); s += v[i]; }
  for (int o = 32; o; o >>= 1) s += __shfl_xor(s, o);
  float m = s / DM; float q = 0;
  for (int i = 0; i < 9; ++i) { float d = v[i] - m; q += d * d; }
  for (int o = 32; o; o >>= 1) q += __shfl_xor(q, o);
  float var = rbf(q / DM), mb = rbf(m);
  float den = rbf(sqrtf(rbf(var + 1e-5f)));
  for (int i = 0; i < 9; ++i) xn[row * DM + lane + 64 * i] = f2bf(rbf(v[i] - mb) / den);
}

template <int MODE>
__global__ __launch_bounds__(256) void gemm_kernel(const u16* __restrict__ A, const u16* __restrict__ B, u16* __restrict__ C, int N, int K,
                                                   const u16* __restrict__ S, const u16* __restrict__ X) {
  __shared__ __attribute__((aligned(16))) u16 As[64][40];
  __shared__ __attribute__((aligned(16))) u16 Bs[64][40];
  int tid = threadIdx.x, wid = tid >> 6, lane = tid & 63, r = lane & 31, hi = lane >> 5;
  int m0 = blockIdx.y * 64, n0 = blockIdx.x * 64, wm = (wid >> 1) * 32, wn = (wid & 1) * 32;
  f32x16 acc = {};
  for (int k0 = 0; k0 < K; k0 += 32) {
    { int row = tid >> 2, c = (tid & 3) * 8;
      *(bf16x8*)&As[row][c] = *(const bf16x8*)&A[(long)(m0 + row) * K + k0 + c]; }
    { int kk = tid >> 3, c = (tid & 7) * 8;
      bf16x8 v = *(const bf16x8*)&B[(long)(k0 + kk) * N + n0 + c];
      for (int j = 0; j < 8; ++j) Bs[c + j][kk] = (u16)v[j]; }
    __syncthreads();
    for (int s = 0; s < 2; ++s) {
      bf16x8 a = *(const bf16x8*)&As[wm + r][s * 16 + hi * 8];
      bf16x8 b = *(const bf16x8*)&Bs[wn + r][s * 16 + hi * 8];
      acc = __builtin_amdgcn_mfma_f32_32x32x16_bf16(a, b, acc, 0, 0, 0);
    }
    __syncthreads();
  }
  for (int i = 0; i < 16; ++i) {
    int row = m0 + wm + (i & 3) + 8 * (i >> 2) + 4 * hi, col = n0 + wn + r;
    float v = acc[i];
    if (MODE == 1) v = fmaxf(v, 0.f);
    if (MODE == 2) {
      float y = rbf(v); float g = rbf(y / (1.f + __expf(-y)));
      float s = bf2f(S[(long)row * N + col]);
      v = bf2f(X[(long)row * N + col]) + rbf(s * g);
    }
    C[(long)row * N + col] = f2bf(v);
  }
}

__global__ __launch_bounds__(256) void attn_kernel(const u16* __restrict__ Q, const u16* __restrict__ K, const u16* __restrict__ V, u16* __restrict__ O) {
  __shared__ __attribute__((aligned(16))) u16 Vt[128][40];
  __shared__ __attribute__((aligned(16))) u16 Ks[32][136];
  int tid = threadIdx.x, wid = tid >> 6, lane = tid & 63, r = lane & 31, hi = lane >> 5;
  int h = blockIdx.y, q0 = blockIdx.x * 128 + wid * 32;
  bf16x8 qf[8];
#pragma unroll
  for (int d0 = 0; d0 < 8; ++d0) qf[d0] = *(const bf16x8*)&Q[(long)(q0 + r) * HD + h * DH + d0 * 16 + hi * 8];
  f32x16 o[4] = {}; float m = -1e30f, l = 0;
  const float C = 0.088388347648318440f * 1.4426950408889634f;
  for (int k0 = 0; k0 < SQ; k0 += 32) {
    __syncthreads();
    for (int i = 0; i < 2; ++i) { int idx = tid + 256 * i, key = idx >> 4, c = (idx & 15) * 8;
      *(bf16x8*)&Ks[key][c] = *(const bf16x8*)&K[((long)h * SQ + k0 + key) * DH + c];
      bf16x8 v = *(const bf16x8*)&V[((long)h * SQ + k0 + key) * DH + c];
      for (int j = 0; j < 8; ++j) Vt[c + j][key] = (u16)v[j]; }
    __syncthreads();
    f32x16 s = {};
#pragma unroll
    for (int d0 = 0; d0 < 8; ++d0) { bf16x8 a = *(const bf16x8*)&Ks[r][d0 * 16 + hi * 8]; s = __builtin_amdgcn_mfma_f32_32x32x16_bf16(a, qf[d0], s, 0, 0, 0); }
    float mx = s[0];
#pragma unroll
    for (int i = 1; i < 16; ++i) mx = fmaxf(mx, s[i]);
    mx = fmaxf(mx, __shfl_xor(mx, 32));
    float mn = fmaxf(m, mx), al = exp2f((m - mn) * C); m = mn;
    float ps = 0;
#pragma unroll
    for (int i = 0; i < 16; ++i) { s[i] = exp2f((s[i] - mn) * C); ps += s[i]; }
    ps += __shfl_xor(ps, 32);
    l = l * al + ps;
#pragma unroll
    for (int db = 0; db < 4; ++db)
#pragma unroll
      for (int i = 0; i < 16; ++i) o[db][i] *= al;
#pragma unroll
    for (int ks = 0; ks < 2; ++ks) {
      u32x4 w = {cvtpk(s[8 * ks + 0], s[8 * ks + 1]), cvtpk(s[8 * ks + 2], s[8 * ks + 3]), cvtpk(s[8 * ks + 4], s[8 * ks + 5]), cvtpk(s[8 * ks + 6], s[8 * ks + 7])};
      bf16x8 p = *(bf16x8*)&w;
#pragma unroll
      for (int db = 0; db < 4; ++db) {
        s16x4 lo = *(const s16x4*)&Vt[db * 32 + r][16 * ks + 4 * hi], up = *(const s16x4*)&Vt[db * 32 + r][16 * ks + 8 + 4 * hi];
        bf16x8 a = {lo[0], lo[1], lo[2], lo[3], up[0], up[1], up[2], up[3]};
        o[db] = __builtin_amdgcn_mfma_f32_32x32x16_bf16(a, p, o[db], 0, 0, 0);
      }
    }
  }
  float il = 1.f / l;
#pragma unroll
  for (int db = 0; db < 4; ++db)
#pragma unroll
    for (int g = 0; g < 4; ++g) {
      uint2 w = {cvtpk(o[db][4 * g] * il, o[db][4 * g + 1] * il), cvtpk(o[db][4 * g + 2] * il, o[db][4 * g + 3] * il)};
      *(uint2*)&O[(long)(q0 + r) * HD + h * DH + db * 32 + 8 * g + 4 * hi] = w;
    }
}

extern "C" void kernel_launch(void* const* d_in, const int* in_sizes, int n_in, void* d_out, int out_size, void* d_ws, size_t ws_size, hipStream_t stream) {
  const u16* x = (const u16*)d_in[0]; const u16* kc = (const u16*)d_in[1]; const u16* vc = (const u16*)d_in[2];
  const u16* wq = (const u16*)d_in[3]; const u16* wo = (const u16*)d_in[4]; const u16* w1 = (const u16*)d_in[6]; const u16* w2 = (const u16*)d_in[7];
  u16* ws = (u16*)d_ws;
  u16* xn = ws; u16* q = xn + SQ * DM; u16* hh = q + SQ * HD; u16* o = hh + SQ * DFF; u16* s = o + SQ * HD;
  ln_kernel<<<SQ / 4, 256, 0, stream>>>(x, xn);
  gemm_kernel<0><<<dim3(HD / 64, SQ / 64), 256, 0, stream>>>(xn, wq, q, HD, DM, nullptr, nullptr);
  gemm_kernel<1><<<dim3(DFF / 64, SQ / 64), 256, 0, stream>>>(xn, w1, hh, DFF, DM, nullptr, nullptr);
  attn_kernel<<<dim3(SQ / 128, NH), 256, 0, stream>>>(q, kc, vc, o);
  gemm_kernel<0><<<dim3(DM / 64, SQ / 64), 256, 0, stream>>>(o, wo, s, DM, HD, nullptr, nullptr);
  gemm_kernel<2><<<dim3(DM / 64, SQ / 64), 256, 0, stream>>>(hh, w2, (u16*)d_out, DM, DFF, s, x);
}
```

```cpp
#include <hip/hip_runtime.h>

extern "C" __global__ __attribute__((aligned(4096))) __launch_bounds__(256, 1) void fused_block(const void* x, const void* k, const void* v, const void* wq, const void* wo,
                                                             const void* w1, const void* w2, void* out, void* ws) {
  __shared__ __attribute__((aligned(1024))) char lds[163840];
  const void* ka = (const void*)__builtin_amdgcn_kernarg_segment_ptr();
  unsigned bid = blockIdx.x;
  unsigned wid = __builtin_amdgcn_readfirstlane(threadIdx.x >> 6);
  asm volatile(
R"(.macro WL n
s_waitcnt lgkmcnt(\n)
.endm
.macro DM v,r,s,o
buffer_load_dwordx4 v[\v], s[\r:\r+3], s[\s] offen offset:\o lds
.endm
.macro CV d,x,y
v_cvt_pk_bf16_f32 v[\d], v[\x], v[\y]
.endm
.macro GL d,v,r,s,o
buffer_load_dwordx4 a[\d:\d+3], v[\v], s[\r:\r+3], s[\s] offen offset:\o sc1
.endm
.macro TR d,x,o
ds_read_b64_tr_b16 a[\d:\d+1], v[\x] offset:\o
.endm
.macro Mvaa d,x,y
v_mfma_f32_16x16x32_bf16 v[\d:\d+3], a[\x:\x+3], a[\y:\y+3], v[\d:\d+3]
.endm
.macro Maaa d,x,y
v_mfma_f32_16x16x32_bf16 a[\d:\d+3], a[\x:\x+3], a[\y:\y+3], a[\d:\d+3]
.endm
.macro Mvav d,x,y
v_mfma_f32_16x16x32_bf16 v[\d:\d+3], a[\x:\x+3], v[\y:\y+3], v[\d:\d+3]
.endm
s_mov_b64 s[16:17], %0
s_mov_b32 s18, %1
s_mov_b32 s19, %2
s_load_dwordx16 s[20:35], s[16:17], 0x0
s_load_dwordx2 s[36:37], s[16:17], 0x40
v_mbcnt_lo_u32_b32 v255, -1, 0
v_mbcnt_hi_u32_b32 v255, -1, v255
s_lshr_b32 s96, s18, 3
s_lshl_b32 s81, s96, 2
s_add_u32 s81, s81, s19
s_getpc_b64 s[6:7]
Lpfbase_fused_block:
s_lshl_b32 s95, s19, 4
s_lshl_b32 s97, s81, 3
s_add_u32 s97, s97, 48
v_mov_b32 v6, s97
v_mov_b32 v7, s95
v_cmp_gt_u32 vcc, 16, v255
v_min_u32 v61, 23, v255
s_nop 0
v_cndmask_b32 v6, v6, v7, vcc
v_add_u32 v6, v6, v61
v_lshlrev_b32 v6, 7, v6
v_min_u32 v6, Lcommon_fused_block-Lpfbase_fused_block, v6
global_load_dword v60, v6, s[6:7]
s_lshl_b32 s98, s18, 2
s_add_u32 s98, s98, s19
v_and_b32 v26, 31, v255
v_lshrrev_b32 v27, 5, v255
v_mul_u32_u24 v1, 1152, v27
v_lshl_add_u32 v0, v26, 4, v1
v_lshl_add_u32 v1, v26, 2, v1
v_mov_b32 v3, 0
s_mul_i32 s95, s98, 2304
WL 0
s_mov_b32 s40, s20
s_and_b32 s41, s21, 0xffff
s_mov_b32 s42, 2359296
s_mov_b32 s43, 0x20000
buffer_load_dwordx4 v[8:11], v0, s[40:43], s95 offen offset:0
buffer_load_dwordx4 v[12:15], v0, s[40:43], s95 offen offset:512
buffer_load_dword v16, v1, s[40:43], s95 offen offset:1024
s_mov_b32 s72, s36
s_and_b32 s73, s37, 0xffff
s_mov_b32 s74, 56950784
s_mov_b32 s75, 0x20000
s_mov_b32 s97, 655360
buffer_load_dwordx2 v[4:5], v3, s[72:75], s97 offen sc1
s_mov_b32 s52, s26
s_and_b32 s53, s27, 0xffff
s_mov_b32 s54, 4718592
s_mov_b32 s55, 0x20000
s_mov_b32 s60, s30
s_and_b32 s61, s31, 0xffff
s_mov_b32 s62, 5308416
s_mov_b32 s63, 0x20000
s_mov_b64 s[44:45], s[22:23]
s_mov_b64 s[48:49], s[24:25]
s_mov_b64 s[56:57], s[28:29]
s_mov_b64 s[64:65], s[32:33]
s_mov_b64 s[68:69], s[34:35]
s_mov_b32 s27, s98
s_and_b32 s95, s18, 7
s_lshl_b32 s76, s95, 2
s_and_b32 s80, s96, 3
s_add_u32 s76, s76, s80
s_lshr_b32 s77, s96, 2
s_mov_b32 s79, s77
v_lshrrev_b32 v249, 4, v255
v_and_b32 v250, 15, v255
v_add_u32 v251, 0, v249
v_and_b32 v252, 7, v251
v_lshlrev_b32 v252, 1, v252
v_xor_b32 v252, v252, v250
v_mul_u32_u24 v251, 8192, v251
v_lshl_add_u32 v212, v252, 4, v251
v_add_u32 v251, 4, v249
v_and_b32 v252, 7, v251
v_lshlrev_b32 v252, 1, v252
v_xor_b32 v252, v252, v250
v_mul_u32_u24 v251, 8192, v251
v_subrev_u32 v251, 1024, v251
v_lshl_add_u32 v213, v252, 4, v251
v_add_u32 v214, 63488, v212
v_add_u32 v215, 63488, v213
v_lshl_add_u32 v235, v249, 10, v212
v_lshl_add_u32 v236, v249, 10, v213
v_add_u32 v236, 4096, v236
v_add_u32 v237, 71680, v235
v_add_u32 v238, 71680, v236
v_lshrrev_b32 v251, 1, v255
v_mul_u32_u24 v251, 9216, v251
v_and_b32 v252, 1, v255
v_lshl_add_u32 v239, v252, 4, v251
s_lshl_b32 s84, s19, 12
s_add_u32 s85, s84, 65536
s_lshl_b32 s86, s19, 10
s_add_u32 s86, s86, 131072
s_lshl_b32 s20, s76, 8
s_lshl_b32 s95, s19, 17
s_add_u32 s20, s20, s95
s_lshl_b32 s21, s76, 8
s_mul_i32 s97, s19, 147456
s_add_u32 s21, s21, s97
s_and_b32 s22, s76, 30
s_lshr_b32 s97, s19, 1
s_add_u32 s22, s22, s97
s_lshl_b32 s22, s22, 5
s_add_u32 s22, s22, 8192
s_and_b32 s97, s19, 1
s_mul_i32 s97, s97, 294912
s_add_u32 s22, s22, s97
s_and_b32 s92, s77, 3
s_mov_b32 s91, s92
s_lshl_b32 s87, s92, 19
s_add_u32 s87, s87, s20
s_mul_i32 s89, s92, 589824
s_add_u32 s88, s89, s21
s_add_u32 s89, s89, s22
s_add_u32 s92, s92, 1
s_cmp_eq_u32 s92, 9
s_cselect_b32 s92, 0, s92
s_add_u32 m0, s84, 0
s_nop 0
DM 212,52,87,0
DM 213,52,87,1024
s_add_u32 m0, s85, 0
s_nop 0
DM 235,60,88,0
DM 236,60,88,1024
v_lshlrev_b32 v234, 4, v255
s_lshl_b32 s95, s19, 5
s_add_u32 s95, s95, 147456
v_lshlrev_b32 v27, 4, v27
v_add_u32 v27, s95, v27
v_and_b32 v251, 28, v26
v_lshl_add_u32 v28, v251, 7, v27
v_and_b32 v252, 1, v26
v_lshl_add_u32 v28, v252, 8, v28
v_and_b32 v251, 2, v26
v_lshl_add_u32 v28, v251, 2, v28
v_add_u32 v31, 4096, v28
v_lshl_add_u32 v27, v252, 2, v27
v_and_b32 v251, 8, v26
v_add_u32 v27, v27, v251
v_and_b32 v251, 6, v26
v_lshl_add_u32 v27, v251, 6, v27
v_and_b32 v251, 16, v26
v_lshl_add_u32 v27, v251, 5, v27
v_mov_b32 v2, 0x7ffffff0
s_mul_i32 s95, s19, 2304
s_add_u32 s95, s95, 147456
v_add_u32 v29, s95, v234
v_min_u32 v30, 240, v234
s_add_u32 s95, s95, 2048
v_add_u32 v30, s95, v30
v_lshrrev_b32 v0, 3, v255
v_lshlrev_b32 v0, 8, v0
v_and_b32 v1, 7, v255
v_lshl_add_u32 v0, v1, 4, v0
v_add_u32 v1, 4096, v0
v_cmp_gt_u32 vcc, 16, v255
s_lshr_b32 s95, s18, 1
s_mul_i32 s95, s95, 18432
v_cndmask_b32 v1, v2, v1, vcc
s_and_b32 s96, s18, 1
s_lshl_b32 s96, s96, 7
s_add_u32 s95, s95, s96
s_mul_i32 s96, s19, 4608
s_add_u32 s95, s95, s96
s_add_u32 s95, s95, 1048576
s_waitcnt vmcnt(5)
v_lshlrev_b32 v32, 16, v8
v_and_b32 v33, 0xffff0000, v8
v_lshlrev_b32 v34, 16, v9
v_and_b32 v35, 0xffff0000, v9
v_lshlrev_b32 v36, 16, v10
v_and_b32 v37, 0xffff0000, v10
v_lshlrev_b32 v38, 16, v11
v_and_b32 v39, 0xffff0000, v11
v_lshlrev_b32 v40, 16, v12
v_and_b32 v41, 0xffff0000, v12
v_lshlrev_b32 v42, 16, v13
v_and_b32 v43, 0xffff0000, v13
v_lshlrev_b32 v44, 16, v14
v_and_b32 v45, 0xffff0000, v14
v_lshlrev_b32 v46, 16, v15
v_and_b32 v47, 0xffff0000, v15
v_lshlrev_b32 v48, 16, v16
v_and_b32 v49, 0xffff0000, v16
v_pk_add_f32 v[64:65], v[32:33], v[40:41]
v_pk_add_f32 v[66:67], v[34:35], v[42:43]
v_pk_add_f32 v[68:69], v[36:37], v[44:45]
v_pk_add_f32 v[70:71], v[38:39], v[46:47]
v_pk_add_f32 v[64:65], v[64:65], v[68:69]
v_pk_add_f32 v[66:67], v[66:67], v[70:71]
v_pk_add_f32 v[64:65], v[64:65], v[48:49]
v_pk_add_f32 v[64:65], v[64:65], v[66:67]
v_add_f32 v20, v64, v65
s_nop 1
v_add_f32_dpp v20, v20, v20 quad_perm:[1,0,3,2] row_mask:0xf bank_mask:0xf
s_nop 1
v_add_f32_dpp v20, v20, v20 quad_perm:[2,3,0,1] row_mask:0xf bank_mask:0xf
s_nop 1
v_add_f32_dpp v20, v20, v20 row_half_mirror row_mask:0xf bank_mask:0xf
s_nop 1
v_add_f32_dpp v20, v20, v20 row_mirror row_mask:0xf bank_mask:0xf
v_mov_b32 v22, v20
s_nop 1
v_permlane16_swap_b32 v20, v22
v_add_f32 v20, v20, v22
v_mul_f32 v20, 0xbae38e39, v20
v_pk_add_f32 v[32:33], v[32:33], v[20:21] op_sel_hi:[1,0]
v_pk_add_f32 v[34:35], v[34:35], v[20:21] op_sel_hi:[1,0]
v_pk_add_f32 v[36:37], v[36:37], v[20:21] op_sel_hi:[1,0]
v_pk_add_f32 v[38:39], v[38:39], v[20:21] op_sel_hi:[1,0]
v_pk_add_f32 v[40:41], v[40:41], v[20:21] op_sel_hi:[1,0]
v_pk_add_f32 v[42:43], v[42:43], v[20:21] op_sel_hi:[1,0]
v_pk_add_f32 v[44:45], v[44:45], v[20:21] op_sel_hi:[1,0]
v_pk_add_f32 v[46:47], v[46:47], v[20:21] op_sel_hi:[1,0]
v_pk_add_f32 v[48:49], v[48:49], v[20:21] op_sel_hi:[1,0]
v_pk_mul_f32 v[24:25], v[32:33], v[32:33]
v_pk_fma_f32 v[24:25], v[34:35], v[34:35], v[24:25]
v_pk_fma_f32 v[24:25], v[36:37], v[36:37], v[24:25]
v_pk_fma_f32 v[24:25], v[38:39], v[38:39], v[24:25]
v_pk_fma_f32 v[24:25], v[40:41], v[40:41], v[24:25]
v_pk_fma_f32 v[24:25], v[42:43], v[42:43], v[24:25]
v_pk_fma_f32 v[24:25], v[44:45], v[44:45], v[24:25]
v_pk_fma_f32 v[24:25], v[46:47], v[46:47], v[24:25]
v_pk_fma_f32 v[24:25], v[48:49], v[48:49], v[24:25]
v_add_f32 v24, v24, v25
s_nop 1
v_add_f32_dpp v24, v24, v24 quad_perm:[1,0,3,2] row_mask:0xf bank_mask:0xf
s_nop 1
v_add_f32_dpp v24, v24, v24 quad_perm:[2,3,0,1] row_mask:0xf bank_mask:0xf
s_nop 1
v_add_f32_dpp v24, v24, v24 row_half_mirror row_mask:0xf bank_mask:0xf
s_nop 1
v_add_f32_dpp v24, v24, v24 row_mirror row_mask:0xf bank_mask:0xf
v_mov_b32 v22, v24
s_nop 1
v_permlane16_swap_b32 v24, v22
v_add_f32 v24, v24, v22
v_mul_f32 v24, 0x3ae38e39, v24
CV 24,24,24
v_lshlrev_b32 v24, 16, v24
v_add_f32 v24, 0x3727c5ac, v24
CV 24,24,24
v_lshlrev_b32 v24, 16, v24
v_sqrt_f32 v24, v24
s_nop 0
CV 24,24,24
v_lshlrev_b32 v24, 16, v24
v_rcp_f32 v24, v24
s_nop 0
v_mul_f32 v24, 0x3e0293ee, v24
v_pk_mul_f32 v[32:33], v[32:33], v[24:25] op_sel_hi:[1,0]
v_pk_mul_f32 v[34:35], v[34:35], v[24:25] op_sel_hi:[1,0]
v_pk_mul_f32 v[36:37], v[36:37], v[24:25] op_sel_hi:[1,0]
v_pk_mul_f32 v[38:39], v[38:39], v[24:25] op_sel_hi:[1,0]
v_pk_mul_f32 v[40:41], v[40:41], v[24:25] op_sel_hi:[1,0]
v_pk_mul_f32 v[42:43], v[42:43], v[24:25] op_sel_hi:[1,0]
v_pk_mul_f32 v[44:45], v[44:45], v[24:25] op_sel_hi:[1,0]
v_pk_mul_f32 v[46:47], v[46:47], v[24:25] op_sel_hi:[1,0]
v_pk_mul_f32 v[48:49], v[48:49], v[24:25] op_sel_hi:[1,0]
CV 8,32,33
CV 9,34,35
CV 10,36,37
CV 11,38,39
ds_write2_b64 v28, v[8:9], v[10:11] offset1:16
CV 12,40,41
CV 13,42,43
CV 14,44,45
CV 15,46,47
ds_write2_b64 v31, v[12:13], v[14:15] offset1:16
CV 16,48,49
ds_write_b32 v27, v16 offset:8192
WL 0
s_barrier
ds_read_b128 v[8:11], v29 offset:0
ds_read_b128 v[12:15], v29 offset:1024
ds_read_b128 v[16:19], v30 offset:0
WL 2
buffer_store_dwordx4 v[8:11], v0, s[72:75], s95 offen offset:0 sc1
WL 1
buffer_store_dwordx4 v[12:15], v0, s[72:75], s95 offen offset:2048 sc1
WL 0
buffer_store_dwordx4 v[16:19], v1, s[72:75], s95 offen offset:0 sc1
s_waitcnt vmcnt(7)
v_readfirstlane_b32 s82, v4
v_readfirstlane_b32 s83, v5
s_and_b32 s95, s76, 1
s_mul_i32 s95, s95, 36864
s_sub_u32 s96, 36864, s95
v_add_u32 v241, s95, v234
v_add_u32 v243, s96, v234
v_add_u32 v242, 18432, v241
v_add_u32 v244, 18432, v243
s_add_u32 s82, s82, 1
s_addc_u32 s83, s83, 0
v_lshrrev_b32 v251, 2, v255
v_and_b32 v249, 7, v251
v_and_b32 v250, 3, v255
v_lshlrev_b32 v252, 5, v251
v_lshl_add_u32 v252, v250, 3, v252
v_add_u32 v240, 131072, v252
v_lshlrev_b32 v251, 8, v251
v_lshlrev_b32 v253, 1, v249
v_add_u32 v252, 0, v250
v_xor_b32 v252, v252, v253
v_lshl_add_u32 v208, v252, 4, v251
v_add_u32 v252, 4, v250
v_xor_b32 v252, v252, v253
v_lshl_add_u32 v209, v252, 4, v251
v_add_u32 v252, 8, v250
v_xor_b32 v252, v252, v253
v_lshl_add_u32 v210, v252, 4, v251
v_add_u32 v252, 12, v250
v_xor_b32 v252, v252, v253
v_lshl_add_u32 v211, v252, 4, v251
v_lshl_add_u32 v251, v250, 3, v251
v_add_u32 v251, 65536, v251
v_xor_b32 v252, 0, v249
v_lshl_add_u32 v220, v252, 5, v251
v_xor_b32 v252, 1, v249
v_lshl_add_u32 v221, v252, 5, v251
v_xor_b32 v252, 2, v249
v_lshl_add_u32 v222, v252, 5, v251
v_xor_b32 v252, 3, v249
v_lshl_add_u32 v223, v252, 5, v251
v_xor_b32 v252, 4, v249
v_lshl_add_u32 v224, v252, 5, v251
v_xor_b32 v252, 5, v249
v_lshl_add_u32 v225, v252, 5, v251
v_xor_b32 v252, 6, v249
v_lshl_add_u32 v226, v252, 5, v251
v_xor_b32 v252, 7, v249
v_lshl_add_u32 v227, v252, 5, v251
s_lshl_b32 s95, s77, 2
s_add_u32 s95, s95, s19
s_mul_i32 s23, s95, 73728
s_add_u32 s23, s23, 1048576
s_lshl_b32 s36, s95, 12
s_lshl_b32 s96, s76, 7
s_add_u32 s37, s36, s96
s_lshl_b32 s97, s27, 7
s_waitcnt vmcnt(0)
s_add_u32 s98, s97, 0
v_mov_b32 v250, s82
v_mov_b32 v251, s83
v_lshlrev_b32 v249, 3, v255
s_mov_b64 exec, 0xffff
buffer_store_dwordx2 v[250:251], v249, s[72:75], s98 offen sc1
s_mov_b64 exec, -1
s_add_u32 m0, s84, 0
s_nop 0
DM 214,52,87,2048
DM 215,52,87,3072
s_add_u32 m0, s85, 0
s_nop 0
DM 237,60,88,2048
DM 238,60,88,3072
s_add_u32 m0, s86, 0
s_nop 0
DM 239,60,89,0
s_mov_b64 exec, 0
s_cmp_eq_u32 s19, 0
s_cbranch_scc1 Lstub0_fused_block
s_cmp_eq_u32 s19, 1
s_cbranch_scc1 Lstub1_fused_block
s_cmp_eq_u32 s19, 2
s_cbranch_scc1 Lstub2_fused_block
s_branch Lstub3_fused_block
Ltourend_fused_block:
s_mov_b64 exec, -1
s_add_u32 s98, s36, 0
v_and_b32 v249, 31, v255
v_lshlrev_b32 v249, 7, v249
s_mov_b32 s99, 0x20000
Lpoll0_fused_block:
buffer_load_dwordx2 v[250:251], v249, s[72:75], s98 offen sc1
s_waitcnt vmcnt(0)
v_cmp_ne_u64 vcc, s[82:83], v[250:251]
s_sub_u32 s99, s99, 1
s_cbranch_scc1 Lpolld0_fused_block
s_nop 2
s_cbranch_vccnz Lpoll0_fused_block
Lpolld0_fused_block:
s_lshl_b32 s90, s91, 11
s_add_u32 s90, s90, s23
s_add_u32 s91, s91, 1
s_cmp_eq_u32 s91, 9
)"
R"(s_cselect_b32 s91, 0, s91
GL 0,241,72,90,0
GL 4,241,72,90,1024
GL 8,242,72,90,0
GL 12,242,72,90,1024
GL 16,243,72,90,0
GL 20,243,72,90,1024
GL 24,244,72,90,0
GL 28,244,72,90,1024
s_barrier
TR 224,208,0
TR 226,208,4096
TR 228,208,8192
TR 230,208,12288
TR 232,208,8
TR 234,208,4104
TR 236,208,8200
TR 238,208,12296
s_waitcnt vmcnt(0) lgkmcnt(4)
v_mfma_f32_16x16x32_bf16 v[0:3], a[224:227], a[0:3], 0
TR 240,209,0
Mvaa 0,228,4
s_lshl_b32 s87, s92, 19
s_add_u32 s87, s87, s20
v_mfma_f32_16x16x32_bf16 v[32:35], a[224:227], a[8:11], 0
s_mul_i32 s89, s92, 589824
s_add_u32 s88, s89, s21
TR 242,209,4096
Mvaa 32,228,12
s_add_u32 s89, s89, s22
s_add_u32 s92, s92, 1
v_mfma_f32_16x16x32_bf16 v[64:67], a[224:227], a[16:19], 0
s_cmp_eq_u32 s92, 9
s_cselect_b32 s92, 0, s92
s_add_u32 m0, s84, 16384
TR 244,209,8192
Mvaa 64,228,20
DM 212,52,87,0
v_mfma_f32_16x16x32_bf16 v[96:99], a[224:227], a[24:27], 0
TR 246,209,12288
Mvaa 96,228,28
WL 4
v_mfma_f32_16x16x32_bf16 v[100:103], a[232:235], a[24:27], 0
TR 248,209,8
Mvaa 100,236,28
DM 213,52,87,1024
v_mfma_f32_16x16x32_bf16 v[68:71], a[232:235], a[16:19], 0
TR 250,209,4104
Mvaa 68,236,20
v_mfma_f32_16x16x32_bf16 v[36:39], a[232:235], a[8:11], 0
TR 252,209,8200
Mvaa 36,236,12
DM 214,52,87,2048
v_mfma_f32_16x16x32_bf16 v[4:7], a[232:235], a[0:3], 0
TR 254,209,12296
Mvaa 4,236,4
WL 4
v_mfma_f32_16x16x32_bf16 v[8:11], a[240:243], a[0:3], 0
TR 224,240,0
Mvaa 8,244,4
DM 215,52,87,3072
v_mfma_f32_16x16x32_bf16 v[40:43], a[240:243], a[8:11], 0
s_add_u32 m0, s85, 16384
TR 226,240,512
Mvaa 40,244,12
v_mfma_f32_16x16x32_bf16 v[72:75], a[240:243], a[16:19], 0
TR 228,240,1024
Mvaa 72,244,20
DM 235,60,88,0
v_mfma_f32_16x16x32_bf16 v[104:107], a[240:243], a[24:27], 0
TR 230,240,1536
Mvaa 104,244,28
WL 4
v_mfma_f32_16x16x32_bf16 v[108:111], a[248:251], a[24:27], 0
TR 232,210,0
Mvaa 108,252,28
TR 234,210,4096
v_mfma_f32_16x16x32_bf16 v[76:79], a[248:251], a[16:19], 0
TR 236,210,8192
Mvaa 76,252,20
TR 238,210,12288
v_mfma_f32_16x16x32_bf16 v[44:47], a[248:251], a[8:11], 0
DM 236,60,88,1024
Mvaa 44,252,12
v_mfma_f32_16x16x32_bf16 v[12:15], a[248:251], a[0:3], 0
Mvaa 12,252,4
WL 4
v_mfma_f32_16x16x32_bf16 v[144:147], a[224:227], a[0:3], 0
TR 240,210,8
Mvaa 144,228,4
TR 242,210,4104
v_mfma_f32_16x16x32_bf16 v[184:187], a[224:227], a[8:11], 0
TR 244,210,8200
Mvaa 184,228,12
WL 3
TR 246,210,12296
v_mfma_f32_16x16x32_bf16 v[112:115], a[232:235], a[24:27], 0
TR 248,211,0
Mvaa 112,236,28
DM 237,60,88,2048
v_mfma_f32_16x16x32_bf16 v[80:83], a[232:235], a[16:19], 0
TR 250,211,4096
Mvaa 80,236,20
v_mfma_f32_16x16x32_bf16 v[48:51], a[232:235], a[8:11], 0
TR 252,211,8192
Mvaa 48,236,12
DM 238,60,88,3072
v_mfma_f32_16x16x32_bf16 v[16:19], a[232:235], a[0:3], 0
s_add_u32 m0, s86, 4096
TR 254,211,12288
Mvaa 16,236,4
WL 4
v_mfma_f32_16x16x32_bf16 v[20:23], a[240:243], a[0:3], 0
TR 224,211,8
Mvaa 20,244,4
DM 239,60,89,0
v_mfma_f32_16x16x32_bf16 v[52:55], a[240:243], a[8:11], 0
s_lshl_b32 s90, s91, 11
s_add_u32 s90, s90, s23
TR 226,211,4104
Mvaa 52,244,12
s_add_u32 s91, s91, 1
s_cmp_eq_u32 s91, 9
s_cselect_b32 s91, 0, s91
v_mfma_f32_16x16x32_bf16 v[84:87], a[240:243], a[16:19], 0
TR 228,211,8200
Mvaa 84,244,20
GL 128,241,72,90,0
v_mfma_f32_16x16x32_bf16 v[116:119], a[240:243], a[24:27], 0
TR 230,211,12296
Mvaa 116,244,28
WL 4
v_mfma_f32_16x16x32_bf16 v[120:123], a[248:251], a[24:27], 0
TR 232,220,0
Mvaa 120,252,28
GL 132,241,72,90,1024
v_mfma_f32_16x16x32_bf16 v[88:91], a[248:251], a[16:19], 0
TR 234,220,4096
Mvaa 88,252,20
v_mfma_f32_16x16x32_bf16 v[56:59], a[248:251], a[8:11], 0
TR 236,220,8192
Mvaa 56,252,12
GL 136,242,72,90,0
v_mfma_f32_16x16x32_bf16 v[24:27], a[248:251], a[0:3], 0
TR 238,220,12288
Mvaa 24,252,4
WL 4
v_mfma_f32_16x16x32_bf16 v[28:31], a[224:227], a[0:3], 0
TR 240,221,0
Mvaa 28,228,4
GL 140,242,72,90,1024
v_mfma_f32_16x16x32_bf16 v[60:63], a[224:227], a[8:11], 0
TR 242,221,4096
Mvaa 60,228,12
v_mfma_f32_16x16x32_bf16 v[92:95], a[224:227], a[16:19], 0
TR 244,221,8192
Mvaa 92,228,20
GL 144,243,72,90,0
v_mfma_f32_16x16x32_bf16 v[124:127], a[224:227], a[24:27], 0
TR 246,221,12288
Mvaa 124,228,28
WL 4
v_mfma_f32_16x16x32_bf16 a[112:115], a[232:235], a[24:27], 0
TR 248,222,0
Maaa 112,236,28
GL 148,243,72,90,1024
v_mfma_f32_16x16x32_bf16 a[96:99], a[232:235], a[16:19], 0
TR 250,222,4096
Maaa 96,236,20
v_mfma_f32_16x16x32_bf16 a[80:83], a[232:235], a[8:11], 0
TR 252,222,8192
Maaa 80,236,12
GL 152,244,72,90,0
v_mfma_f32_16x16x32_bf16 a[64:67], a[232:235], a[0:3], 0
TR 254,222,12288
Maaa 64,236,4
WL 4
v_mfma_f32_16x16x32_bf16 a[68:71], a[240:243], a[0:3], 0
TR 224,240,2048
Maaa 68,244,4
GL 156,244,72,90,1024
v_mfma_f32_16x16x32_bf16 a[84:87], a[240:243], a[8:11], 0
s_lshl_b32 s90, s91, 11
s_add_u32 s90, s90, s23
TR 226,240,2560
Maaa 84,244,12
s_add_u32 s91, s91, 1
s_cmp_eq_u32 s91, 9
s_cselect_b32 s91, 0, s91
v_mfma_f32_16x16x32_bf16 a[100:103], a[240:243], a[16:19], 0
TR 228,240,3072
Maaa 100,244,20
GL 160,241,72,90,0
v_mfma_f32_16x16x32_bf16 a[116:119], a[240:243], a[24:27], 0
TR 230,240,3584
Maaa 116,244,28
WL 4
v_mfma_f32_16x16x32_bf16 a[120:123], a[248:251], a[24:27], 0
TR 232,223,0
Maaa 120,252,28
TR 234,223,4096
v_mfma_f32_16x16x32_bf16 a[104:107], a[248:251], a[16:19], 0
TR 236,223,8192
Maaa 104,252,20
TR 238,223,12288
v_mfma_f32_16x16x32_bf16 a[88:91], a[248:251], a[8:11], 0
GL 164,241,72,90,1024
Maaa 88,252,12
v_mfma_f32_16x16x32_bf16 a[72:75], a[248:251], a[0:3], 0
Maaa 72,252,4
WL 4
v_mfma_f32_16x16x32_bf16 v[164:167], a[224:227], a[0:3], 0
TR 240,224,0
Mvaa 164,228,4
TR 242,224,4096
v_mfma_f32_16x16x32_bf16 v[204:207], a[224:227], a[8:11], 0
TR 244,224,8192
Mvaa 204,228,12
WL 3
TR 246,224,12288
v_mfma_f32_16x16x32_bf16 a[124:127], a[232:235], a[24:27], 0
TR 248,225,0
Maaa 124,236,28
GL 168,242,72,90,0
v_mfma_f32_16x16x32_bf16 a[108:111], a[232:235], a[16:19], 0
TR 250,225,4096
Maaa 108,236,20
v_mfma_f32_16x16x32_bf16 a[92:95], a[232:235], a[8:11], 0
TR 252,225,8192
Maaa 92,236,12
GL 172,242,72,90,1024
v_mfma_f32_16x16x32_bf16 a[76:79], a[232:235], a[0:3], 0
TR 254,225,12288
Maaa 76,236,4
WL 4
v_mfma_f32_16x16x32_bf16 v[128:131], a[240:243], a[0:3], 0
TR 224,226,0
Mvaa 128,244,4
GL 176,243,72,90,0
v_mfma_f32_16x16x32_bf16 v[148:151], a[240:243], a[8:11], 0
TR 226,226,4096
Mvaa 148,244,12
v_mfma_f32_16x16x32_bf16 v[168:171], a[240:243], a[16:19], 0
TR 228,226,8192
Mvaa 168,244,20
Lstub0_fused_block:
s_cbranch_execz Lstub4_fused_block
GL 180,243,72,90,1024
v_mfma_f32_16x16x32_bf16 v[188:191], a[240:243], a[24:27], 0
TR 230,226,12288
Mvaa 188,244,28
WL 4
v_mfma_f32_16x16x32_bf16 v[192:195], a[248:251], a[24:27], 0
TR 232,227,0
Mvaa 192,252,28
GL 184,244,72,90,0
v_mfma_f32_16x16x32_bf16 v[172:175], a[248:251], a[16:19], 0
TR 234,227,4096
Mvaa 172,252,20
v_mfma_f32_16x16x32_bf16 v[152:155], a[248:251], a[8:11], 0
TR 236,227,8192
Mvaa 152,252,12
GL 188,244,72,90,1024
v_mfma_f32_16x16x32_bf16 v[132:135], a[248:251], a[0:3], 0
TR 238,227,12288
Mvaa 132,252,4
WL 4
v_mfma_f32_16x16x32_bf16 v[136:139], a[224:227], a[0:3], 0
Mvaa 136,228,4
v_mfma_f32_16x16x32_bf16 v[156:159], a[224:227], a[8:11], 0
Mvaa 156,228,12
s_waitcnt vmcnt(16) lgkmcnt(0)
s_barrier
v_mfma_f32_16x16x32_bf16 v[176:179], a[224:227], a[16:19], 0
TR 240,208,16384
Mvaa 176,228,20
TR 242,208,20480
v_mfma_f32_16x16x32_bf16 v[196:199], a[224:227], a[24:27], 0
TR 244,208,24576
Mvaa 196,228,28
TR 246,208,28672
v_mfma_f32_16x16x32_bf16 v[200:203], a[232:235], a[24:27], 0
TR 248,208,16392
Mvaa 200,236,28
v_mfma_f32_16x16x32_bf16 v[180:183], a[232:235], a[16:19], 0
TR 250,208,20488
Mvaa 180,236,20
v_mfma_f32_16x16x32_bf16 v[160:163], a[232:235], a[8:11], 0
TR 252,208,24584
Mvaa 160,236,12
v_mfma_f32_16x16x32_bf16 v[140:143], a[232:235], a[0:3], 0
TR 254,208,28680
Mvaa 140,236,4
s_waitcnt vmcnt(8) lgkmcnt(4)
Mvaa 0,240,128
TR 224,209,16384
v_lshrrev_b32 v249, 4, v255
Mvaa 0,244,132
s_lshl_b32 s87, s92, 19
s_add_u32 s87, s87, s20
v_and_b32 v250, 15, v255
Mvaa 32,240,136
s_mul_i32 s89, s92, 589824
s_add_u32 s88, s89, s21
TR 226,209,20480
Mvaa 32,244,140
s_add_u32 s89, s89, s22
s_add_u32 s92, s92, 1
v_add_u32 v251, 0, v249
Mvaa 64,240,144
s_cmp_eq_u32 s92, 9
s_cselect_b32 s92, 0, s92
s_add_u32 m0, s84, 32768
TR 228,209,24576
Mvaa 64,244,148
DM 212,52,87,0
v_xor_b32 v252, v251, v250
Mvaa 96,240,152
TR 230,209,28672
v_mul_u32_u24 v251, 256, v251
Mvaa 96,244,156
WL 4
v_lshl_add_u32 v228, v252, 4, v251
v_add_u32 v251, 4, v249
Mvaa 100,248,152
TR 232,209,16392
v_xor_b32 v252, v251, v250
Mvaa 100,252,156
DM 213,52,87,1024
v_mul_u32_u24 v251, 256, v251
Mvaa 68,248,144
TR 234,209,20488
v_subrev_u32 v251, 1024, v251
Mvaa 68,252,148
v_lshl_add_u32 v229, v252, 4, v251
v_add_u32 v251, 8, v249
Mvaa 36,248,136
TR 236,209,24584
v_xor_b32 v252, v251, v250
Mvaa 36,252,140
DM 214,52,87,2048
v_mul_u32_u24 v251, 256, v251
Mvaa 4,248,128
TR 238,209,28680
v_subrev_u32 v251, 2048, v251
Mvaa 4,252,132
WL 4
v_lshl_add_u32 v230, v252, 4, v251
v_add_u32 v251, 12, v249
Mvaa 8,224,128
TR 240,240,4096
v_xor_b32 v252, v251, v250
Mvaa 8,228,132
DM 215,52,87,3072
v_mul_u32_u24 v251, 256, v251
Mvaa 40,224,136
s_add_u32 m0, s85, 32768
TR 242,240,4608
Mvaa 40,228,140
v_subrev_u32 v251, 3072, v251
v_lshl_add_u32 v231, v252, 4, v251
Mvaa 72,224,144
TR 244,240,5120
v_add_u32 v251, 0, v249
Mvaa 72,228,148
DM 235,60,88,0
v_and_b32 v252, 7, v251
Mvaa 104,224,152
TR 246,240,5632
v_lshlrev_b32 v252, 1, v252
Mvaa 104,228,156
WL 4
v_xor_b32 v252, v252, v250
v_mul_u32_u24 v251, 256, v251
Mvaa 108,232,152
TR 248,210,16384
v_lshl_add_u32 v232, v252, 4, v251
Mvaa 108,236,156
TR 250,210,20480
v_add_u32 v251, 4, v249
Mvaa 76,232,144
TR 252,210,24576
v_and_b32 v252, 7, v251
Mvaa 76,236,148
TR 254,210,28672
v_lshlrev_b32 v252, 1, v252
Mvaa 44,232,136
DM 236,60,88,1024
v_xor_b32 v252, v252, v250
Mvaa 44,236,140
v_mul_u32_u24 v251, 256, v251
v_subrev_u32 v251, 1024, v251
Mvaa 12,232,128
v_lshl_add_u32 v233, v252, 4, v251
v_add_u32 v252, 0, v249
Mvaa 12,236,132
WL 4
v_xor_b32 v252, v252, v250
v_lshlrev_b32 v252, 4, v252
Mvaa 144,240,128
)"
R"(TR 224,210,16392
v_lshl_add_u32 v216, v250, 8, v252
Mvaa 144,244,132
TR 226,210,20488
v_add_u32 v252, 4, v249
Mvaa 184,240,136
TR 228,210,24584
v_xor_b32 v252, v252, v250
Mvaa 184,244,140
WL 3
TR 230,210,28680
Mvaa 112,248,152
TR 232,211,16384
v_lshlrev_b32 v252, 4, v252
Mvaa 112,252,156
DM 237,60,88,2048
v_lshl_add_u32 v217, v250, 8, v252
Mvaa 80,248,144
TR 234,211,20480
v_add_u32 v252, 8, v249
Mvaa 80,252,148
v_xor_b32 v252, v252, v250
v_lshlrev_b32 v252, 4, v252
Mvaa 48,248,136
TR 236,211,24576
v_lshl_add_u32 v218, v250, 8, v252
Mvaa 48,252,140
DM 238,60,88,3072
v_add_u32 v252, 12, v249
Mvaa 16,248,128
s_add_u32 m0, s86, 8192
TR 238,211,28672
Mvaa 16,252,132
WL 4
v_xor_b32 v252, v252, v250
v_lshlrev_b32 v252, 4, v252
Mvaa 20,224,128
TR 240,211,16392
v_lshl_add_u32 v219, v250, 8, v252
Mvaa 20,228,132
DM 239,60,89,0
Mvaa 52,224,136
s_lshl_b32 s90, s91, 11
s_add_u32 s90, s90, s23
TR 242,211,20488
Mvaa 52,228,140
s_add_u32 s91, s91, 1
s_cmp_eq_u32 s91, 9
s_cselect_b32 s91, 0, s91
Mvaa 84,224,144
TR 244,211,24584
Mvaa 84,228,148
GL 192,241,72,90,0
Mvaa 116,224,152
TR 246,211,28680
Mvaa 116,228,156
WL 4
Mvaa 120,232,152
TR 248,220,16384
Mvaa 120,236,156
GL 196,241,72,90,1024
Mvaa 88,232,144
TR 250,220,20480
Mvaa 88,236,148
Mvaa 56,232,136
TR 252,220,24576
Mvaa 56,236,140
GL 200,242,72,90,0
Mvaa 24,232,128
TR 254,220,28672
Mvaa 24,236,132
WL 4
Mvaa 28,240,128
TR 224,221,16384
Mvaa 28,244,132
GL 204,242,72,90,1024
Mvaa 60,240,136
TR 226,221,20480
Mvaa 60,244,140
Mvaa 92,240,144
TR 228,221,24576
Mvaa 92,244,148
GL 208,243,72,90,0
Mvaa 124,240,152
TR 230,221,28672
Mvaa 124,244,156
WL 4
Maaa 112,248,152
TR 232,222,16384
Maaa 112,252,156
GL 212,243,72,90,1024
Maaa 96,248,144
TR 234,222,20480
Maaa 96,252,148
Maaa 80,248,136
TR 236,222,24576
Maaa 80,252,140
GL 216,244,72,90,0
Maaa 64,248,128
TR 238,222,28672
Maaa 64,252,132
WL 4
Maaa 68,224,128
TR 240,240,6144
Maaa 68,228,132
GL 220,244,72,90,1024
Maaa 84,224,136
s_lshl_b32 s87, s92, 19
s_add_u32 s87, s87, s20
TR 242,240,6656
Maaa 84,228,140
s_mul_i32 s89, s92, 589824
s_add_u32 s88, s89, s21
Maaa 100,224,144
s_add_u32 s89, s89, s22
s_add_u32 s92, s92, 1
TR 244,240,7168
Maaa 100,228,148
s_cmp_eq_u32 s92, 9
s_cselect_b32 s92, 0, s92
s_add_u32 m0, s84, 49152
Maaa 116,224,152
TR 246,240,7680
Maaa 116,228,156
WL 4
DM 212,52,87,0
Maaa 120,232,152
TR 248,223,16384
Maaa 120,236,156
TR 250,223,20480
Maaa 104,232,144
TR 252,223,24576
Maaa 104,236,148
TR 254,223,28672
Maaa 88,232,136
DM 213,52,87,1024
Maaa 88,236,140
Maaa 72,232,128
Maaa 72,236,132
WL 4
Mvaa 164,240,128
TR 224,224,16384
Mvaa 164,244,132
TR 226,224,20480
Mvaa 204,240,136
TR 228,224,24576
Mvaa 204,244,140
WL 3
TR 230,224,28672
Maaa 124,248,152
TR 232,225,16384
Maaa 124,252,156
DM 214,52,87,2048
Maaa 108,248,144
TR 234,225,20480
Maaa 108,252,148
Maaa 92,248,136
TR 236,225,24576
Maaa 92,252,140
DM 215,52,87,3072
Maaa 76,248,128
s_add_u32 m0, s85, 49152
TR 238,225,28672
Maaa 76,252,132
WL 4
Mvaa 128,224,128
TR 240,226,16384
Mvaa 128,228,132
DM 235,60,88,0
Mvaa 148,224,136
TR 242,226,20480
Mvaa 148,228,140
Mvaa 168,224,144
TR 244,226,24576
Mvaa 168,228,148
DM 236,60,88,1024
Mvaa 188,224,152
TR 246,226,28672
Mvaa 188,228,156
WL 4
Mvaa 192,232,152
TR 248,227,16384
Mvaa 192,236,156
DM 237,60,88,2048
Mvaa 172,232,144
TR 250,227,20480
Mvaa 172,236,148
Mvaa 152,232,136
TR 252,227,24576
Mvaa 152,236,140
DM 238,60,88,3072
Mvaa 132,232,128
s_add_u32 m0, s86, 12288
TR 254,227,28672
Mvaa 132,236,132
WL 4
Mvaa 136,240,128
Mvaa 136,244,132
DM 239,60,89,0
Mvaa 156,240,136
Mvaa 156,244,140
s_waitcnt vmcnt(17) lgkmcnt(0)
s_barrier
Mvaa 176,240,144
TR 224,208,32768
Mvaa 176,244,148
TR 226,208,36864
Mvaa 196,240,152
TR 228,208,40960
Mvaa 196,244,156
TR 230,208,45056
Mvaa 200,248,152
TR 232,208,32776
Mvaa 200,252,156
Mvaa 180,248,144
TR 234,208,36872
Mvaa 180,252,148
Mvaa 160,248,136
TR 236,208,40968
Mvaa 160,252,140
Mvaa 140,248,128
TR 238,208,45064
Mvaa 140,252,132
WL 4
Mvaa 0,224,160
TR 240,209,32768
Mvaa 0,228,164
s_lshl_b32 s90, s91, 11
s_add_u32 s90, s90, s23
Mvaa 32,224,168
s_add_u32 s91, s91, 1
s_cmp_eq_u32 s91, 9
s_cselect_b32 s91, 0, s91
TR 242,209,36864
Mvaa 32,228,172
GL 32,241,72,90,0
Mvaa 64,224,176
TR 244,209,40960
Mvaa 64,228,180
Mvaa 96,224,184
TR 246,209,45056
Mvaa 96,228,188
WL 4
GL 36,241,72,90,1024
Mvaa 100,232,184
TR 248,209,32776
Mvaa 100,236,188
Mvaa 68,232,176
TR 250,209,36872
Mvaa 68,236,180
GL 40,242,72,90,0
Mvaa 36,232,168
TR 252,209,40968
Mvaa 36,236,172
Mvaa 4,232,160
TR 254,209,45064
Mvaa 4,236,164
WL 4
GL 44,242,72,90,1024
Mvaa 8,240,160
TR 224,240,8192
Mvaa 8,244,164
Mvaa 40,240,168
TR 226,240,8704
Mvaa 40,244,172
GL 48,243,72,90,0
Mvaa 72,240,176
TR 228,240,9216
Mvaa 72,244,180
Mvaa 104,240,184
TR 230,240,9728
Mvaa 104,244,188
WL 4
GL 52,243,72,90,1024
Mvaa 108,248,184
TR 232,210,32768
Mvaa 108,252,188
TR 234,210,36864
Mvaa 76,248,176
TR 236,210,40960
Mvaa 76,252,180
TR 238,210,45056
Mvaa 44,248,168
GL 56,244,72,90,0
Mvaa 44,252,172
Mvaa 12,248,160
Mvaa 12,252,164
WL 4
Mvaa 144,224,160
TR 240,210,32776
Mvaa 144,228,164
TR 242,210,36872
Mvaa 184,224,168
TR 244,210,40968
Mvaa 184,228,172
WL 3
TR 246,210,45064
Mvaa 112,232,184
TR 248,211,32768
Mvaa 112,236,188
GL 60,244,72,90,1024
Mvaa 80,232,176
s_lshl_b32 s87, s92, 19
s_add_u32 s87, s87, s20
TR 250,211,36864
Mvaa 80,236,180
s_mul_i32 s89, s92, 589824
s_add_u32 s88, s89, s21
Mvaa 48,232,168
s_add_u32 s89, s89, s22
s_add_u32 s92, s92, 1
TR 252,211,40960
Mvaa 48,236,172
s_cmp_eq_u32 s92, 9
s_cselect_b32 s92, 0, s92
s_add_u32 m0, s84, 0
Mvaa 16,232,160
TR 254,211,45056
Mvaa 16,236,164
WL 4
DM 212,52,87,0
Mvaa 20,240,160
TR 224,211,32776
Mvaa 20,244,164
Mvaa 52,240,168
TR 226,211,36872
Mvaa 52,244,172
DM 213,52,87,1024
Mvaa 84,240,176
TR 228,211,40968
Mvaa 84,244,180
Mvaa 116,240,184
TR 230,211,45064
Mvaa 116,244,188
WL 4
DM 214,52,87,2048
Mvaa 120,248,184
TR 232,220,32768
Mvaa 120,252,188
Mvaa 88,248,176
TR 234,220,36864
Mvaa 88,252,180
DM 215,52,87,3072
Mvaa 56,248,168
s_add_u32 m0, s85, 0
TR 236,220,40960
Mvaa 56,252,172
Mvaa 24,248,160
TR 238,220,45056
Mvaa 24,252,164
WL 4
DM 235,60,88,0
Mvaa 28,224,160
TR 240,221,32768
Mvaa 28,228,164
Mvaa 60,224,168
TR 242,221,36864
Mvaa 60,228,172
DM 236,60,88,1024
Mvaa 92,224,176
TR 244,221,40960
Mvaa 92,228,180
Mvaa 124,224,184
TR 246,221,45056
Mvaa 124,228,188
WL 4
DM 237,60,88,2048
Maaa 112,232,184
TR 248,222,32768
Maaa 112,236,188
Maaa 96,232,176
TR 250,222,36864
Maaa 96,236,180
DM 238,60,88,3072
Maaa 80,232,168
s_add_u32 m0, s86, 0
TR 252,222,40960
Maaa 80,236,172
Maaa 64,232,160
TR 254,222,45056
Maaa 64,236,164
WL 4
DM 239,60,89,0
Maaa 68,240,160
s_lshl_b32 s90, s91, 11
s_add_u32 s90, s90, s23
TR 224,240,10240
Maaa 68,244,164
s_add_u32 s91, s91, 1
s_cmp_eq_u32 s91, 9
s_cselect_b32 s91, 0, s91
Maaa 84,240,168
TR 226,240,10752
Maaa 84,244,172
GL 0,241,72,90,0
Maaa 100,240,176
TR 228,240,11264
Maaa 100,244,180
Maaa 116,240,184
TR 230,240,11776
)"
R"(Maaa 116,244,188
WL 4
GL 4,241,72,90,1024
Maaa 120,248,184
TR 232,223,32768
Maaa 120,252,188
TR 234,223,36864
Maaa 104,248,176
TR 236,223,40960
Maaa 104,252,180
TR 238,223,45056
Maaa 88,248,168
Lstub1_fused_block:
s_cbranch_execz Lstub5_fused_block
GL 8,242,72,90,0
Maaa 88,252,172
Maaa 72,248,160
Maaa 72,252,164
WL 4
Mvaa 164,224,160
TR 240,224,32768
Mvaa 164,228,164
TR 242,224,36864
Mvaa 204,224,168
TR 244,224,40960
Mvaa 204,228,172
WL 3
TR 246,224,45056
Maaa 124,232,184
TR 248,225,32768
Maaa 124,236,188
GL 12,242,72,90,1024
Maaa 108,232,176
TR 250,225,36864
Maaa 108,236,180
Maaa 92,232,168
TR 252,225,40960
Maaa 92,236,172
GL 16,243,72,90,0
Maaa 76,232,160
TR 254,225,45056
Maaa 76,236,164
WL 4
Mvaa 128,240,160
TR 224,226,32768
Mvaa 128,244,164
GL 20,243,72,90,1024
Mvaa 148,240,168
TR 226,226,36864
Mvaa 148,244,172
Mvaa 168,240,176
TR 228,226,40960
Mvaa 168,244,180
GL 24,244,72,90,0
Mvaa 188,240,184
TR 230,226,45056
Mvaa 188,244,188
WL 4
Mvaa 192,248,184
TR 232,227,32768
Mvaa 192,252,188
GL 28,244,72,90,1024
Mvaa 172,248,176
TR 234,227,36864
Mvaa 172,252,180
Mvaa 152,248,168
TR 236,227,40960
Mvaa 152,252,172
Mvaa 132,248,160
TR 238,227,45056
Mvaa 132,252,164
WL 4
Mvaa 136,224,160
Mvaa 136,228,164
Mvaa 156,224,168
Mvaa 156,228,172
s_waitcnt vmcnt(25) lgkmcnt(0)
s_barrier
Mvaa 176,224,176
TR 240,208,49152
Mvaa 176,228,180
TR 242,208,53248
Mvaa 196,224,184
TR 244,208,57344
Mvaa 196,228,188
TR 246,208,61440
Mvaa 200,232,184
TR 248,208,49160
Mvaa 200,236,188
Mvaa 180,232,176
TR 250,208,53256
Mvaa 180,236,180
Mvaa 160,232,168
TR 252,208,57352
Mvaa 160,236,172
Mvaa 140,232,160
TR 254,208,61448
Mvaa 140,236,164
WL 4
Mvaa 0,240,192
s_and_b32 s45, s45, 0xffff
TR 224,209,49152
Mvaa 0,244,196
s_lshl_b32 s87, s92, 19
s_add_u32 s87, s87, s20
Mvaa 32,240,200
s_mul_i32 s89, s92, 589824
s_add_u32 s88, s89, s21
TR 226,209,53248
Mvaa 32,244,204
s_add_u32 s89, s89, s22
s_add_u32 s92, s92, 1
Mvaa 64,240,208
s_cmp_eq_u32 s92, 9
s_cselect_b32 s92, 0, s92
s_add_u32 m0, s84, 16384
TR 228,209,57344
Mvaa 64,244,212
s_mov_b32 s46, 16777216
DM 212,52,87,0
Mvaa 96,240,216
s_mov_b32 s47, 0x20000
TR 230,209,61440
Mvaa 96,244,220
WL 4
Mvaa 100,248,216
s_and_b32 s49, s49, 0xffff
TR 232,209,49160
Mvaa 100,252,220
s_mov_b32 s50, 16777216
DM 213,52,87,1024
Mvaa 68,248,208
s_mov_b32 s51, 0x20000
TR 234,209,53256
Mvaa 68,252,212
s_and_b32 s57, s57, 0xffff
Mvaa 36,248,200
s_mov_b32 s58, 4718592
TR 236,209,57352
Mvaa 36,252,204
s_mov_b32 s59, 0x20000
DM 214,52,87,2048
Mvaa 4,248,192
s_and_b32 s65, s65, 0xffff
TR 238,209,61448
Mvaa 4,252,196
WL 4
Mvaa 8,224,192
s_mov_b32 s66, 5308416
TR 240,240,12288
Mvaa 8,228,196
s_mov_b32 s67, 0x20000
DM 215,52,87,3072
Mvaa 40,224,200
s_add_u32 m0, s85, 16384
TR 242,240,12800
Mvaa 40,228,204
s_and_b32 s69, s69, 0xffff
Mvaa 72,224,208
s_mov_b32 s70, 2359296
TR 244,240,13312
Mvaa 72,228,212
s_mov_b32 s71, 0x20000
DM 235,60,88,0
Mvaa 104,224,216
s_lshl_b32 s95, s77, 2
TR 246,240,13824
Mvaa 104,228,220
WL 4
Mvaa 108,232,216
s_add_u32 s95, s95, s19
TR 248,210,49152
Mvaa 108,236,220
s_lshl_b32 s33, s95, 15
TR 250,210,53248
Mvaa 76,232,208
s_add_u32 s33, s33, 36962304
TR 252,210,57344
Mvaa 76,236,212
s_lshl_b32 s34, s95, 18
TR 254,210,61440
Mvaa 44,232,200
s_add_u32 s34, s34, 3407872
DM 236,60,88,1024
Mvaa 44,236,204
s_lshl_b32 s95, s95, 1
Mvaa 12,232,192
s_and_b32 s96, s76, 1
Mvaa 12,236,196
WL 4
Mvaa 144,240,192
s_add_u32 s95, s95, s96
TR 224,210,49160
Mvaa 144,244,196
s_lshl_b32 s96, s76, 12
TR 226,210,53256
Mvaa 184,240,200
s_add_u32 s96, s96, 3407872
TR 228,210,57352
Mvaa 184,244,204
WL 3
TR 230,210,61448
Mvaa 112,248,216
s_lshl_b32 s25, s95, 18
TR 232,211,49152
Mvaa 112,252,220
s_xor_b32 s26, s25, 0x40000
DM 237,60,88,2048
Mvaa 80,248,208
s_add_u32 s25, s25, s96
TR 234,211,53248
Mvaa 80,252,212
s_add_u32 s26, s26, s96
Mvaa 48,248,200
s_lshl_b32 s32, s95, 15
TR 236,211,57344
Mvaa 48,252,204
s_lshr_b32 s96, s76, 1
DM 238,60,88,3072
Mvaa 16,248,192
s_add_u32 m0, s86, 4096
TR 238,211,61440
Mvaa 16,252,196
WL 4
Mvaa 20,224,192
s_lshl_b32 s96, s96, 10
TR 240,211,49160
Mvaa 20,228,196
s_add_u32 s32, s32, s96
DM 239,60,89,0
Mvaa 52,224,200
s_lshl_b32 s90, s91, 11
s_add_u32 s90, s90, s23
TR 242,211,53256
Mvaa 52,228,204
s_add_u32 s91, s91, 1
s_cmp_eq_u32 s91, 9
s_cselect_b32 s91, 0, s91
Mvaa 84,224,208
s_add_u32 s32, s32, 36962304
TR 244,211,57352
Mvaa 84,228,212
s_lshl_b32 s24, s76, 19
GL 128,241,72,90,0
Mvaa 116,224,216
s_add_u32 s24, s24, s84
TR 246,211,61448
Mvaa 116,228,220
WL 4
Mvaa 120,232,216
s_lshl_b32 s28, s80, 8
TR 248,220,49152
Mvaa 120,236,220
s_mul_i32 s97, s19, 18432
GL 132,241,72,90,1024
Mvaa 88,232,208
s_add_u32 s28, s28, s97
TR 250,220,53248
Mvaa 88,236,212
s_mul_i32 s29, s80, 278528
Mvaa 56,232,200
s_lshl_b32 s97, s19, 10
TR 252,220,57344
Mvaa 56,236,204
s_add_u32 s29, s29, s97
GL 136,242,72,90,0
Mvaa 24,232,192
s_add_u32 s29, s29, 39059456
TR 254,220,61440
Mvaa 24,236,196
WL 4
Mvaa 28,240,192
s_lshl_b32 s35, s79, 2
TR 224,221,49152
Mvaa 28,244,196
s_add_u32 s35, s35, s80
GL 140,242,72,90,1024
Mvaa 60,240,200
s_lshl_b32 s35, s35, 2
TR 226,221,53248
Mvaa 60,244,204
s_add_u32 s35, s35, s19
Mvaa 92,240,208
s_lshl_b32 s35, s35, 2
TR 228,221,57344
Mvaa 92,244,212
s_mov_b32 s94, 0
GL 144,243,72,90,0
Mvaa 124,240,216
s_lshl_b32 s95, s79, 1
TR 230,221,61440
Mvaa 124,244,220
WL 4
Maaa 112,248,216
s_lshr_b32 s96, s19, 1
TR 232,222,49152
Maaa 112,252,220
s_add_u32 s95, s95, s96
GL 148,243,72,90,1024
Maaa 96,248,208
s_lshl_b32 s36, s95, 12
TR 234,222,53248
Maaa 96,252,212
Maaa 80,248,200
TR 236,222,57344
Maaa 80,252,204
GL 152,244,72,90,0
Maaa 64,248,192
TR 238,222,61440
Maaa 64,252,196
WL 4
Maaa 68,224,192
TR 240,240,14336
Maaa 68,228,196
GL 156,244,72,90,1024
Maaa 84,224,200
s_lshl_b32 s87, s92, 19
s_add_u32 s87, s87, s20
TR 242,240,14848
Maaa 84,228,204
s_mul_i32 s89, s92, 589824
s_add_u32 s88, s89, s21
Maaa 100,224,208
s_add_u32 s89, s89, s22
s_add_u32 s92, s92, 1
TR 244,240,15360
Maaa 100,228,212
s_cmp_eq_u32 s92, 9
s_cselect_b32 s92, 0, s92
s_add_u32 m0, s84, 32768
Maaa 116,224,216
TR 246,240,15872
Maaa 116,228,220
WL 4
DM 212,52,87,0
Maaa 120,232,216
TR 248,223,49152
Maaa 120,236,220
TR 250,223,53248
Maaa 104,232,208
TR 252,223,57344
Maaa 104,236,212
TR 254,223,61440
Maaa 88,232,200
DM 213,52,87,1024
Maaa 88,236,204
Maaa 72,232,192
Maaa 72,236,196
WL 4
Mvaa 164,240,192
TR 224,224,49152
Mvaa 164,244,196
TR 226,224,53248
Mvaa 204,240,200
TR 228,224,57344
Mvaa 204,244,204
WL 3
TR 230,224,61440
Maaa 124,248,216
TR 232,225,49152
Maaa 124,252,220
DM 214,52,87,2048
Maaa 108,248,208
TR 234,225,53248
Maaa 108,252,212
Maaa 92,248,200
TR 236,225,57344
Maaa 92,252,204
DM 215,52,87,3072
Maaa 76,248,192
s_add_u32 m0, s85, 32768
TR 238,225,61440
Maaa 76,252,196
WL 4
Mvaa 128,224,192
TR 240,226,49152
Mvaa 128,228,196
DM 235,60,88,0
Mvaa 148,224,200
TR 242,226,53248
Mvaa 148,228,204
Mvaa 168,224,208
TR 244,226,57344
Mvaa 168,228,212
DM 236,60,88,1024
Mvaa 188,224,216
TR 246,226,61440
Mvaa 188,228,220
WL 4
Mvaa 192,232,216
TR 248,227,49152
Mvaa 192,236,220
DM 237,60,88,2048
Mvaa 172,232,208
TR 250,227,53248
Mvaa 172,236,212
Mvaa 152,232,200
TR 252,227,57344
Mvaa 152,236,204
DM 238,60,88,3072
)"
R"(Mvaa 132,232,192
s_add_u32 m0, s86, 8192
TR 254,227,61440
Mvaa 132,236,196
WL 4
Mvaa 136,240,192
Mvaa 136,244,196
DM 239,60,89,0
Mvaa 156,240,200
Mvaa 156,244,204
s_waitcnt vmcnt(34) lgkmcnt(0)
s_barrier
Mvaa 176,240,208
TR 224,208,0
Mvaa 176,244,212
TR 226,208,4096
Mvaa 196,240,216
TR 228,208,8192
Mvaa 196,244,220
TR 230,208,12288
Mvaa 200,248,216
TR 232,208,8
Mvaa 200,252,220
Mvaa 180,248,208
TR 234,208,4104
Mvaa 180,252,212
Mvaa 160,248,200
TR 236,208,8200
Mvaa 160,252,204
Mvaa 140,248,192
TR 238,208,12296
Mvaa 140,252,196
WL 4
Mvaa 0,224,32
TR 240,209,0
Mvaa 0,228,36
s_lshl_b32 s90, s91, 11
s_add_u32 s90, s90, s23
Mvaa 32,224,40
s_add_u32 s91, s91, 1
s_cmp_eq_u32 s91, 9
s_cselect_b32 s91, 0, s91
TR 242,209,4096
Mvaa 32,228,44
GL 160,241,72,90,0
Mvaa 64,224,48
TR 244,209,8192
Mvaa 64,228,52
Mvaa 96,224,56
TR 246,209,12288
Mvaa 96,228,60
WL 4
GL 164,241,72,90,1024
Mvaa 100,232,56
TR 248,209,8
Mvaa 100,236,60
Mvaa 68,232,48
TR 250,209,4104
Mvaa 68,236,52
GL 168,242,72,90,0
Mvaa 36,232,40
TR 252,209,8200
Mvaa 36,236,44
Mvaa 4,232,32
TR 254,209,12296
Mvaa 4,236,36
WL 4
GL 172,242,72,90,1024
Mvaa 8,240,32
TR 224,240,0
Mvaa 8,244,36
Mvaa 40,240,40
TR 226,240,512
Mvaa 40,244,44
GL 176,243,72,90,0
Mvaa 72,240,48
TR 228,240,1024
Mvaa 72,244,52
Mvaa 104,240,56
TR 230,240,1536
Mvaa 104,244,60
WL 4
GL 180,243,72,90,1024
Mvaa 108,248,56
TR 232,210,0
Mvaa 108,252,60
TR 234,210,4096
Mvaa 76,248,48
TR 236,210,8192
Mvaa 76,252,52
TR 238,210,12288
Mvaa 44,248,40
GL 184,244,72,90,0
Mvaa 44,252,44
Mvaa 12,248,32
Mvaa 12,252,36
WL 4
Mvaa 144,224,32
TR 240,210,8
Mvaa 144,228,36
TR 242,210,4104
Mvaa 184,224,40
TR 244,210,8200
Mvaa 184,228,44
WL 3
TR 246,210,12296
Mvaa 112,232,56
TR 248,211,0
Mvaa 112,236,60
GL 188,244,72,90,1024
Mvaa 80,232,48
s_lshl_b32 s87, s92, 19
s_add_u32 s87, s87, s20
TR 250,211,4096
Mvaa 80,236,52
s_mul_i32 s89, s92, 589824
s_add_u32 s88, s89, s21
Mvaa 48,232,40
s_add_u32 s89, s89, s22
s_add_u32 s92, s92, 1
TR 252,211,8192
Mvaa 48,236,44
s_cmp_eq_u32 s92, 9
s_cselect_b32 s92, 0, s92
s_add_u32 m0, s84, 49152
Mvaa 16,232,32
TR 254,211,12288
Mvaa 16,236,36
WL 4
DM 212,52,87,0
Mvaa 20,240,32
TR 224,211,8
Mvaa 20,244,36
Mvaa 52,240,40
TR 226,211,4104
Mvaa 52,244,44
DM 213,52,87,1024
Mvaa 84,240,48
TR 228,211,8200
Mvaa 84,244,52
Mvaa 116,240,56
TR 230,211,12296
Mvaa 116,244,60
WL 4
DM 214,52,87,2048
Mvaa 120,248,56
TR 232,220,0
Mvaa 120,252,60
Mvaa 88,248,48
TR 234,220,4096
Mvaa 88,252,52
DM 215,52,87,3072
Mvaa 56,248,40
s_add_u32 m0, s85, 49152
TR 236,220,8192
Mvaa 56,252,44
Mvaa 24,248,32
TR 238,220,12288
Mvaa 24,252,36
WL 4
DM 235,60,88,0
Mvaa 28,224,32
TR 240,221,0
Mvaa 28,228,36
Mvaa 60,224,40
TR 242,221,4096
Mvaa 60,228,44
DM 236,60,88,1024
Mvaa 92,224,48
TR 244,221,8192
Mvaa 92,228,52
Mvaa 124,224,56
TR 246,221,12288
Mvaa 124,228,60
Lstub2_fused_block:
s_cbranch_execz Lstub6_fused_block
WL 4
DM 237,60,88,2048
Maaa 112,232,56
TR 248,222,0
Maaa 112,236,60
Maaa 96,232,48
TR 250,222,4096
Maaa 96,236,52
DM 238,60,88,3072
Maaa 80,232,40
s_add_u32 m0, s86, 12288
TR 252,222,8192
Maaa 80,236,44
Maaa 64,232,32
TR 254,222,12288
Maaa 64,236,36
WL 4
DM 239,60,89,0
Maaa 68,240,32
s_lshl_b32 s90, s91, 11
s_add_u32 s90, s90, s23
TR 224,240,2048
Maaa 68,244,36
s_add_u32 s91, s91, 1
s_cmp_eq_u32 s91, 9
s_cselect_b32 s91, 0, s91
Maaa 84,240,40
TR 226,240,2560
Maaa 84,244,44
GL 192,241,72,90,0
Maaa 100,240,48
TR 228,240,3072
Maaa 100,244,52
Maaa 116,240,56
TR 230,240,3584
Maaa 116,244,60
WL 4
GL 196,241,72,90,1024
Maaa 120,248,56
TR 232,223,0
Maaa 120,252,60
TR 234,223,4096
Maaa 104,248,48
TR 236,223,8192
Maaa 104,252,52
TR 238,223,12288
Maaa 88,248,40
GL 200,242,72,90,0
Maaa 88,252,44
Maaa 72,248,32
Maaa 72,252,36
WL 4
Mvaa 164,224,32
TR 240,224,0
Mvaa 164,228,36
TR 242,224,4096
Mvaa 204,224,40
TR 244,224,8192
Mvaa 204,228,44
WL 3
TR 246,224,12288
Maaa 124,232,56
TR 248,225,0
Maaa 124,236,60
GL 204,242,72,90,1024
Maaa 108,232,48
TR 250,225,4096
Maaa 108,236,52
Maaa 92,232,40
TR 252,225,8192
Maaa 92,236,44
GL 208,243,72,90,0
Maaa 76,232,32
TR 254,225,12288
Maaa 76,236,36
WL 4
Mvaa 128,240,32
TR 224,226,0
Mvaa 128,244,36
GL 212,243,72,90,1024
Mvaa 148,240,40
TR 226,226,4096
Mvaa 148,244,44
Mvaa 168,240,48
TR 228,226,8192
Mvaa 168,244,52
GL 216,244,72,90,0
Mvaa 188,240,56
TR 230,226,12288
Mvaa 188,244,60
WL 4
Mvaa 192,248,56
TR 232,227,0
Mvaa 192,252,60
GL 220,244,72,90,1024
Mvaa 172,248,48
TR 234,227,4096
Mvaa 172,252,52
Mvaa 152,248,40
TR 236,227,8192
Mvaa 152,252,44
Mvaa 132,248,32
TR 238,227,12288
Mvaa 132,252,36
WL 4
Mvaa 136,224,32
Mvaa 136,228,36
Mvaa 156,224,40
Mvaa 156,228,44
s_waitcnt vmcnt(42) lgkmcnt(0)
s_barrier
Mvaa 176,224,48
TR 240,208,16384
Mvaa 176,228,52
TR 242,208,20480
Mvaa 196,224,56
TR 244,208,24576
Mvaa 196,228,60
TR 246,208,28672
Mvaa 200,232,56
TR 248,208,16392
Mvaa 200,236,60
Mvaa 180,232,48
TR 250,208,20488
Mvaa 180,236,52
Mvaa 160,232,40
TR 252,208,24584
Mvaa 160,236,44
Mvaa 140,232,32
TR 254,208,28680
Mvaa 140,236,36
WL 4
Mvaa 0,240,0
TR 224,209,16384
Mvaa 0,244,4
s_lshl_b32 s87, s92, 19
s_add_u32 s87, s87, s20
Mvaa 32,240,8
s_mul_i32 s89, s92, 589824
s_add_u32 s88, s89, s21
TR 226,209,20480
Mvaa 32,244,12
s_add_u32 s89, s89, s22
s_add_u32 s92, s92, 1
Mvaa 64,240,16
s_cmp_eq_u32 s92, 9
s_cselect_b32 s92, 0, s92
s_add_u32 m0, s84, 0
TR 228,209,24576
Mvaa 64,244,20
DM 212,52,87,0
Mvaa 96,240,24
TR 230,209,28672
Mvaa 96,244,28
WL 4
Mvaa 100,248,24
TR 232,209,16392
Mvaa 100,252,28
DM 213,52,87,1024
Mvaa 68,248,16
TR 234,209,20488
Mvaa 68,252,20
Mvaa 36,248,8
TR 236,209,24584
Mvaa 36,252,12
DM 214,52,87,2048
Mvaa 4,248,0
TR 238,209,28680
Mvaa 4,252,4
WL 4
Mvaa 8,224,0
TR 240,240,4096
Mvaa 8,228,4
DM 215,52,87,3072
Mvaa 40,224,8
s_add_u32 m0, s85, 0
TR 242,240,4608
Mvaa 40,228,12
Mvaa 72,224,16
TR 244,240,5120
Mvaa 72,228,20
DM 235,60,88,0
Mvaa 104,224,24
TR 246,240,5632
Mvaa 104,228,28
WL 4
Mvaa 108,232,24
TR 248,210,16384
Mvaa 108,236,28
TR 250,210,20480
Mvaa 76,232,16
TR 252,210,24576
Mvaa 76,236,20
TR 254,210,28672
Mvaa 44,232,8
DM 236,60,88,1024
Mvaa 44,236,12
Mvaa 12,232,0
Mvaa 12,236,4
WL 4
Mvaa 144,240,0
TR 224,210,16392
Mvaa 144,244,4
TR 226,210,20488
Mvaa 184,240,8
TR 228,210,24584
Mvaa 184,244,12
WL 3
TR 230,210,28680
Mvaa 112,248,24
TR 232,211,16384
Mvaa 112,252,28
DM 237,60,88,2048
Mvaa 80,248,16
TR 234,211,20480
Mvaa 80,252,20
Mvaa 48,248,8
TR 236,211,24576
Mvaa 48,252,12
DM 238,60,88,3072
Mvaa 16,248,0
s_add_u32 m0, s86, 0
TR 238,211,28672
Mvaa 16,252,4
)"
R"(WL 4
Mvaa 20,224,0
TR 240,211,16392
Mvaa 20,228,4
DM 239,60,89,0
v_min_u32 v250, 15, v255
Mvaa 52,224,8
TR 242,211,20488
v_lshlrev_b32 v250, 7, v250
Mvaa 52,228,12
s_lshl_b32 s95, s76, 6
s_add_u32 s95, s95, s77
Mvaa 84,224,16
s_lshl_b32 s95, s95, 2
s_add_u32 s95, s95, s19
TR 244,211,24584
Mvaa 84,228,20
s_lshl_b32 s95, s95, 11
buffer_load_dword v253, v250, s[44:47], s95 offen
Mvaa 116,224,24
TR 246,211,28680
Mvaa 116,228,28
WL 4
Mvaa 120,232,24
TR 248,220,16384
Mvaa 120,236,28
buffer_load_dword v253, v250, s[48:51], s95 offen
Mvaa 88,232,16
TR 250,220,20480
Mvaa 88,236,20
Mvaa 56,232,8
TR 252,220,24576
Mvaa 56,236,12
Mvaa 24,232,0
TR 254,220,28672
Mvaa 24,236,4
WL 4
Mvaa 28,240,0
TR 224,221,16384
Mvaa 28,244,4
Mvaa 60,240,8
TR 226,221,20480
Mvaa 60,244,12
Mvaa 92,240,16
TR 228,221,24576
Mvaa 92,244,20
Mvaa 124,240,24
TR 230,221,28672
Mvaa 124,244,28
WL 4
Maaa 112,248,24
TR 232,222,16384
Maaa 112,252,28
Maaa 96,248,16
TR 234,222,20480
Maaa 96,252,20
Maaa 80,248,8
TR 236,222,24576
Maaa 80,252,12
Maaa 64,248,0
TR 238,222,28672
Maaa 64,252,4
WL 4
Maaa 68,224,0
TR 240,240,6144
Maaa 68,228,4
Maaa 84,224,8
TR 242,240,6656
Maaa 84,228,12
Maaa 100,224,16
TR 244,240,7168
Maaa 100,228,20
Maaa 116,224,24
TR 246,240,7680
Maaa 116,228,28
WL 4
Maaa 120,232,24
TR 248,223,16384
Maaa 120,236,28
TR 250,223,20480
Maaa 104,232,16
TR 252,223,24576
Maaa 104,236,20
TR 254,223,28672
Maaa 88,232,8
Maaa 88,236,12
Maaa 72,232,0
Maaa 72,236,4
WL 4
Mvaa 164,240,0
TR 224,224,16384
Mvaa 164,244,4
TR 226,224,20480
Mvaa 204,240,8
TR 228,224,24576
Mvaa 204,244,12
WL 3
TR 230,224,28672
Maaa 124,248,24
TR 232,225,16384
Maaa 124,252,28
Maaa 108,248,16
TR 234,225,20480
Maaa 108,252,20
Maaa 92,248,8
TR 236,225,24576
Maaa 92,252,12
Maaa 76,248,0
TR 238,225,28672
Maaa 76,252,4
WL 4
Mvaa 128,224,0
TR 240,226,16384
Mvaa 128,228,4
Mvaa 148,224,8
TR 242,226,20480
Mvaa 148,228,12
Mvaa 168,224,16
TR 244,226,24576
Mvaa 168,228,20
Mvaa 188,224,24
TR 246,226,28672
Mvaa 188,228,28
WL 4
Mvaa 192,232,24
TR 248,227,16384
Mvaa 192,236,28
Mvaa 172,232,16
TR 250,227,20480
Mvaa 172,236,20
Mvaa 152,232,8
TR 252,227,24576
Mvaa 152,236,12
Mvaa 132,232,0
TR 254,227,28672
Mvaa 132,236,4
WL 4
Mvaa 136,240,0
Mvaa 136,244,4
Mvaa 156,240,8
Mvaa 156,244,12
s_waitcnt vmcnt(2) lgkmcnt(0)
s_barrier
Mvaa 176,240,16
TR 224,224,32768
Mvaa 176,244,20
TR 226,224,36864
Mvaa 196,240,24
TR 228,224,40960
Mvaa 196,244,28
TR 230,224,45056
Mvaa 200,248,24
TR 232,224,49152
Mvaa 200,252,28
Mvaa 180,248,16
TR 234,224,53248
Mvaa 180,252,20
Mvaa 160,248,8
TR 236,224,57344
Mvaa 160,252,12
Mvaa 140,248,0
TR 238,224,61440
Mvaa 140,252,4
WL 4
Mvaa 128,224,128
TR 240,224,0
v_lshrrev_b32 v249, 4, v255
Mvaa 128,228,132
s_add_u32 s87, s24, 0
s_add_u32 m0, s84, 16384
v_and_b32 v250, 15, v255
Mvaa 148,224,136
TR 242,224,4096
v_add_u32 v251, 0, v249
Mvaa 148,228,140
DM 228,44,87,0
v_and_b32 v252, 7, v251
Mvaa 168,224,144
TR 244,224,8192
v_lshlrev_b32 v252, 1, v252
Mvaa 168,228,148
v_xor_b32 v252, v252, v250
v_mul_u32_u24 v251, 1152, v251
Mvaa 188,224,152
TR 246,224,12288
v_lshl_add_u32 v235, v252, 4, v251
Mvaa 188,228,156
WL 4
DM 229,44,87,1024
Mvaa 188,232,184
TR 248,225,32768
v_add_u32 v251, 4, v249
Mvaa 188,236,188
v_and_b32 v252, 7, v251
v_lshlrev_b32 v252, 1, v252
Mvaa 168,232,176
TR 250,225,36864
v_xor_b32 v252, v252, v250
Mvaa 168,236,180
DM 230,44,87,2048
v_mul_u32_u24 v251, 1152, v251
Mvaa 148,232,168
TR 252,225,40960
v_subrev_u32 v251, 1024, v251
Mvaa 148,236,172
v_lshl_add_u32 v236, v252, 4, v251
v_add_u32 v237, 7168, v235
Mvaa 128,232,160
TR 254,225,45056
v_add_u32 v238, 7168, v236
Mvaa 128,236,164
WL 4
DM 231,44,87,3072
Mvaa 128,240,192
s_add_u32 m0, s85, 16384
TR 224,225,49152
Mvaa 128,244,196
v_add_u32 v246, s84, v234
v_add_u32 v246, 131072, v246
Mvaa 148,240,200
TR 226,225,53248
v_add_u32 v247, 1024, v234
Mvaa 148,244,204
DM 232,48,87,0
v_and_b32 v251, 1, v255
Mvaa 168,240,208
TR 228,225,57344
v_mul_u32_u24 v251, 0xaaaa, v251
Mvaa 168,244,212
v_add_u32 v251, 0x4444, v251
v_lshl_or_b32 v239, v251, 16, v251
Mvaa 188,240,216
TR 230,225,61440
Mvaa 188,244,220
WL 4
DM 233,48,87,1024
Mvaa 192,248,152
TR 232,225,0
Mvaa 192,252,156
Mvaa 172,248,144
TR 234,225,4096
Mvaa 172,252,148
DM 232,48,87,2048
Mvaa 152,248,136
TR 236,225,8192
Mvaa 152,252,140
Mvaa 132,248,128
TR 238,225,12288
Mvaa 132,252,132
WL 4
DM 233,48,87,3072
Mvaa 132,224,160
TR 240,220,32768
Mvaa 132,228,164
Mvaa 152,224,168
TR 242,220,36864
Mvaa 152,228,172
s_add_u32 s20, s25, 0
Mvaa 172,224,176
TR 244,220,40960
Mvaa 172,228,180
s_add_u32 s21, s25, 131072
Mvaa 192,224,184
TR 246,220,45056
Mvaa 192,228,188
WL 4
s_add_u32 s22, s26, 0
Mvaa 192,232,216
TR 248,220,49152
Mvaa 192,236,220
s_add_u32 s23, s26, 131072
Mvaa 172,232,208
TR 250,220,53248
Mvaa 172,236,212
s_add_u32 s17, s32, 16384
Mvaa 152,232,200
TR 252,220,57344
Mvaa 152,236,204
Mvaa 132,232,192
TR 254,220,61440
Mvaa 132,236,196
WL 4
Maaa 64,240,128
TR 224,220,0
Maaa 64,244,132
Maaa 80,240,136
TR 226,220,4096
Maaa 80,244,140
Maaa 96,240,144
TR 228,220,8192
CV 128,128,129
Maaa 96,244,148
CV 129,130,131
CV 130,132,133
Maaa 112,240,152
TR 230,220,12288
CV 131,134,135
Maaa 112,244,156
WL 4
v_pk_max_i16 v128, v128, 0
v_pk_max_i16 v129, v129, 0
Maaa 112,248,184
TR 232,221,32768
v_pk_max_i16 v130, v130, 0
Maaa 112,252,188
v_pk_max_i16 v131, v131, 0
CV 148,148,149
Maaa 96,248,176
TR 234,221,36864
CV 149,150,151
Maaa 96,252,180
buffer_store_dwordx4 v[128:131], v234, s[72:75], s20 offen offset:2048 sc1
CV 150,152,153
Maaa 80,248,168
TR 236,221,40960
CV 151,154,155
Maaa 80,252,172
v_pk_max_i16 v148, v148, 0
v_pk_max_i16 v149, v149, 0
Maaa 64,248,160
TR 238,221,45056
v_pk_max_i16 v150, v150, 0
Maaa 64,252,164
Lstub3_fused_block:
s_cbranch_execz Lstub7_fused_block
WL 4
v_pk_max_i16 v151, v151, 0
CV 168,168,169
Maaa 64,224,192
TR 240,221,49152
CV 169,170,171
Maaa 64,228,196
CV 170,172,173
CV 171,174,175
Maaa 80,224,200
TR 242,221,53248
v_pk_max_i16 v168, v168, 0
Maaa 80,228,204
v_pk_max_i16 v169, v169, 0
v_pk_max_i16 v170, v170, 0
Maaa 96,224,208
TR 244,221,57344
v_pk_max_i16 v171, v171, 0
Maaa 96,228,212
CV 188,188,189
CV 189,190,191
Maaa 112,224,216
TR 246,221,61440
CV 190,192,193
Maaa 112,228,220
WL 4
CV 191,194,195
v_pk_max_i16 v188, v188, 0
Maaa 116,232,152
TR 248,221,0
v_pk_max_i16 v189, v189, 0
Maaa 116,236,156
buffer_store_dwordx4 v[148:151], v234, s[72:75], s21 offen offset:2048 sc1
v_pk_max_i16 v190, v190, 0
Maaa 100,232,144
TR 250,221,4096
v_pk_max_i16 v191, v191, 0
Maaa 100,236,148
Maaa 84,232,136
TR 252,221,8192
Maaa 84,236,140
Maaa 68,232,128
TR 254,221,12288
Maaa 68,236,132
WL 4
Maaa 68,240,160
TR 224,226,32768
Maaa 68,244,164
Maaa 84,240,168
TR 226,226,36864
Maaa 84,244,172
Maaa 100,240,176
TR 228,226,40960
Maaa 100,244,180
Maaa 116,240,184
TR 230,226,45056
Maaa 116,244,188
WL 4
buffer_store_dwordx4 v[168:171], v234, s[72:75], s22 offen offset:2048 sc1
Maaa 116,248,216
TR 232,226,49152
Maaa 116,252,220
Maaa 100,248,208
TR 234,226,53248
Maaa 100,252,212
Maaa 84,248,200
TR 236,226,57344
Maaa 84,252,204
Maaa 68,248,192
TR 238,226,61440
Maaa 68,252,196
WL 4
Mvaa 136,224,128
TR 240,226,0
Mvaa 136,228,132
)"
R"(Mvaa 156,224,136
TR 242,226,4096
Mvaa 156,228,140
Mvaa 176,224,144
TR 244,226,8192
v_accvgpr_read_b32 v212, a64
Mvaa 176,228,148
buffer_store_dwordx4 v[188:191], v234, s[72:75], s23 offen offset:2048 sc1
v_accvgpr_read_b32 v213, a65
Mvaa 196,224,152
TR 246,226,12288
v_accvgpr_read_b32 v214, a66
Mvaa 196,228,156
WL 4
v_accvgpr_read_b32 v215, a67
CV 132,212,213
Mvaa 196,232,184
TR 248,227,32768
CV 133,214,215
Mvaa 196,236,188
v_accvgpr_read_b32 v212, a68
v_accvgpr_read_b32 v213, a69
Mvaa 176,232,176
TR 250,227,36864
v_accvgpr_read_b32 v214, a70
Mvaa 176,236,180
v_accvgpr_read_b32 v215, a71
CV 134,212,213
Mvaa 156,232,168
TR 252,227,40960
CV 135,214,215
Mvaa 156,236,172
v_pk_max_i16 v132, v132, 0
v_pk_max_i16 v133, v133, 0
Mvaa 136,232,160
TR 254,227,45056
v_pk_max_i16 v134, v134, 0
Mvaa 136,236,164
WL 4
v_pk_max_i16 v135, v135, 0
v_accvgpr_read_b32 v212, a80
Mvaa 136,240,192
TR 224,227,49152
v_accvgpr_read_b32 v213, a81
Mvaa 136,244,196
v_accvgpr_read_b32 v214, a82
v_accvgpr_read_b32 v215, a83
Mvaa 156,240,200
TR 226,227,53248
CV 152,212,213
Mvaa 156,244,204
buffer_store_dwordx4 v[132:135], v234, s[72:75], s20 offen offset:0 sc1
CV 153,214,215
Mvaa 176,240,208
TR 228,227,57344
v_accvgpr_read_b32 v212, a84
Mvaa 176,244,212
v_accvgpr_read_b32 v213, a85
v_accvgpr_read_b32 v214, a86
Mvaa 196,240,216
TR 230,227,61440
v_accvgpr_read_b32 v215, a87
Mvaa 196,244,220
WL 4
CV 154,212,213
CV 155,214,215
Mvaa 200,248,152
TR 232,227,0
v_pk_max_i16 v152, v152, 0
Mvaa 200,252,156
v_pk_max_i16 v153, v153, 0
v_pk_max_i16 v154, v154, 0
Mvaa 180,248,144
TR 234,227,4096
v_pk_max_i16 v155, v155, 0
Mvaa 180,252,148
v_accvgpr_read_b32 v212, a96
v_accvgpr_read_b32 v213, a97
Mvaa 160,248,136
TR 236,227,8192
v_accvgpr_read_b32 v214, a98
Mvaa 160,252,140
v_accvgpr_read_b32 v215, a99
CV 172,212,213
Mvaa 140,248,128
TR 238,227,12288
CV 173,214,215
Mvaa 140,252,132
WL 4
v_accvgpr_read_b32 v212, a100
v_accvgpr_read_b32 v213, a101
Mvaa 140,224,160
TR 240,222,32768
v_accvgpr_read_b32 v214, a102
Mvaa 140,228,164
buffer_store_dwordx4 v[152:155], v234, s[72:75], s21 offen offset:0 sc1
v_accvgpr_read_b32 v215, a103
Mvaa 160,224,168
TR 242,222,36864
CV 174,212,213
Mvaa 160,228,172
CV 175,214,215
v_pk_max_i16 v172, v172, 0
Mvaa 180,224,176
TR 244,222,40960
v_pk_max_i16 v173, v173, 0
Mvaa 180,228,180
v_pk_max_i16 v174, v174, 0
v_pk_max_i16 v175, v175, 0
Mvaa 200,224,184
TR 246,222,45056
v_accvgpr_read_b32 v212, a112
Mvaa 200,228,188
WL 4
v_accvgpr_read_b32 v213, a113
v_accvgpr_read_b32 v214, a114
Mvaa 200,232,216
TR 248,240,8192
v_accvgpr_read_b32 v215, a115
Mvaa 200,236,220
CV 192,212,213
CV 193,214,215
Mvaa 180,232,208
TR 250,240,8704
v_accvgpr_read_b32 v212, a116
Mvaa 180,236,212
v_accvgpr_read_b32 v213, a117
v_accvgpr_read_b32 v214, a118
Mvaa 160,232,200
TR 252,240,9216
v_accvgpr_read_b32 v215, a119
Mvaa 160,236,204
CV 194,212,213
CV 195,214,215
Mvaa 140,232,192
TR 254,240,9728
v_pk_max_i16 v192, v192, 0
Mvaa 140,236,196
WL 4
buffer_store_dwordx4 v[172:175], v234, s[72:75], s22 offen offset:0 sc1
Maaa 72,240,128
TR 224,222,49152
v_pk_max_i16 v193, v193, 0
Maaa 72,244,132
TR 226,222,53248
v_pk_max_i16 v194, v194, 0
Maaa 88,240,136
TR 228,222,57344
v_pk_max_i16 v195, v195, 0
Maaa 88,244,140
TR 230,222,61440
Maaa 104,240,144
CV 136,136,137
CV 137,138,139
Maaa 104,244,148
CV 138,140,141
CV 139,142,143
Maaa 120,240,152
v_pk_max_i16 v136, v136, 0
v_pk_max_i16 v137, v137, 0
Maaa 120,244,156
WL 4
v_pk_max_i16 v138, v138, 0
v_pk_max_i16 v139, v139, 0
Mvaa 184,248,136
TR 232,240,12288
CV 156,156,157
Mvaa 184,252,140
TR 234,240,12800
CV 157,158,159
Mvaa 144,248,128
TR 236,240,13312
CV 158,160,161
Mvaa 144,252,132
WL 3
TR 238,240,13824
Maaa 72,224,160
TR 240,222,0
CV 159,162,163
Maaa 72,228,164
TR 242,222,4096
v_pk_max_i16 v156, v156, 0
Maaa 88,224,168
TR 244,222,8192
v_pk_max_i16 v157, v157, 0
Maaa 88,228,172
TR 246,222,12288
v_pk_max_i16 v158, v158, 0
Maaa 104,224,176
buffer_store_dwordx4 v[192:195], v234, s[72:75], s23 offen offset:0 sc1
v_pk_max_i16 v159, v159, 0
Maaa 104,228,180
ds_write_b128 v246, v[136:139] offset:16384
CV 176,176,177
Maaa 120,224,184
ds_write_b128 v246, v[156:159] offset:17408
CV 177,178,179
Maaa 120,228,188
WL 6
CV 178,180,181
CV 179,182,183
Mvaa 184,232,168
TR 248,240,0
v_pk_max_i16 v176, v176, 0
Mvaa 184,236,172
TR 250,240,512
v_pk_max_i16 v177, v177, 0
Mvaa 144,232,160
TR 252,240,1024
v_pk_max_i16 v178, v178, 0
Mvaa 144,236,164
WL 5
TR 254,240,1536
Maaa 72,240,192
TR 224,223,32768
v_pk_max_i16 v179, v179, 0
Maaa 72,244,196
TR 226,223,36864
CV 196,196,197
Maaa 88,240,200
TR 228,223,40960
CV 197,198,199
Maaa 88,244,204
TR 230,223,45056
CV 198,200,201
Maaa 104,240,208
ds_write_b128 v246, v[176:179] offset:18432
CV 199,202,203
Maaa 104,244,212
v_pk_max_i16 v196, v196, 0
v_pk_max_i16 v197, v197, 0
Maaa 120,240,216
v_pk_max_i16 v198, v198, 0
v_pk_max_i16 v199, v199, 0
Maaa 120,244,220
WL 5
ds_write_b128 v246, v[196:199] offset:19456
Mvaa 184,248,200
TR 232,240,10240
Mvaa 184,252,204
TR 234,240,10752
Mvaa 144,248,192
TR 236,240,11264
Mvaa 144,252,196
WL 5
TR 238,240,11776
Maaa 76,224,128
TR 240,223,49152
Maaa 76,228,132
TR 242,223,53248
Maaa 92,224,136
TR 244,223,57344
Maaa 92,228,140
TR 246,223,61440
Maaa 108,224,144
Maaa 108,228,148
Maaa 124,224,152
Maaa 124,228,156
WL 4
Mvaa 204,232,136
TR 248,240,14336
Mvaa 204,236,140
TR 250,240,14848
Mvaa 164,232,128
TR 252,240,15360
Mvaa 164,236,132
WL 3
TR 254,240,15872
Maaa 76,240,160
TR 224,223,0
Maaa 76,244,164
TR 226,223,4096
Maaa 92,240,168
TR 228,223,8192
Maaa 92,244,172
TR 230,223,12288
Maaa 108,240,176
Maaa 108,244,180
Maaa 124,240,184
Maaa 124,244,188
WL 4
Mvaa 204,248,168
TR 232,240,2048
Mvaa 204,252,172
TR 234,240,2560
Mvaa 164,248,160
TR 236,240,3072
Mvaa 164,252,164
WL 3
TR 238,240,3584
Maaa 76,224,192
TR 240,208,32768
Maaa 76,228,196
TR 242,208,36864
Maaa 92,224,200
TR 244,208,40960
Maaa 92,228,204
TR 246,208,45056
Maaa 108,224,208
Maaa 108,228,212
Maaa 124,224,216
Maaa 124,228,220
WL 4
Mvaa 204,232,200
TR 248,208,32776
Mvaa 204,236,204
TR 250,208,36872
Mvaa 164,232,192
TR 252,208,40968
Mvaa 164,236,196
WL 3
TR 254,208,45064
Mvaa 0,240,128
TR 224,209,32768
v_accvgpr_read_b32 v212, a72
Mvaa 0,244,132
s_barrier
v_accvgpr_read_b32 v213, a73
Mvaa 32,240,136
s_add_u32 s87, s24, 16384
s_add_u32 m0, s85, 32768
TR 226,209,36864
Mvaa 32,244,140
DM 232,48,87,0
v_accvgpr_read_b32 v214, a74
Mvaa 64,240,144
TR 228,209,40960
v_accvgpr_read_b32 v215, a75
Mvaa 64,244,148
CV 140,212,213
CV 141,214,215
Mvaa 96,240,152
TR 230,209,45056
v_accvgpr_read_b32 v212, a76
Mvaa 96,244,156
WL 4
v_accvgpr_read_b32 v213, a77
v_accvgpr_read_b32 v214, a78
Mvaa 100,248,152
TR 232,209,32776
v_accvgpr_read_b32 v215, a79
Mvaa 100,252,156
CV 142,212,213
CV 143,214,215
Mvaa 68,248,144
TR 234,209,36872
v_pk_max_i16 v140, v140, 0
Mvaa 68,252,148
DM 233,48,87,1024
v_pk_max_i16 v141, v141, 0
Mvaa 36,248,136
TR 236,209,40968
v_pk_max_i16 v142, v142, 0
Mvaa 36,252,140
v_pk_max_i16 v143, v143, 0
v_accvgpr_read_b32 v212, a88
Mvaa 4,248,128
TR 238,209,45064
v_accvgpr_read_b32 v213, a89
Mvaa 4,252,132
WL 4
ds_write_b128 v246, v[140:143] offset:0
Mvaa 8,224,128
TR 240,210,32768
v_accvgpr_read_b32 v214, a90
Mvaa 8,228,132
v_accvgpr_read_b32 v215, a91
CV 160,212,213
Mvaa 40,224,136
TR 242,210,36864
CV 161,214,215
Mvaa 40,228,140
DM 232,48,87,2048
v_accvgpr_read_b32 v212, a92
Mvaa 72,224,144
TR 244,210,40960
v_accvgpr_read_b32 v213, a93
Mvaa 72,228,148
v_accvgpr_read_b32 v214, a94
v_accvgpr_read_b32 v215, a95
Mvaa 104,224,152
TR 246,210,45056
CV 162,212,213
Mvaa 104,228,156
WL 5
CV 163,214,215
v_pk_max_i16 v160, v160, 0
Mvaa 108,232,152
TR 248,210,32776
v_pk_max_i16 v161, v161, 0
Mvaa 108,236,156
v_pk_max_i16 v162, v162, 0
v_pk_max_i16 v163, v163, 0
Mvaa 76,232,144
TR 250,210,36872
v_accvgpr_read_b32 v212, a104
Mvaa 76,236,148
DM 233,48,87,3072
v_accvgpr_read_b32 v213, a105
Mvaa 44,232,136
)"
R"(TR 252,210,40968
v_accvgpr_read_b32 v214, a106
Mvaa 44,236,140
ds_write_b128 v246, v[160:163] offset:1024
v_accvgpr_read_b32 v215, a107
Mvaa 12,232,128
TR 254,210,45064
CV 180,212,213
Mvaa 12,236,132
WL 5
CV 181,214,215
v_accvgpr_read_b32 v212, a108
Mvaa 16,240,128
TR 224,211,32768
v_accvgpr_read_b32 v213, a109
Mvaa 16,244,132
v_accvgpr_read_b32 v214, a110
v_accvgpr_read_b32 v215, a111
Mvaa 48,240,136
TR 226,211,36864
CV 182,212,213
Mvaa 48,244,140
CV 183,214,215
v_pk_max_i16 v180, v180, 0
Mvaa 80,240,144
TR 228,211,40960
v_pk_max_i16 v181, v181, 0
Mvaa 80,244,148
v_pk_max_i16 v182, v182, 0
v_pk_max_i16 v183, v183, 0
Mvaa 112,240,152
TR 230,211,45056
v_accvgpr_read_b32 v212, a120
Mvaa 112,244,156
WL 4
ds_write_b128 v246, v[180:183] offset:2048
Mvaa 116,248,152
TR 232,211,32776
v_accvgpr_read_b32 v213, a121
Mvaa 116,252,156
v_accvgpr_read_b32 v214, a122
v_accvgpr_read_b32 v215, a123
Mvaa 84,248,144
TR 234,211,36872
CV 200,212,213
Mvaa 84,252,148
CV 201,214,215
v_accvgpr_read_b32 v212, a124
Mvaa 52,248,136
TR 236,211,40968
v_accvgpr_read_b32 v213, a125
Mvaa 52,252,140
Lstub4_fused_block:
s_cbranch_execz Lstub8_fused_block
v_accvgpr_read_b32 v214, a126
v_accvgpr_read_b32 v215, a127
Mvaa 20,248,128
TR 238,211,45064
CV 202,212,213
Mvaa 20,252,132
WL 5
CV 203,214,215
v_pk_max_i16 v200, v200, 0
Mvaa 24,224,128
TR 240,208,49152
v_pk_max_i16 v201, v201, 0
Mvaa 24,228,132
v_pk_max_i16 v202, v202, 0
v_pk_max_i16 v203, v203, 0
Mvaa 56,224,136
TR 242,208,53248
CV 144,144,145
Mvaa 56,228,140
ds_write_b128 v246, v[200:203] offset:3072
CV 145,146,147
Mvaa 88,224,144
TR 244,208,57344
CV 146,164,165
Mvaa 88,228,148
CV 147,166,167
v_pk_max_i16 v144, v144, 0
Mvaa 120,224,152
TR 246,208,61440
v_pk_max_i16 v145, v145, 0
Mvaa 120,228,156
WL 5
v_pk_max_i16 v146, v146, 0
v_pk_max_i16 v147, v147, 0
Mvaa 124,232,152
TR 248,208,0
CV 184,184,185
Mvaa 124,236,156
buffer_store_dwordx4 v[144:147], v234, s[72:75], s32 offen offset:0 sc1
CV 185,186,187
Mvaa 92,232,144
TR 250,208,4096
CV 186,204,205
Mvaa 92,236,148
s_waitcnt vmcnt(13)
s_barrier
CV 187,206,207
Mvaa 60,232,136
s_add_u32 s87, s24, 16384
s_add_u32 m0, s84, 32768
TR 252,208,8192
Mvaa 60,236,140
DM 228,44,87,0
v_pk_max_i16 v184, v184, 0
Mvaa 28,232,128
TR 254,208,12288
v_pk_max_i16 v185, v185, 0
Mvaa 28,236,132
WL 4
v_pk_max_i16 v186, v186, 0
v_pk_max_i16 v187, v187, 0
Mvaa 0,240,160
TR 224,208,49160
Mvaa 0,244,164
ds_read_b128 a[64:67], v216 offset:16384
Mvaa 32,240,168
TR 226,208,53256
Mvaa 32,244,172
DM 229,44,87,1024
Mvaa 64,240,176
TR 228,208,57352
Mvaa 64,244,180
ds_read_b128 a[68:71], v217 offset:16384
Mvaa 96,240,184
TR 230,208,61448
Mvaa 96,244,188
WL 6
buffer_store_dwordx4 v[184:187], v234, s[72:75], s17 offen offset:0 sc1
Mvaa 96,248,216
TR 232,208,8
Mvaa 96,252,220
DM 230,44,87,2048
Mvaa 64,248,208
TR 234,208,4104
Mvaa 64,252,212
ds_read_b128 a[72:75], v218 offset:16384
Mvaa 32,248,200
TR 236,208,8200
Mvaa 32,252,204
ds_read_b128 a[76:79], v219 offset:16384
Mvaa 0,248,192
TR 238,208,12296
Mvaa 0,252,196
WL 6
DM 231,44,87,3072
Mvaa 4,224,160
TR 240,209,49152
Mvaa 4,228,164
ds_read_b128 a[80:83], v216 offset:20480
Mvaa 36,224,168
TR 242,209,53248
Mvaa 36,228,172
s_add_u32 s87, s24, 32768
s_add_u32 m0, s85, 49152
ds_read_b128 a[84:87], v217 offset:20480
Mvaa 68,224,176
TR 244,209,57344
Mvaa 68,228,180
DM 232,48,87,0
Mvaa 100,224,184
TR 246,209,61440
Mvaa 100,228,188
WL 6
ds_read_b128 a[88:91], v218 offset:20480
Mvaa 100,232,216
TR 248,209,0
Mvaa 100,236,220
ds_read_b128 a[92:95], v219 offset:20480
Mvaa 68,232,208
TR 250,209,4096
Mvaa 68,236,212
ds_read_b128 a[96:99], v216 offset:24576
Mvaa 36,232,200
TR 252,209,8192
Mvaa 36,236,204
DM 233,48,87,1024
Mvaa 4,232,192
TR 254,209,12288
Mvaa 4,236,196
WL 7
ds_read_b128 a[100:103], v217 offset:24576
Mvaa 8,240,160
TR 224,209,49160
Mvaa 8,244,164
ds_read_b128 a[104:107], v218 offset:24576
Mvaa 40,240,168
TR 226,209,53256
Mvaa 40,244,172
ds_read_b128 a[108:111], v219 offset:24576
Mvaa 72,240,176
TR 228,209,57352
CV 0,0,1
Mvaa 72,244,180
DM 232,48,87,2048
CV 1,2,3
Mvaa 104,240,184
TR 230,209,61448
CV 2,4,5
Mvaa 104,244,188
WL 7
ds_read_b128 a[112:115], v216 offset:28672
Mvaa 104,248,216
TR 232,209,8
CV 3,6,7
Mvaa 104,252,220
ds_read_b128 a[116:119], v217 offset:28672
v_accvgpr_write_b32 a0, v0
Mvaa 72,248,208
TR 234,209,4104
v_accvgpr_write_b32 a1, v1
Mvaa 72,252,212
ds_read_b128 a[120:123], v218 offset:28672
v_accvgpr_write_b32 a2, v2
Mvaa 40,248,200
TR 236,209,8200
v_accvgpr_write_b32 a3, v3
Mvaa 40,252,204
DM 233,48,87,3072
CV 32,32,33
Mvaa 8,248,192
s_add_u32 s87, s24, 49152
s_add_u32 m0, s85, 0
TR 238,209,12296
Mvaa 8,252,196
WL 7
ds_read_b128 a[124:127], v219 offset:28672
Mvaa 12,224,160
TR 240,210,49152
CV 33,34,35
Mvaa 12,228,164
CV 34,36,37
CV 35,38,39
Mvaa 44,224,168
TR 242,210,53248
v_accvgpr_write_b32 a16, v32
Mvaa 44,228,172
v_accvgpr_write_b32 a17, v33
v_accvgpr_write_b32 a18, v34
Mvaa 76,224,176
TR 244,210,57344
v_accvgpr_write_b32 a19, v35
Mvaa 76,228,180
DM 232,48,87,0
CV 64,64,65
Mvaa 108,224,184
TR 246,210,61440
CV 65,66,67
Mvaa 108,228,188
WL 5
CV 66,68,69
CV 67,70,71
Mvaa 108,232,216
TR 248,210,0
v_accvgpr_write_b32 a32, v64
Mvaa 108,236,220
v_accvgpr_write_b32 a33, v65
v_accvgpr_write_b32 a34, v66
Mvaa 76,232,208
TR 250,210,4096
v_accvgpr_write_b32 a35, v67
Mvaa 76,236,212
CV 96,96,97
CV 97,98,99
Mvaa 44,232,200
TR 252,210,8192
CV 98,100,101
Mvaa 44,236,204
DM 233,48,87,1024
CV 99,102,103
Mvaa 12,232,192
TR 254,210,12288
v_accvgpr_write_b32 a48, v96
Mvaa 12,236,196
WL 4
v_accvgpr_write_b32 a49, v97
v_accvgpr_write_b32 a50, v98
Mvaa 16,240,160
TR 224,210,49160
v_accvgpr_write_b32 a51, v99
Mvaa 16,244,164
Mvaa 48,240,168
TR 226,210,53256
Mvaa 48,244,172
Mvaa 80,240,176
TR 228,210,57352
CV 8,8,9
Mvaa 80,244,180
DM 232,48,87,2048
CV 9,10,11
Mvaa 112,240,184
TR 230,210,61448
CV 10,12,13
Mvaa 112,244,188
WL 4
CV 11,14,15
v_accvgpr_write_b32 a4, v8
Mvaa 112,248,216
TR 232,210,8
v_accvgpr_write_b32 a5, v9
Mvaa 112,252,220
v_accvgpr_write_b32 a6, v10
v_accvgpr_write_b32 a7, v11
Mvaa 80,248,208
TR 234,210,4104
CV 40,40,41
Mvaa 80,252,212
CV 41,42,43
CV 42,44,45
Mvaa 48,248,200
TR 236,210,8200
CV 43,46,47
Mvaa 48,252,204
DM 233,48,87,3072
v_accvgpr_write_b32 a20, v40
Mvaa 16,248,192
TR 238,210,12296
v_accvgpr_write_b32 a21, v41
Mvaa 16,252,196
WL 4
v_accvgpr_write_b32 a22, v42
v_accvgpr_write_b32 a23, v43
Mvaa 20,224,160
TR 240,211,49152
CV 72,72,73
Mvaa 20,228,164
CV 73,74,75
CV 74,76,77
Mvaa 52,224,168
TR 242,211,53248
CV 75,78,79
Mvaa 52,228,172
v_accvgpr_write_b32 a36, v72
v_accvgpr_write_b32 a37, v73
Mvaa 84,224,176
TR 244,211,57344
v_accvgpr_write_b32 a38, v74
Mvaa 84,228,180
v_accvgpr_write_b32 a39, v75
CV 104,104,105
Mvaa 116,224,184
TR 246,211,61440
CV 105,106,107
Mvaa 116,228,188
WL 4
CV 106,108,109
CV 107,110,111
Mvaa 116,232,216
TR 248,211,0
v_accvgpr_write_b32 a52, v104
Mvaa 116,236,220
v_accvgpr_write_b32 a53, v105
v_accvgpr_write_b32 a54, v106
Mvaa 84,232,208
TR 250,211,4096
v_accvgpr_write_b32 a55, v107
Mvaa 84,236,212
Mvaa 52,232,200
TR 252,211,8192
Mvaa 52,236,204
Mvaa 20,232,192
TR 254,211,12288
Mvaa 20,236,196
WL 4
Mvaa 24,240,160
TR 224,211,49160
Mvaa 24,244,164
Mvaa 56,240,168
TR 226,211,53256
Mvaa 56,244,172
Mvaa 88,240,176
TR 228,211,57352
CV 16,16,17
Mvaa 88,244,180
CV 17,18,19
CV 18,20,21
Mvaa 120,240,184
TR 230,211,61448
CV 19,22,23
Mvaa 120,244,188
WL 4
v_accvgpr_write_b32 a8, v16
v_accvgpr_write_b32 a9, v17
Mvaa 120,248,216
TR 232,211,8
v_accvgpr_write_b32 a10, v18
Mvaa 120,252,220
v_accvgpr_write_b32 a11, v19
CV 48,48,49
Mvaa 88,248,208
TR 234,211,4104
CV 49,50,51
Mvaa 88,252,212
CV 50,52,53
CV 51,54,55
Mvaa 56,248,200
TR 236,211,8200
)"
R"(v_accvgpr_write_b32 a24, v48
Mvaa 56,252,204
v_accvgpr_write_b32 a25, v49
v_accvgpr_write_b32 a26, v50
Mvaa 24,248,192
TR 238,211,12296
v_accvgpr_write_b32 a27, v51
Mvaa 24,252,196
WL 4
CV 80,80,81
CV 81,82,83
Mvaa 28,224,160
CV 82,84,85
CV 83,86,87
Mvaa 28,228,164
v_accvgpr_write_b32 a40, v80
v_accvgpr_write_b32 a41, v81
Mvaa 60,224,168
v_accvgpr_write_b32 a42, v82
v_accvgpr_write_b32 a43, v83
Mvaa 60,228,172
WL 0
s_barrier
CV 112,112,113
Mvaa 92,224,176
CV 113,114,115
CV 114,116,117
Mvaa 92,228,180
CV 115,118,119
v_accvgpr_write_b32 a56, v112
Mvaa 124,224,184
v_accvgpr_write_b32 a57, v113
v_accvgpr_write_b32 a58, v114
Mvaa 124,228,188
v_accvgpr_write_b32 a59, v115
Mvaa 124,232,216
Mvaa 124,236,220
Mvaa 92,232,208
Mvaa 92,236,212
Mvaa 60,232,200
Mvaa 60,236,204
Mvaa 28,232,192
Mvaa 28,236,196
v_mov_b32 v212, 0
v_mov_b32 v213, 0
v_mov_b32 v214, 0
v_mov_b32 v215, 0
s_add_u32 s87, s24, 32768
s_add_u32 m0, s84, 49152
v_mfma_f32_32x32x16_bf16 v[0:15], v[212:215], v[212:215], 0
CV 24,24,25
CV 25,26,27
DM 228,44,87,0
CV 26,28,29
CV 27,30,31
v_accvgpr_write_b32 a12, v24
v_accvgpr_write_b32 a13, v25
DM 229,44,87,1024
v_accvgpr_write_b32 a14, v26
v_accvgpr_write_b32 a15, v27
v_mfma_f32_32x32x16_bf16 v[32:47], v[212:215], v[212:215], 0
CV 56,56,57
CV 57,58,59
DM 230,44,87,2048
CV 58,60,61
CV 59,62,63
v_accvgpr_write_b32 a28, v56
v_accvgpr_write_b32 a29, v57
DM 231,44,87,3072
v_accvgpr_write_b32 a30, v58
v_accvgpr_write_b32 a31, v59
v_mfma_f32_32x32x16_bf16 v[64:79], v[212:215], v[212:215], 0
CV 88,88,89
CV 89,90,91
s_add_u32 s87, s24, 49152
s_nop 0
s_add_u32 m0, s84, 0
s_nop 0
DM 228,44,87,0
CV 90,92,93
CV 91,94,95
v_accvgpr_write_b32 a44, v88
v_accvgpr_write_b32 a45, v89
DM 229,44,87,1024
v_accvgpr_write_b32 a46, v90
v_accvgpr_write_b32 a47, v91
v_mfma_f32_32x32x16_bf16 v[96:111], v[212:215], v[212:215], 0
CV 120,120,121
CV 121,122,123
DM 230,44,87,2048
CV 122,124,125
CV 123,126,127
v_accvgpr_write_b32 a60, v120
v_accvgpr_write_b32 a61, v121
DM 231,44,87,3072
v_accvgpr_write_b32 a62, v122
v_accvgpr_write_b32 a63, v123
v_mfma_f32_16x16x32_bf16 v[144:147], a[64:67], a[0:3], 0
TR 128,220,16384
Mvaa 144,68,4
TR 130,220,20480
Mvaa 144,72,8
TR 132,220,24576
Mvaa 144,76,12
TR 134,220,28672
v_mfma_f32_16x16x32_bf16 v[148:151], a[64:67], a[16:19], 0
TR 136,221,16384
Mvaa 148,68,20
TR 138,221,20480
Mvaa 148,72,24
TR 140,221,24576
Mvaa 148,76,28
TR 142,221,28672
v_mfma_f32_16x16x32_bf16 v[152:155], a[64:67], a[32:35], 0
TR 144,222,16384
Mvaa 152,68,36
TR 146,222,20480
Mvaa 152,72,40
TR 148,222,24576
Mvaa 152,76,44
TR 150,222,28672
v_mfma_f32_16x16x32_bf16 v[156:159], a[64:67], a[48:51], 0
TR 152,223,16384
Mvaa 156,68,52
TR 154,223,20480
Mvaa 156,72,56
TR 156,223,24576
Mvaa 156,76,60
TR 158,223,28672
v_mfma_f32_32x32x16_bf16 v[16:31], v[212:215], v[212:215], 0
v_max3_f32 v176, v144, v145, v146
v_max_f32 v176, v176, v147
v_max3_f32 v180, v148, v149, v150
v_max_f32 v180, v180, v151
v_max3_f32 v184, v152, v153, v154
v_max_f32 v184, v184, v155
v_max3_f32 v188, v156, v157, v158
v_max_f32 v188, v188, v159
v_mfma_f32_32x32x16_bf16 v[48:63], v[212:215], v[212:215], 0
v_mov_b32 v177, v176
v_mov_b32 v181, v180
v_mov_b32 v185, v184
v_mov_b32 v189, v188
v_permlane32_swap_b32 v176, v177
v_permlane32_swap_b32 v180, v181
v_permlane32_swap_b32 v184, v185
v_permlane32_swap_b32 v188, v189
v_mfma_f32_32x32x16_bf16 v[80:95], v[212:215], v[212:215], 0
v_max_f32 v176, v176, v177
v_max_f32 v180, v180, v181
v_max_f32 v184, v184, v185
v_max_f32 v188, v188, v189
v_mov_b32 v177, v176
v_mov_b32 v181, v180
v_mov_b32 v185, v184
v_mov_b32 v189, v188
v_mfma_f32_32x32x16_bf16 v[112:127], v[212:215], v[212:215], 0
v_permlane16_swap_b32 v176, v177
v_permlane16_swap_b32 v180, v181
v_permlane16_swap_b32 v184, v185
v_permlane16_swap_b32 v188, v189
v_min_f32 v176, -v176, -v177
v_min_f32 v180, -v180, -v181
v_min_f32 v184, -v184, -v185
v_min_f32 v188, -v188, -v189
v_mfma_f32_32x32x16_bf16 v[192:207], v[212:215], v[212:215], 0
v_mov_b32 v177, v176
v_mov_b32 v181, v180
v_mov_b32 v185, v184
v_mov_b32 v189, v188
v_mov_b32 v178, v176
v_mov_b32 v182, v180
v_mov_b32 v186, v184
v_mov_b32 v190, v188
v_mov_b32 v179, v176
v_mov_b32 v183, v180
v_mov_b32 v187, v184
v_mov_b32 v191, v188
Latt_fused_block:
v_mov_b32 v208, 0x3f803f80
v_mov_b32 v209, 0x3f803f80
v_mov_b32 v210, 0x3f803f80
v_mov_b32 v211, 0x3f803f80
v_mov_b32 v252, 0x7ffffff0
v_mfma_f32_16x16x32_bf16 v[128:131], a[64:67], a[0:3], v[176:179]
TR 160,224,16384
Mvaa 128,68,4
TR 162,224,20480
Mvaa 128,72,8
TR 164,224,24576
Mvaa 128,76,12
TR 166,224,28672
v_mfma_f32_16x16x32_bf16 v[132:135], a[92:95], a[12:15], v[176:179]
TR 168,225,16384
Mvaa 132,88,8
TR 170,225,20480
Mvaa 132,84,4
TR 172,225,24576
Mvaa 132,80,0
Lstub5_fused_block:
s_cbranch_execz Lstub9_fused_block
TR 174,225,28672
v_mfma_f32_16x16x32_bf16 v[136:139], a[96:99], a[0:3], v[176:179]
TR 176,226,16384
Mvaa 136,100,4
TR 178,226,20480
Mvaa 136,104,8
TR 180,226,24576
Mvaa 136,108,12
TR 182,226,28672
v_mfma_f32_16x16x32_bf16 v[140:143], a[124:127], a[12:15], v[176:179]
TR 184,227,16384
Mvaa 140,120,8
TR 186,227,20480
Mvaa 140,116,4
TR 188,227,24576
Mvaa 140,112,0
TR 190,227,28672
v_mfma_f32_16x16x32_bf16 v[144:147], a[64:67], a[16:19], v[180:183]
ds_read_b128 v[242:245], v246 offset:0
v_exp_f32 v212, v128
Mvaa 144,68,20
buffer_load_dword v253, v252, s[72:75], 0 offen
v_exp_f32 v213, v129
Mvaa 144,72,24
s_getpc_b64 s[8:9]
Lhk0_fused_block:
s_add_u32 s8, s8, Lhook_fused_block-Lhk0_fused_block
s_addc_u32 s9, s9, 0
v_exp_f32 v214, v130
CV 160,212,213
Mvaa 144,76,28
buffer_load_dword v253, v252, s[72:75], 0 offen
v_exp_f32 v215, v131
v_mfma_f32_16x16x32_bf16 v[148:151], a[92:95], a[28:31], v[180:183]
v_exp_f32 v212, v132
CV 161,214,215
Mvaa 148,88,24
buffer_load_dword v253, v252, s[72:75], 0 offen
v_exp_f32 v213, v133
Mvaa 148,84,20
v_exp_f32 v214, v134
CV 162,212,213
Mvaa 148,80,16
buffer_load_dword v253, v252, s[72:75], 0 offen
v_exp_f32 v215, v135
v_mfma_f32_16x16x32_bf16 v[152:155], a[96:99], a[16:19], v[180:183]
v_exp_f32 v212, v136
CV 163,214,215
Mvaa 152,100,20
buffer_load_dword v253, v252, s[72:75], 0 offen
v_exp_f32 v213, v137
Mvaa 152,104,24
v_exp_f32 v214, v138
CV 164,212,213
Mvaa 152,108,28
v_exp_f32 v215, v139
v_mfma_f32_16x16x32_bf16 v[156:159], a[124:127], a[28:31], v[180:183]
v_exp_f32 v212, v140
CV 165,214,215
Mvaa 156,120,24
v_exp_f32 v213, v141
Mvaa 156,116,20
v_exp_f32 v214, v142
CV 166,212,213
Mvaa 156,112,16
v_exp_f32 v215, v143
s_add_u32 s87, s24, 65536
CV 167,214,215
s_mov_b32 s93, 7
s_waitcnt vmcnt(21) lgkmcnt(0)
s_barrier
.p2align 6
Lloop_fused_block:
v_mfma_f32_16x16x32_bf16 v[128:131], a[64:67], a[32:35], v[184:187]
s_add_u32 m0, s84, 16384
v_exp_f32 v212, v144
Mvaa 128,68,36
DM 228,44,87,0
Mvaa 128,72,40
v_exp_f32 v213, v145
Mvaa 128,76,44
TR 192,220,32768
CV 168,212,213
v_mfma_f32_16x16x32_bf16 v[132:135], a[92:95], a[44:47], v[184:187]
v_exp_f32 v214, v146
Mvaa 132,88,40
DM 229,44,87,1024
Mvaa 132,84,36
v_exp_f32 v215, v147
Mvaa 132,80,32
TR 194,220,36864
CV 169,214,215
v_mfma_f32_16x16x32_bf16 v[136:139], a[96:99], a[32:35], v[184:187]
v_exp_f32 v212, v148
Mvaa 136,100,36
DM 230,44,87,2048
Mvaa 136,104,40
v_exp_f32 v213, v149
Mvaa 136,108,44
TR 196,220,40960
CV 170,212,213
v_mfma_f32_16x16x32_bf16 v[140:143], a[124:127], a[44:47], v[184:187]
v_exp_f32 v214, v150
Mvaa 140,120,40
DM 231,44,87,3072
Mvaa 140,116,36
v_exp_f32 v215, v151
Mvaa 140,112,32
TR 198,220,45056
CV 171,214,215
Mvav 0,128,160
s_add_u32 m0, s85, 16384
v_exp_f32 v212, v152
Mvav 0,132,164
DM 232,48,87,0
Mvav 4,136,160
v_exp_f32 v213, v153
Mvav 4,140,164
TR 200,221,32768
CV 172,212,213
Mvav 8,144,160
v_exp_f32 v214, v154
Mvav 8,148,164
DM 233,48,87,1024
Mvav 12,152,160
v_exp_f32 v215, v155
Mvav 12,156,164
TR 202,221,36864
CV 173,214,215
Mvav 16,160,160
v_exp_f32 v212, v156
Mvav 16,164,164
DM 232,48,87,2048
Mvav 20,168,160
v_exp_f32 v213, v157
Mvav 20,172,164
TR 204,221,40960
CV 174,212,213
Mvav 24,176,160
v_exp_f32 v214, v158
Mvav 24,180,164
DM 233,48,87,3072
Mvav 28,184,160
v_exp_f32 v215, v159
Mvav 28,188,164
TR 206,221,45056
CV 175,214,215
v_smfmac_f32_16x16x64_bf16 v[192:195], v[208:211], v[160:167], v239
buffer_store_dwordx4 v[242:245], v247, s[72:75], s20 offen sc1
v_mfma_f32_16x16x32_bf16 v[144:147], a[64:67], a[48:51], v[188:191]
v_exp_f32 v212, v128
Mvaa 144,68,52
ds_read_b128 a[64:67], v216 offset:32768
Mvaa 144,72,56
v_exp_f32 v213, v129
Mvaa 144,76,60
ds_read_b128 a[68:71], v217 offset:32768
CV 160,212,213
v_mfma_f32_16x16x32_bf16 v[148:151], a[92:95], a[60:63], v[188:191]
s_add_u32 s87, s87, 16384
v_exp_f32 v214, v130
Mvaa 148,88,56
ds_read_b128 a[72:75], v218 offset:32768
Mvaa 148,84,52
v_exp_f32 v215, v131
Mvaa 148,80,48
ds_read_b128 a[76:79], v219 offset:32768
CV 161,214,215
v_mfma_f32_16x16x32_bf16 v[152:155], a[96:99], a[48:51], v[188:191]
v_exp_f32 v212, v132
Mvaa 152,100,52
ds_read_b128 a[92:95], v219 offset:36864
Mvaa 152,104,56
v_exp_f32 v213, v133
Mvaa 152,108,60
ds_read_b128 a[88:91], v218 offset:36864
CV 162,212,213
v_mfma_f32_16x16x32_bf16 v[156:159], a[124:127], a[60:63], v[188:191]
v_exp_f32 v214, v134
Mvaa 156,120,56
ds_read_b128 a[84:87], v217 offset:36864
Mvaa 156,116,52
v_exp_f32 v215, v135
Mvaa 156,112,48
ds_read_b128 a[80:83], v216 offset:36864
CV 163,214,215
Mvav 32,128,168
v_exp_f32 v212, v136
Mvav 32,132,172
ds_read_b128 a[96:99], v216 offset:40960
Mvav 36,136,168
v_exp_f32 v213, v137
Mvav 36,140,172
ds_read_b128 a[100:103], v217 offset:40960
CV 164,212,213
Mvav 40,144,168
v_exp_f32 v214, v138
Mvav 40,148,172
ds_read_b128 a[104:107], v218 offset:40960
)"
R"(Mvav 44,152,168
v_exp_f32 v215, v139
Mvav 44,156,172
ds_read_b128 a[108:111], v219 offset:40960
CV 165,214,215
Mvav 48,160,168
v_exp_f32 v212, v140
Mvav 48,164,172
ds_read_b128 a[124:127], v219 offset:45056
Mvav 52,168,168
v_exp_f32 v213, v141
Mvav 52,172,172
ds_read_b128 a[120:123], v218 offset:45056
CV 166,212,213
Mvav 56,176,168
v_exp_f32 v214, v142
Mvav 56,180,172
ds_read_b128 a[116:119], v217 offset:45056
Mvav 60,184,168
v_exp_f32 v215, v143
Mvav 60,188,172
ds_read_b128 a[112:115], v216 offset:45056
CV 167,214,215
v_smfmac_f32_16x16x64_bf16 v[196:199], v[208:211], v[168:175], v239
WL 8
v_mfma_f32_16x16x32_bf16 v[128:131], a[64:67], a[0:3], v[176:179]
v_exp_f32 v212, v144
Mvaa 128,68,4
Mvaa 128,72,8
v_exp_f32 v213, v145
Mvaa 128,76,12
CV 168,212,213
v_mfma_f32_16x16x32_bf16 v[132:135], a[92:95], a[12:15], v[176:179]
v_exp_f32 v214, v146
Mvaa 132,88,8
Mvaa 132,84,4
WL 0
v_exp_f32 v215, v147
Mvaa 132,80,0
TR 208,222,32768
CV 169,214,215
v_mfma_f32_16x16x32_bf16 v[136:139], a[96:99], a[0:3], v[176:179]
v_exp_f32 v212, v148
Mvaa 136,100,4
TR 210,222,36864
Mvaa 136,104,8
v_exp_f32 v213, v149
Mvaa 136,108,12
TR 212,222,40960
CV 170,212,213
v_mfma_f32_16x16x32_bf16 v[140:143], a[124:127], a[12:15], v[176:179]
v_exp_f32 v214, v150
Mvaa 140,120,8
TR 214,222,45056
Mvaa 140,116,4
v_exp_f32 v215, v151
Mvaa 140,112,0
TR 216,223,32768
CV 171,214,215
Mvav 64,128,160
v_exp_f32 v212, v152
Mvav 64,132,164
TR 218,223,36864
Mvav 68,136,160
v_exp_f32 v213, v153
Mvav 68,140,164
TR 220,223,40960
CV 172,212,213
Mvav 72,144,160
v_exp_f32 v214, v154
Mvav 72,148,164
TR 222,223,45056
Mvav 76,152,160
v_exp_f32 v215, v155
Mvav 76,156,164
TR 224,224,32768
CV 173,214,215
Mvav 80,160,160
v_exp_f32 v212, v156
Mvav 80,164,164
TR 226,224,36864
Mvav 84,168,160
v_exp_f32 v213, v157
Mvav 84,172,164
TR 228,224,40960
CV 174,212,213
Mvav 88,176,160
v_exp_f32 v214, v158
Mvav 88,180,164
TR 230,224,45056
Mvav 92,184,160
v_exp_f32 v215, v159
Mvav 92,188,164
TR 232,225,32768
CV 175,214,215
v_smfmac_f32_16x16x64_bf16 v[200:203], v[208:211], v[160:167], v239
TR 234,225,36864
v_mfma_f32_16x16x32_bf16 v[144:147], a[64:67], a[16:19], v[180:183]
v_exp_f32 v212, v128
Mvaa 144,68,20
TR 236,225,40960
Mvaa 144,72,24
v_exp_f32 v213, v129
Mvaa 144,76,28
TR 238,225,45056
CV 160,212,213
v_mfma_f32_16x16x32_bf16 v[148:151], a[92:95], a[28:31], v[180:183]
v_exp_f32 v214, v130
Mvaa 148,88,24
TR 240,226,32768
Mvaa 148,84,20
v_exp_f32 v215, v131
Mvaa 148,80,16
TR 242,226,36864
CV 161,214,215
v_mfma_f32_16x16x32_bf16 v[152:155], a[96:99], a[16:19], v[180:183]
v_exp_f32 v212, v132
Mvaa 152,100,20
TR 244,226,40960
Mvaa 152,104,24
v_exp_f32 v213, v133
Mvaa 152,108,28
TR 246,226,45056
CV 162,212,213
v_mfma_f32_16x16x32_bf16 v[156:159], a[124:127], a[28:31], v[180:183]
v_exp_f32 v214, v134
Mvaa 156,120,24
TR 248,227,32768
Mvaa 156,116,20
v_exp_f32 v215, v135
Mvaa 156,112,16
TR 250,227,36864
CV 163,214,215
Mvav 96,128,168
v_exp_f32 v212, v136
Mvav 96,132,172
TR 252,227,40960
Mvav 100,136,168
v_exp_f32 v213, v137
Mvav 100,140,172
TR 254,227,45056
CV 164,212,213
Mvav 104,144,168
v_exp_f32 v214, v138
Mvav 104,148,172
Mvav 108,152,168
v_exp_f32 v215, v139
Mvav 108,156,172
ds_read_b128 v[242:245], v246 offset:1024
CV 165,214,215
Mvav 112,160,168
v_exp_f32 v212, v140
Mvav 112,164,172
Mvav 116,168,168
v_exp_f32 v213, v141
Mvav 116,172,172
CV 166,212,213
Mvav 120,176,168
v_exp_f32 v214, v142
Mvav 120,180,172
s_waitcnt vmcnt(18) lgkmcnt(0)
s_barrier
Mvav 124,184,168
v_exp_f32 v215, v143
Mvav 124,188,172
CV 167,214,215
v_smfmac_f32_16x16x64_bf16 v[204:207], v[208:211], v[168:175], v239
v_mfma_f32_16x16x32_bf16 v[128:131], a[64:67], a[32:35], v[184:187]
s_add_u32 m0, s84, 32768
v_exp_f32 v212, v144
Mvaa 128,68,36
DM 228,44,87,0
Mvaa 128,72,40
v_exp_f32 v213, v145
Mvaa 128,76,44
TR 128,220,49152
CV 168,212,213
v_mfma_f32_16x16x32_bf16 v[132:135], a[92:95], a[44:47], v[184:187]
v_exp_f32 v214, v146
Mvaa 132,88,40
DM 229,44,87,1024
Mvaa 132,84,36
v_exp_f32 v215, v147
Mvaa 132,80,32
TR 130,220,53248
CV 169,214,215
v_mfma_f32_16x16x32_bf16 v[136:139], a[96:99], a[32:35], v[184:187]
v_exp_f32 v212, v148
Mvaa 136,100,36
DM 230,44,87,2048
Mvaa 136,104,40
v_exp_f32 v213, v149
Mvaa 136,108,44
TR 132,220,57344
CV 170,212,213
v_mfma_f32_16x16x32_bf16 v[140:143], a[124:127], a[44:47], v[184:187]
v_exp_f32 v214, v150
Mvaa 140,120,40
DM 231,44,87,3072
Mvaa 140,116,36
v_exp_f32 v215, v151
Mvaa 140,112,32
TR 134,220,61440
CV 171,214,215
Mvav 0,192,160
s_add_u32 m0, s85, 32768
v_exp_f32 v212, v152
Mvav 0,196,164
DM 232,48,87,0
Mvav 4,200,160
v_exp_f32 v213, v153
Mvav 4,204,164
TR 136,221,49152
CV 172,212,213
Mvav 8,208,160
v_exp_f32 v214, v154
Mvav 8,212,164
DM 233,48,87,1024
Mvav 12,216,160
v_exp_f32 v215, v155
Mvav 12,220,164
TR 138,221,53248
CV 173,214,215
Mvav 16,224,160
v_exp_f32 v212, v156
Mvav 16,228,164
DM 232,48,87,2048
Mvav 20,232,160
v_exp_f32 v213, v157
Mvav 20,236,164
TR 140,221,57344
CV 174,212,213
Mvav 24,240,160
v_exp_f32 v214, v158
Mvav 24,244,164
DM 233,48,87,3072
Mvav 28,248,160
v_exp_f32 v215, v159
Mvav 28,252,164
TR 142,221,61440
CV 175,214,215
v_smfmac_f32_16x16x64_bf16 v[192:195], v[208:211], v[160:167], v239
buffer_store_dwordx4 v[242:245], v247, s[72:75], s21 offen sc1
v_mfma_f32_16x16x32_bf16 v[144:147], a[64:67], a[48:51], v[188:191]
v_exp_f32 v212, v128
Mvaa 144,68,52
ds_read_b128 a[64:67], v216 offset:49152
Mvaa 144,72,56
v_exp_f32 v213, v129
Mvaa 144,76,60
ds_read_b128 a[68:71], v217 offset:49152
CV 160,212,213
v_mfma_f32_16x16x32_bf16 v[148:151], a[92:95], a[60:63], v[188:191]
s_add_u32 s87, s87, 16384
v_exp_f32 v214, v130
Mvaa 148,88,56
ds_read_b128 a[72:75], v218 offset:49152
Mvaa 148,84,52
v_exp_f32 v215, v131
Mvaa 148,80,48
ds_read_b128 a[76:79], v219 offset:49152
CV 161,214,215
v_mfma_f32_16x16x32_bf16 v[152:155], a[96:99], a[48:51], v[188:191]
v_exp_f32 v212, v132
Mvaa 152,100,52
ds_read_b128 a[92:95], v219 offset:53248
Mvaa 152,104,56
v_exp_f32 v213, v133
Mvaa 152,108,60
ds_read_b128 a[88:91], v218 offset:53248
CV 162,212,213
v_mfma_f32_16x16x32_bf16 v[156:159], a[124:127], a[60:63], v[188:191]
v_exp_f32 v214, v134
Mvaa 156,120,56
ds_read_b128 a[84:87], v217 offset:53248
Mvaa 156,116,52
v_exp_f32 v215, v135
Mvaa 156,112,48
ds_read_b128 a[80:83], v216 offset:53248
CV 163,214,215
Mvav 32,192,168
v_exp_f32 v212, v136
Mvav 32,196,172
ds_read_b128 a[96:99], v216 offset:57344
Mvav 36,200,168
v_exp_f32 v213, v137
Mvav 36,204,172
ds_read_b128 a[100:103], v217 offset:57344
CV 164,212,213
Mvav 40,208,168
v_exp_f32 v214, v138
Mvav 40,212,172
ds_read_b128 a[104:107], v218 offset:57344
Mvav 44,216,168
v_exp_f32 v215, v139
Mvav 44,220,172
ds_read_b128 a[108:111], v219 offset:57344
CV 165,214,215
Mvav 48,224,168
v_exp_f32 v212, v140
Mvav 48,228,172
ds_read_b128 a[124:127], v219 offset:61440
Mvav 52,232,168
v_exp_f32 v213, v141
Mvav 52,236,172
ds_read_b128 a[120:123], v218 offset:61440
CV 166,212,213
Mvav 56,240,168
v_exp_f32 v214, v142
Mvav 56,244,172
ds_read_b128 a[116:119], v217 offset:61440
Mvav 60,248,168
v_exp_f32 v215, v143
Mvav 60,252,172
ds_read_b128 a[112:115], v216 offset:61440
CV 167,214,215
v_smfmac_f32_16x16x64_bf16 v[196:199], v[208:211], v[168:175], v239
WL 8
v_mfma_f32_16x16x32_bf16 v[128:131], a[64:67], a[0:3], v[176:179]
v_exp_f32 v212, v144
Mvaa 128,68,4
Mvaa 128,72,8
v_exp_f32 v213, v145
Mvaa 128,76,12
CV 168,212,213
v_mfma_f32_16x16x32_bf16 v[132:135], a[92:95], a[12:15], v[176:179]
v_exp_f32 v214, v146
Mvaa 132,88,8
Mvaa 132,84,4
WL 0
v_exp_f32 v215, v147
Mvaa 132,80,0
TR 144,222,49152
CV 169,214,215
v_mfma_f32_16x16x32_bf16 v[136:139], a[96:99], a[0:3], v[176:179]
v_exp_f32 v212, v148
Mvaa 136,100,4
TR 146,222,53248
Mvaa 136,104,8
v_exp_f32 v213, v149
Mvaa 136,108,12
TR 148,222,57344
CV 170,212,213
v_mfma_f32_16x16x32_bf16 v[140:143], a[124:127], a[12:15], v[176:179]
v_exp_f32 v214, v150
Mvaa 140,120,8
TR 150,222,61440
Mvaa 140,116,4
v_exp_f32 v215, v151
Mvaa 140,112,0
TR 152,223,49152
CV 171,214,215
Mvav 64,192,160
v_exp_f32 v212, v152
Mvav 64,196,164
TR 154,223,53248
Mvav 68,200,160
v_exp_f32 v213, v153
Mvav 68,204,164
TR 156,223,57344
CV 172,212,213
Mvav 72,208,160
v_exp_f32 v214, v154
Mvav 72,212,164
TR 158,223,61440
Mvav 76,216,160
v_exp_f32 v215, v155
Mvav 76,220,164
TR 160,224,49152
CV 173,214,215
Mvav 80,224,160
v_exp_f32 v212, v156
Mvav 80,228,164
TR 162,224,53248
Mvav 84,232,160
v_exp_f32 v213, v157
Mvav 84,236,164
TR 164,224,57344
CV 174,212,213
Mvav 88,240,160
Lstub6_fused_block:
s_cbranch_execz Ltourend_fused_block
v_exp_f32 v214, v158
Mvav 88,244,164
TR 166,224,61440
Mvav 92,248,160
v_exp_f32 v215, v159
Mvav 92,252,164
TR 168,225,49152
CV 175,214,215
v_smfmac_f32_16x16x64_bf16 v[200:203], v[208:211], v[160:167], v239
TR 170,225,53248
v_mfma_f32_16x16x32_bf16 v[144:147], a[64:67], a[16:19], v[180:183]
v_exp_f32 v212, v128
Mvaa 144,68,20
TR 172,225,57344
Mvaa 144,72,24
v_exp_f32 v213, v129
Mvaa 144,76,28
TR 174,225,61440
)"
R"(CV 160,212,213
v_mfma_f32_16x16x32_bf16 v[148:151], a[92:95], a[28:31], v[180:183]
v_exp_f32 v214, v130
Mvaa 148,88,24
TR 176,226,49152
Mvaa 148,84,20
v_exp_f32 v215, v131
Mvaa 148,80,16
TR 178,226,53248
CV 161,214,215
v_mfma_f32_16x16x32_bf16 v[152:155], a[96:99], a[16:19], v[180:183]
v_exp_f32 v212, v132
Mvaa 152,100,20
TR 180,226,57344
Mvaa 152,104,24
v_exp_f32 v213, v133
Mvaa 152,108,28
TR 182,226,61440
CV 162,212,213
v_mfma_f32_16x16x32_bf16 v[156:159], a[124:127], a[28:31], v[180:183]
v_exp_f32 v214, v134
Mvaa 156,120,24
TR 184,227,49152
Mvaa 156,116,20
v_exp_f32 v215, v135
Mvaa 156,112,16
TR 186,227,53248
CV 163,214,215
Mvav 96,192,168
v_exp_f32 v212, v136
Mvav 96,196,172
TR 188,227,57344
Mvav 100,200,168
v_exp_f32 v213, v137
Mvav 100,204,172
TR 190,227,61440
CV 164,212,213
Mvav 104,208,168
v_exp_f32 v214, v138
Mvav 104,212,172
Mvav 108,216,168
v_exp_f32 v215, v139
Mvav 108,220,172
ds_read_b128 v[242:245], v246 offset:2048
CV 165,214,215
Mvav 112,224,168
v_exp_f32 v212, v140
Mvav 112,228,172
Mvav 116,232,168
v_exp_f32 v213, v141
Mvav 116,236,172
CV 166,212,213
Mvav 120,240,168
v_exp_f32 v214, v142
Mvav 120,244,172
s_waitcnt vmcnt(18) lgkmcnt(0)
s_barrier
Mvav 124,248,168
v_exp_f32 v215, v143
Mvav 124,252,172
CV 167,214,215
v_smfmac_f32_16x16x64_bf16 v[204:207], v[208:211], v[168:175], v239
v_mfma_f32_16x16x32_bf16 v[128:131], a[64:67], a[32:35], v[184:187]
s_add_u32 m0, s84, 49152
v_exp_f32 v212, v144
Mvaa 128,68,36
DM 228,44,87,0
Mvaa 128,72,40
v_exp_f32 v213, v145
Mvaa 128,76,44
TR 192,220,0
CV 168,212,213
v_mfma_f32_16x16x32_bf16 v[132:135], a[92:95], a[44:47], v[184:187]
v_exp_f32 v214, v146
Mvaa 132,88,40
DM 229,44,87,1024
Mvaa 132,84,36
v_exp_f32 v215, v147
Mvaa 132,80,32
TR 194,220,4096
CV 169,214,215
v_mfma_f32_16x16x32_bf16 v[136:139], a[96:99], a[32:35], v[184:187]
v_exp_f32 v212, v148
Mvaa 136,100,36
DM 230,44,87,2048
Mvaa 136,104,40
v_exp_f32 v213, v149
Mvaa 136,108,44
TR 196,220,8192
CV 170,212,213
v_mfma_f32_16x16x32_bf16 v[140:143], a[124:127], a[44:47], v[184:187]
v_exp_f32 v214, v150
Mvaa 140,120,40
DM 231,44,87,3072
Mvaa 140,116,36
v_exp_f32 v215, v151
Mvaa 140,112,32
TR 198,220,12288
CV 171,214,215
Mvav 0,128,160
s_add_u32 m0, s85, 49152
v_exp_f32 v212, v152
Mvav 0,132,164
DM 232,48,87,0
Mvav 4,136,160
v_exp_f32 v213, v153
Mvav 4,140,164
TR 200,221,0
CV 172,212,213
Mvav 8,144,160
v_exp_f32 v214, v154
Mvav 8,148,164
DM 233,48,87,1024
Mvav 12,152,160
v_exp_f32 v215, v155
Mvav 12,156,164
TR 202,221,4096
CV 173,214,215
Mvav 16,160,160
v_exp_f32 v212, v156
Mvav 16,164,164
DM 232,48,87,2048
Mvav 20,168,160
v_exp_f32 v213, v157
Mvav 20,172,164
TR 204,221,8192
CV 174,212,213
Mvav 24,176,160
v_exp_f32 v214, v158
Mvav 24,180,164
DM 233,48,87,3072
Mvav 28,184,160
v_exp_f32 v215, v159
Mvav 28,188,164
TR 206,221,12288
CV 175,214,215
v_smfmac_f32_16x16x64_bf16 v[192:195], v[208:211], v[160:167], v239
buffer_store_dwordx4 v[242:245], v247, s[72:75], s22 offen sc1
v_mfma_f32_16x16x32_bf16 v[144:147], a[64:67], a[48:51], v[188:191]
v_exp_f32 v212, v128
Mvaa 144,68,52
ds_read_b128 a[64:67], v216 offset:0
Mvaa 144,72,56
v_exp_f32 v213, v129
Mvaa 144,76,60
ds_read_b128 a[68:71], v217 offset:0
CV 160,212,213
v_mfma_f32_16x16x32_bf16 v[148:151], a[92:95], a[60:63], v[188:191]
s_add_u32 s87, s87, 16384
v_exp_f32 v214, v130
Mvaa 148,88,56
ds_read_b128 a[72:75], v218 offset:0
Mvaa 148,84,52
v_exp_f32 v215, v131
Mvaa 148,80,48
ds_read_b128 a[76:79], v219 offset:0
CV 161,214,215
v_mfma_f32_16x16x32_bf16 v[152:155], a[96:99], a[48:51], v[188:191]
v_exp_f32 v212, v132
Mvaa 152,100,52
ds_read_b128 a[92:95], v219 offset:4096
Mvaa 152,104,56
v_exp_f32 v213, v133
Mvaa 152,108,60
ds_read_b128 a[88:91], v218 offset:4096
CV 162,212,213
v_mfma_f32_16x16x32_bf16 v[156:159], a[124:127], a[60:63], v[188:191]
v_exp_f32 v214, v134
Mvaa 156,120,56
ds_read_b128 a[84:87], v217 offset:4096
Mvaa 156,116,52
v_exp_f32 v215, v135
Mvaa 156,112,48
ds_read_b128 a[80:83], v216 offset:4096
CV 163,214,215
Mvav 32,128,168
v_exp_f32 v212, v136
Mvav 32,132,172
ds_read_b128 a[96:99], v216 offset:8192
Mvav 36,136,168
v_exp_f32 v213, v137
Mvav 36,140,172
ds_read_b128 a[100:103], v217 offset:8192
CV 164,212,213
Mvav 40,144,168
v_exp_f32 v214, v138
Mvav 40,148,172
ds_read_b128 a[104:107], v218 offset:8192
Mvav 44,152,168
v_exp_f32 v215, v139
Mvav 44,156,172
ds_read_b128 a[108:111], v219 offset:8192
CV 165,214,215
Mvav 48,160,168
v_exp_f32 v212, v140
Mvav 48,164,172
ds_read_b128 a[124:127], v219 offset:12288
Mvav 52,168,168
v_exp_f32 v213, v141
Mvav 52,172,172
ds_read_b128 a[120:123], v218 offset:12288
CV 166,212,213
Mvav 56,176,168
v_exp_f32 v214, v142
Mvav 56,180,172
ds_read_b128 a[116:119], v217 offset:12288
Mvav 60,184,168
v_exp_f32 v215, v143
Mvav 60,188,172
ds_read_b128 a[112:115], v216 offset:12288
CV 167,214,215
v_smfmac_f32_16x16x64_bf16 v[196:199], v[208:211], v[168:175], v239
WL 8
v_mfma_f32_16x16x32_bf16 v[128:131], a[64:67], a[0:3], v[176:179]
v_exp_f32 v212, v144
Mvaa 128,68,4
Mvaa 128,72,8
v_exp_f32 v213, v145
Mvaa 128,76,12
CV 168,212,213
v_mfma_f32_16x16x32_bf16 v[132:135], a[92:95], a[12:15], v[176:179]
v_exp_f32 v214, v146
Mvaa 132,88,8
Mvaa 132,84,4
WL 0
v_exp_f32 v215, v147
Mvaa 132,80,0
TR 208,222,0
CV 169,214,215
v_mfma_f32_16x16x32_bf16 v[136:139], a[96:99], a[0:3], v[176:179]
v_exp_f32 v212, v148
Mvaa 136,100,4
TR 210,222,4096
Mvaa 136,104,8
v_exp_f32 v213, v149
Mvaa 136,108,12
TR 212,222,8192
CV 170,212,213
v_mfma_f32_16x16x32_bf16 v[140:143], a[124:127], a[12:15], v[176:179]
v_exp_f32 v214, v150
Mvaa 140,120,8
TR 214,222,12288
Mvaa 140,116,4
v_exp_f32 v215, v151
Mvaa 140,112,0
TR 216,223,0
CV 171,214,215
Mvav 64,128,160
v_exp_f32 v212, v152
Mvav 64,132,164
TR 218,223,4096
Mvav 68,136,160
v_exp_f32 v213, v153
Mvav 68,140,164
TR 220,223,8192
CV 172,212,213
Mvav 72,144,160
v_exp_f32 v214, v154
Mvav 72,148,164
TR 222,223,12288
Mvav 76,152,160
v_exp_f32 v215, v155
Mvav 76,156,164
TR 224,224,0
CV 173,214,215
Mvav 80,160,160
v_exp_f32 v212, v156
Mvav 80,164,164
TR 226,224,4096
Mvav 84,168,160
v_exp_f32 v213, v157
Mvav 84,172,164
TR 228,224,8192
CV 174,212,213
Mvav 88,176,160
v_exp_f32 v214, v158
Mvav 88,180,164
TR 230,224,12288
Mvav 92,184,160
v_exp_f32 v215, v159
Mvav 92,188,164
TR 232,225,0
CV 175,214,215
v_smfmac_f32_16x16x64_bf16 v[200:203], v[208:211], v[160:167], v239
TR 234,225,4096
v_mfma_f32_16x16x32_bf16 v[144:147], a[64:67], a[16:19], v[180:183]
v_exp_f32 v212, v128
Mvaa 144,68,20
TR 236,225,8192
Mvaa 144,72,24
v_exp_f32 v213, v129
Mvaa 144,76,28
TR 238,225,12288
CV 160,212,213
v_mfma_f32_16x16x32_bf16 v[148:151], a[92:95], a[28:31], v[180:183]
v_exp_f32 v214, v130
Mvaa 148,88,24
TR 240,226,0
Mvaa 148,84,20
v_exp_f32 v215, v131
Mvaa 148,80,16
TR 242,226,4096
CV 161,214,215
v_mfma_f32_16x16x32_bf16 v[152:155], a[96:99], a[16:19], v[180:183]
v_exp_f32 v212, v132
Mvaa 152,100,20
TR 244,226,8192
Mvaa 152,104,24
v_exp_f32 v213, v133
Mvaa 152,108,28
TR 246,226,12288
CV 162,212,213
v_mfma_f32_16x16x32_bf16 v[156:159], a[124:127], a[28:31], v[180:183]
v_exp_f32 v214, v134
Mvaa 156,120,24
TR 248,227,0
Mvaa 156,116,20
v_exp_f32 v215, v135
Mvaa 156,112,16
TR 250,227,4096
CV 163,214,215
Mvav 96,128,168
v_exp_f32 v212, v136
Mvav 96,132,172
TR 252,227,8192
Mvav 100,136,168
v_exp_f32 v213, v137
Mvav 100,140,172
TR 254,227,12288
CV 164,212,213
Mvav 104,144,168
v_exp_f32 v214, v138
Mvav 104,148,172
Mvav 108,152,168
v_exp_f32 v215, v139
Mvav 108,156,172
ds_read_b128 v[242:245], v246 offset:3072
CV 165,214,215
Mvav 112,160,168
v_exp_f32 v212, v140
Mvav 112,164,172
Mvav 116,168,168
v_exp_f32 v213, v141
Mvav 116,172,172
CV 166,212,213
Mvav 120,176,168
v_exp_f32 v214, v142
Mvav 120,180,172
s_waitcnt vmcnt(18) lgkmcnt(0)
s_barrier
Mvav 124,184,168
v_exp_f32 v215, v143
Mvav 124,188,172
CV 167,214,215
v_smfmac_f32_16x16x64_bf16 v[204:207], v[208:211], v[168:175], v239
v_mfma_f32_16x16x32_bf16 v[128:131], a[64:67], a[32:35], v[184:187]
s_add_u32 m0, s84, 0
v_exp_f32 v212, v144
Mvaa 128,68,36
DM 228,44,87,0
Mvaa 128,72,40
v_exp_f32 v213, v145
Mvaa 128,76,44
TR 128,220,16384
CV 168,212,213
v_mfma_f32_16x16x32_bf16 v[132:135], a[92:95], a[44:47], v[184:187]
v_exp_f32 v214, v146
Mvaa 132,88,40
DM 229,44,87,1024
Mvaa 132,84,36
v_exp_f32 v215, v147
Mvaa 132,80,32
TR 130,220,20480
CV 169,214,215
v_mfma_f32_16x16x32_bf16 v[136:139], a[96:99], a[32:35], v[184:187]
v_exp_f32 v212, v148
Mvaa 136,100,36
DM 230,44,87,2048
Mvaa 136,104,40
v_exp_f32 v213, v149
Mvaa 136,108,44
TR 132,220,24576
CV 170,212,213
v_mfma_f32_16x16x32_bf16 v[140:143], a[124:127], a[44:47], v[184:187]
v_exp_f32 v214, v150
Mvaa 140,120,40
DM 231,44,87,3072
Mvaa 140,116,36
v_exp_f32 v215, v151
Mvaa 140,112,32
TR 134,220,28672
CV 171,214,215
Mvav 0,192,160
s_add_u32 m0, s85, 0
v_exp_f32 v212, v152
Mvav 0,196,164
DM 232,48,87,0
Mvav 4,200,160
v_exp_f32 v213, v153
Mvav 4,204,164
)"
R"(TR 136,221,16384
CV 172,212,213
Mvav 8,208,160
v_exp_f32 v214, v154
Mvav 8,212,164
DM 233,48,87,1024
Mvav 12,216,160
v_exp_f32 v215, v155
Mvav 12,220,164
TR 138,221,20480
CV 173,214,215
Mvav 16,224,160
v_exp_f32 v212, v156
Mvav 16,228,164
DM 232,48,87,2048
Mvav 20,232,160
v_exp_f32 v213, v157
Mvav 20,236,164
TR 140,221,24576
CV 174,212,213
Mvav 24,240,160
v_exp_f32 v214, v158
Mvav 24,244,164
DM 233,48,87,3072
Mvav 28,248,160
v_exp_f32 v215, v159
Mvav 28,252,164
TR 142,221,28672
CV 175,214,215
v_smfmac_f32_16x16x64_bf16 v[192:195], v[208:211], v[160:167], v239
buffer_store_dwordx4 v[242:245], v247, s[72:75], s23 offen sc1
v_mfma_f32_16x16x32_bf16 v[144:147], a[64:67], a[48:51], v[188:191]
v_exp_f32 v212, v128
Mvaa 144,68,52
ds_read_b128 a[64:67], v216 offset:16384
Mvaa 144,72,56
v_exp_f32 v213, v129
Mvaa 144,76,60
ds_read_b128 a[68:71], v217 offset:16384
CV 160,212,213
v_mfma_f32_16x16x32_bf16 v[148:151], a[92:95], a[60:63], v[188:191]
s_add_u32 s87, s87, 16384
v_exp_f32 v214, v130
Mvaa 148,88,56
ds_read_b128 a[72:75], v218 offset:16384
Mvaa 148,84,52
v_exp_f32 v215, v131
Mvaa 148,80,48
ds_read_b128 a[76:79], v219 offset:16384
CV 161,214,215
v_mfma_f32_16x16x32_bf16 v[152:155], a[96:99], a[48:51], v[188:191]
v_exp_f32 v212, v132
Mvaa 152,100,52
ds_read_b128 a[92:95], v219 offset:20480
Mvaa 152,104,56
v_exp_f32 v213, v133
Mvaa 152,108,60
ds_read_b128 a[88:91], v218 offset:20480
CV 162,212,213
v_mfma_f32_16x16x32_bf16 v[156:159], a[124:127], a[60:63], v[188:191]
v_exp_f32 v214, v134
Mvaa 156,120,56
ds_read_b128 a[84:87], v217 offset:20480
Mvaa 156,116,52
v_exp_f32 v215, v135
Mvaa 156,112,48
ds_read_b128 a[80:83], v216 offset:20480
CV 163,214,215
Mvav 32,192,168
v_exp_f32 v212, v136
Mvav 32,196,172
ds_read_b128 a[96:99], v216 offset:24576
Mvav 36,200,168
v_exp_f32 v213, v137
Mvav 36,204,172
ds_read_b128 a[100:103], v217 offset:24576
CV 164,212,213
Mvav 40,208,168
v_exp_f32 v214, v138
Mvav 40,212,172
ds_read_b128 a[104:107], v218 offset:24576
Mvav 44,216,168
v_exp_f32 v215, v139
Mvav 44,220,172
ds_read_b128 a[108:111], v219 offset:24576
CV 165,214,215
Mvav 48,224,168
v_exp_f32 v212, v140
Mvav 48,228,172
ds_read_b128 a[124:127], v219 offset:28672
Mvav 52,232,168
v_exp_f32 v213, v141
Mvav 52,236,172
ds_read_b128 a[120:123], v218 offset:28672
CV 166,212,213
Mvav 56,240,168
v_exp_f32 v214, v142
Mvav 56,244,172
ds_read_b128 a[116:119], v217 offset:28672
Mvav 60,248,168
v_exp_f32 v215, v143
Mvav 60,252,172
ds_read_b128 a[112:115], v216 offset:28672
CV 167,214,215
v_smfmac_f32_16x16x64_bf16 v[196:199], v[208:211], v[168:175], v239
WL 8
v_mfma_f32_16x16x32_bf16 v[128:131], a[64:67], a[0:3], v[176:179]
v_exp_f32 v212, v144
Mvaa 128,68,4
Mvaa 128,72,8
v_exp_f32 v213, v145
Mvaa 128,76,12
CV 168,212,213
v_mfma_f32_16x16x32_bf16 v[132:135], a[92:95], a[12:15], v[176:179]
v_exp_f32 v214, v146
Mvaa 132,88,8
Mvaa 132,84,4
WL 0
v_exp_f32 v215, v147
Mvaa 132,80,0
TR 144,222,16384
CV 169,214,215
v_mfma_f32_16x16x32_bf16 v[136:139], a[96:99], a[0:3], v[176:179]
v_exp_f32 v212, v148
Mvaa 136,100,4
TR 146,222,20480
Mvaa 136,104,8
v_exp_f32 v213, v149
Mvaa 136,108,12
TR 148,222,24576
CV 170,212,213
v_mfma_f32_16x16x32_bf16 v[140:143], a[124:127], a[12:15], v[176:179]
v_exp_f32 v214, v150
Mvaa 140,120,8
TR 150,222,28672
Mvaa 140,116,4
v_exp_f32 v215, v151
Mvaa 140,112,0
TR 152,223,16384
CV 171,214,215
Mvav 64,192,160
v_exp_f32 v212, v152
Mvav 64,196,164
TR 154,223,20480
Mvav 68,200,160
v_exp_f32 v213, v153
Mvav 68,204,164
TR 156,223,24576
CV 172,212,213
Mvav 72,208,160
v_exp_f32 v214, v154
Mvav 72,212,164
TR 158,223,28672
Mvav 76,216,160
v_exp_f32 v215, v155
Mvav 76,220,164
TR 160,224,16384
CV 173,214,215
Mvav 80,224,160
v_exp_f32 v212, v156
Mvav 80,228,164
TR 162,224,20480
Mvav 84,232,160
Lstub7_fused_block:
s_cbranch_execz Ltourend_fused_block
v_exp_f32 v213, v157
Mvav 84,236,164
TR 164,224,24576
CV 174,212,213
Mvav 88,240,160
v_exp_f32 v214, v158
Mvav 88,244,164
TR 166,224,28672
Mvav 92,248,160
v_exp_f32 v215, v159
Mvav 92,252,164
TR 168,225,16384
CV 175,214,215
v_smfmac_f32_16x16x64_bf16 v[200:203], v[208:211], v[160:167], v239
TR 170,225,20480
v_mfma_f32_16x16x32_bf16 v[144:147], a[64:67], a[16:19], v[180:183]
v_exp_f32 v212, v128
Mvaa 144,68,20
TR 172,225,24576
Mvaa 144,72,24
v_exp_f32 v213, v129
Mvaa 144,76,28
TR 174,225,28672
CV 160,212,213
v_mfma_f32_16x16x32_bf16 v[148:151], a[92:95], a[28:31], v[180:183]
v_exp_f32 v214, v130
Mvaa 148,88,24
TR 176,226,16384
Mvaa 148,84,20
v_exp_f32 v215, v131
Mvaa 148,80,16
TR 178,226,20480
CV 161,214,215
v_mfma_f32_16x16x32_bf16 v[152:155], a[96:99], a[16:19], v[180:183]
v_exp_f32 v212, v132
Mvaa 152,100,20
TR 180,226,24576
Mvaa 152,104,24
v_exp_f32 v213, v133
Mvaa 152,108,28
TR 182,226,28672
CV 162,212,213
v_mfma_f32_16x16x32_bf16 v[156:159], a[124:127], a[28:31], v[180:183]
v_exp_f32 v214, v134
Mvaa 156,120,24
TR 184,227,16384
Mvaa 156,116,20
v_exp_f32 v215, v135
Mvaa 156,112,16
TR 186,227,20480
CV 163,214,215
Mvav 96,192,168
v_exp_f32 v212, v136
Mvav 96,196,172
TR 188,227,24576
Mvav 100,200,168
s_sub_u32 s93, s93, 1
v_exp_f32 v213, v137
Mvav 100,204,172
TR 190,227,28672
CV 164,212,213
Mvav 104,208,168
v_exp_f32 v214, v138
Mvav 104,212,172
s_swappc_b64 s[10:11], s[8:9]
Mvav 108,216,168
v_exp_f32 v215, v139
Mvav 108,220,172
ds_read_b128 v[242:245], v246 offset:0
CV 165,214,215
Mvav 112,224,168
v_exp_f32 v212, v140
Mvav 112,228,172
Mvav 116,232,168
v_exp_f32 v213, v141
Mvav 116,236,172
CV 166,212,213
Mvav 120,240,168
v_exp_f32 v214, v142
Mvav 120,244,172
s_waitcnt vmcnt(18) lgkmcnt(0)
s_barrier
Mvav 124,248,168
s_cmp_lg_u32 s93, 0
v_exp_f32 v215, v143
Mvav 124,252,172
CV 167,214,215
v_smfmac_f32_16x16x64_bf16 v[204:207], v[208:211], v[168:175], v239
s_cbranch_scc1 Lloop_fused_block
s_add_u32 s87, s30, 0
s_add_u32 s88, s30, 73728
s_add_u32 s89, s31, 0
v_mfma_f32_16x16x32_bf16 v[128:131], a[64:67], a[32:35], v[184:187]
s_add_u32 m0, s84, 16384
v_exp_f32 v212, v144
Mvaa 128,68,36
DM 235,64,87,0
Mvaa 128,72,40
v_exp_f32 v213, v145
Mvaa 128,76,44
TR 192,220,32768
CV 168,212,213
v_mfma_f32_16x16x32_bf16 v[132:135], a[92:95], a[44:47], v[184:187]
v_exp_f32 v214, v146
Mvaa 132,88,40
DM 236,64,87,1024
Mvaa 132,84,36
v_exp_f32 v215, v147
Mvaa 132,80,32
TR 194,220,36864
CV 169,214,215
v_mfma_f32_16x16x32_bf16 v[136:139], a[96:99], a[32:35], v[184:187]
v_exp_f32 v212, v148
Mvaa 136,100,36
DM 237,64,87,2048
Mvaa 136,104,40
v_exp_f32 v213, v149
Mvaa 136,108,44
TR 196,220,40960
CV 170,212,213
v_mfma_f32_16x16x32_bf16 v[140:143], a[124:127], a[44:47], v[184:187]
v_exp_f32 v214, v150
Mvaa 140,120,40
DM 238,64,87,3072
Mvaa 140,116,36
v_exp_f32 v215, v151
Mvaa 140,112,32
TR 198,220,45056
CV 171,214,215
Mvav 0,128,160
s_add_u32 m0, s85, 16384
v_exp_f32 v212, v152
Mvav 0,132,164
DM 235,64,88,0
Mvav 4,136,160
v_exp_f32 v213, v153
Mvav 4,140,164
TR 200,221,32768
CV 172,212,213
Mvav 8,144,160
v_exp_f32 v214, v154
Mvav 8,148,164
DM 236,64,88,1024
Mvav 12,152,160
v_exp_f32 v215, v155
Mvav 12,156,164
TR 202,221,36864
CV 173,214,215
Mvav 16,160,160
v_exp_f32 v212, v156
Mvav 16,164,164
DM 237,64,88,2048
Mvav 20,168,160
v_exp_f32 v213, v157
Mvav 20,172,164
TR 204,221,40960
CV 174,212,213
Mvav 24,176,160
v_exp_f32 v214, v158
Mvav 24,180,164
DM 238,64,88,3072
Mvav 28,184,160
s_add_u32 m0, s86, 4096
v_exp_f32 v215, v159
Mvav 28,188,164
TR 206,221,45056
CV 175,214,215
v_smfmac_f32_16x16x64_bf16 v[192:195], v[208:211], v[160:167], v239
buffer_load_dwordx4 v234, s[72:75], s89 offen offset:0 sc1 lds
v_mfma_f32_16x16x32_bf16 v[144:147], a[64:67], a[48:51], v[188:191]
v_exp_f32 v212, v128
Mvaa 144,68,52
ds_read_b128 a[64:67], v216 offset:32768
Mvaa 144,72,56
v_exp_f32 v213, v129
Mvaa 144,76,60
ds_read_b128 a[68:71], v217 offset:32768
CV 160,212,213
v_mfma_f32_16x16x32_bf16 v[148:151], a[92:95], a[60:63], v[188:191]
v_exp_f32 v214, v130
Mvaa 148,88,56
ds_read_b128 a[72:75], v218 offset:32768
Mvaa 148,84,52
v_exp_f32 v215, v131
Mvaa 148,80,48
ds_read_b128 a[76:79], v219 offset:32768
CV 161,214,215
v_mfma_f32_16x16x32_bf16 v[152:155], a[96:99], a[48:51], v[188:191]
v_exp_f32 v212, v132
Mvaa 152,100,52
ds_read_b128 a[92:95], v219 offset:36864
Mvaa 152,104,56
v_exp_f32 v213, v133
Mvaa 152,108,60
ds_read_b128 a[88:91], v218 offset:36864
CV 162,212,213
v_mfma_f32_16x16x32_bf16 v[156:159], a[124:127], a[60:63], v[188:191]
v_exp_f32 v214, v134
Mvaa 156,120,56
ds_read_b128 a[84:87], v217 offset:36864
Mvaa 156,116,52
v_exp_f32 v215, v135
Mvaa 156,112,48
ds_read_b128 a[80:83], v216 offset:36864
CV 163,214,215
Mvav 32,128,168
v_exp_f32 v212, v136
Mvav 32,132,172
ds_read_b128 a[96:99], v216 offset:40960
Mvav 36,136,168
v_exp_f32 v213, v137
Mvav 36,140,172
ds_read_b128 a[100:103], v217 offset:40960
CV 164,212,213
Mvav 40,144,168
v_exp_f32 v214, v138
Mvav 40,148,172
ds_read_b128 a[104:107], v218 offset:40960
Mvav 44,152,168
v_exp_f32 v215, v139
Mvav 44,156,172
ds_read_b128 a[108:111], v219 offset:40960
CV 165,214,215
Mvav 48,160,168
v_exp_f32 v212, v140
Mvav 48,164,172
ds_read_b128 a[124:127], v219 offset:45056
Mvav 52,168,168
v_exp_f32 v213, v141
Mvav 52,172,172
ds_read_b128 a[120:123], v218 offset:45056
CV 166,212,213
Mvav 56,176,168
v_exp_f32 v214, v142
Mvav 56,180,172
)"
R"(ds_read_b128 a[116:119], v217 offset:45056
Mvav 60,184,168
v_exp_f32 v215, v143
Mvav 60,188,172
ds_read_b128 a[112:115], v216 offset:45056
CV 167,214,215
v_smfmac_f32_16x16x64_bf16 v[196:199], v[208:211], v[168:175], v239
WL 8
buffer_store_dwordx4 v[242:245], v247, s[72:75], s23 offen offset:1024 sc1
v_mfma_f32_16x16x32_bf16 v[128:131], a[64:67], a[0:3], v[176:179]
v_exp_f32 v212, v144
Mvaa 128,68,4
Mvaa 128,72,8
v_exp_f32 v213, v145
Mvaa 128,76,12
CV 168,212,213
v_mfma_f32_16x16x32_bf16 v[132:135], a[92:95], a[12:15], v[176:179]
v_exp_f32 v214, v146
Mvaa 132,88,8
Mvaa 132,84,4
WL 0
v_exp_f32 v215, v147
Mvaa 132,80,0
TR 208,222,32768
CV 169,214,215
v_mfma_f32_16x16x32_bf16 v[136:139], a[96:99], a[0:3], v[176:179]
v_exp_f32 v212, v148
Mvaa 136,100,4
TR 210,222,36864
Mvaa 136,104,8
v_exp_f32 v213, v149
Mvaa 136,108,12
TR 212,222,40960
CV 170,212,213
v_mfma_f32_16x16x32_bf16 v[140:143], a[124:127], a[12:15], v[176:179]
v_exp_f32 v214, v150
Mvaa 140,120,8
TR 214,222,45056
Mvaa 140,116,4
v_exp_f32 v215, v151
Mvaa 140,112,0
TR 216,223,32768
CV 171,214,215
Mvav 64,128,160
v_exp_f32 v212, v152
Mvav 64,132,164
TR 218,223,36864
Mvav 68,136,160
v_exp_f32 v213, v153
Mvav 68,140,164
TR 220,223,40960
CV 172,212,213
Mvav 72,144,160
v_exp_f32 v214, v154
Mvav 72,148,164
TR 222,223,45056
Mvav 76,152,160
v_exp_f32 v215, v155
Mvav 76,156,164
TR 224,224,32768
CV 173,214,215
Mvav 80,160,160
v_exp_f32 v212, v156
Mvav 80,164,164
TR 226,224,36864
Mvav 84,168,160
v_exp_f32 v213, v157
Mvav 84,172,164
TR 228,224,40960
CV 174,212,213
Mvav 88,176,160
v_exp_f32 v214, v158
Mvav 88,180,164
TR 230,224,45056
Mvav 92,184,160
v_exp_f32 v215, v159
Mvav 92,188,164
TR 232,225,32768
CV 175,214,215
v_smfmac_f32_16x16x64_bf16 v[200:203], v[208:211], v[160:167], v239
TR 234,225,36864
v_mfma_f32_16x16x32_bf16 v[144:147], a[64:67], a[16:19], v[180:183]
v_exp_f32 v212, v128
Mvaa 144,68,20
TR 236,225,40960
Mvaa 144,72,24
s_add_u32 s87, s30, 147456
v_exp_f32 v213, v129
Mvaa 144,76,28
TR 238,225,45056
CV 160,212,213
v_mfma_f32_16x16x32_bf16 v[148:151], a[92:95], a[28:31], v[180:183]
s_add_u32 s88, s30, 221184
v_exp_f32 v214, v130
Mvaa 148,88,24
TR 240,226,32768
Mvaa 148,84,20
s_add_u32 s89, s31, 4096
v_exp_f32 v215, v131
Mvaa 148,80,16
TR 242,226,36864
CV 161,214,215
v_mfma_f32_16x16x32_bf16 v[152:155], a[96:99], a[16:19], v[180:183]
v_exp_f32 v212, v132
Mvaa 152,100,20
TR 244,226,40960
Mvaa 152,104,24
v_exp_f32 v213, v133
Mvaa 152,108,28
TR 246,226,45056
CV 162,212,213
v_mfma_f32_16x16x32_bf16 v[156:159], a[124:127], a[28:31], v[180:183]
v_exp_f32 v214, v134
Mvaa 156,120,24
TR 248,227,32768
Mvaa 156,116,20
v_exp_f32 v215, v135
Mvaa 156,112,16
TR 250,227,36864
CV 163,214,215
Mvav 96,128,168
v_exp_f32 v212, v136
Mvav 96,132,172
TR 252,227,40960
Mvav 100,136,168
v_exp_f32 v213, v137
Mvav 100,140,172
TR 254,227,45056
CV 164,212,213
Mvav 104,144,168
v_exp_f32 v214, v138
Mvav 104,148,172
Mvav 108,152,168
v_exp_f32 v215, v139
Mvav 108,156,172
CV 165,214,215
Mvav 112,160,168
v_exp_f32 v212, v140
Mvav 112,164,172
Mvav 116,168,168
v_exp_f32 v213, v141
Mvav 116,172,172
CV 166,212,213
Mvav 120,176,168
v_exp_f32 v214, v142
Mvav 120,180,172
s_waitcnt vmcnt(20) lgkmcnt(0)
s_barrier
Mvav 124,184,168
v_exp_f32 v215, v143
Mvav 124,188,172
CV 167,214,215
v_smfmac_f32_16x16x64_bf16 v[204:207], v[208:211], v[168:175], v239
v_mfma_f32_16x16x32_bf16 v[128:131], a[64:67], a[32:35], v[184:187]
s_add_u32 m0, s84, 32768
v_exp_f32 v212, v144
Mvaa 128,68,36
DM 235,64,87,0
Mvaa 128,72,40
v_exp_f32 v213, v145
Mvaa 128,76,44
TR 128,220,49152
CV 168,212,213
v_mfma_f32_16x16x32_bf16 v[132:135], a[92:95], a[44:47], v[184:187]
v_exp_f32 v214, v146
Mvaa 132,88,40
DM 236,64,87,1024
Mvaa 132,84,36
v_exp_f32 v215, v147
Mvaa 132,80,32
TR 130,220,53248
CV 169,214,215
v_mfma_f32_16x16x32_bf16 v[136:139], a[96:99], a[32:35], v[184:187]
v_exp_f32 v212, v148
Mvaa 136,100,36
DM 237,64,87,2048
Mvaa 136,104,40
v_exp_f32 v213, v149
Mvaa 136,108,44
TR 132,220,57344
CV 170,212,213
v_mfma_f32_16x16x32_bf16 v[140:143], a[124:127], a[44:47], v[184:187]
v_exp_f32 v214, v150
Mvaa 140,120,40
DM 238,64,87,3072
Mvaa 140,116,36
v_exp_f32 v215, v151
Mvaa 140,112,32
TR 134,220,61440
CV 171,214,215
Mvav 0,192,160
s_add_u32 m0, s85, 32768
v_exp_f32 v212, v152
Mvav 0,196,164
DM 235,64,88,0
Mvav 4,200,160
v_exp_f32 v213, v153
Mvav 4,204,164
TR 136,221,49152
CV 172,212,213
Mvav 8,208,160
v_exp_f32 v214, v154
Mvav 8,212,164
DM 236,64,88,1024
Mvav 12,216,160
v_exp_f32 v215, v155
Mvav 12,220,164
TR 138,221,53248
CV 173,214,215
Mvav 16,224,160
v_exp_f32 v212, v156
Mvav 16,228,164
DM 237,64,88,2048
Mvav 20,232,160
v_exp_f32 v213, v157
Mvav 20,236,164
TR 140,221,57344
CV 174,212,213
Mvav 24,240,160
v_exp_f32 v214, v158
Mvav 24,244,164
DM 238,64,88,3072
Mvav 28,248,160
s_add_u32 m0, s86, 8192
v_exp_f32 v215, v159
Mvav 28,252,164
TR 142,221,61440
CV 175,214,215
v_smfmac_f32_16x16x64_bf16 v[192:195], v[208:211], v[160:167], v239
buffer_load_dwordx4 v234, s[72:75], s89 offen offset:0 sc1 lds
v_mfma_f32_16x16x32_bf16 v[144:147], a[64:67], a[48:51], v[188:191]
s_lshl_b32 s96, s38, 3
v_exp_f32 v212, v128
Mvaa 144,68,52
ds_read_b128 a[64:67], v216 offset:49152
Mvaa 144,72,56
s_add_u32 s79, s79, s96
v_exp_f32 v213, v129
Mvaa 144,76,60
ds_read_b128 a[68:71], v217 offset:49152
CV 160,212,213
v_mfma_f32_16x16x32_bf16 v[148:151], a[92:95], a[60:63], v[188:191]
s_lshl_b32 s96, s38, 20
v_exp_f32 v214, v130
Mvaa 148,88,56
ds_read_b128 a[72:75], v218 offset:49152
Mvaa 148,84,52
s_add_u32 s33, s33, s96
v_exp_f32 v215, v131
Mvaa 148,80,48
ds_read_b128 a[76:79], v219 offset:49152
CV 161,214,215
v_mfma_f32_16x16x32_bf16 v[152:155], a[96:99], a[48:51], v[188:191]
s_lshl_b32 s96, s38, 23
v_exp_f32 v212, v132
Mvaa 152,100,52
ds_read_b128 a[92:95], v219 offset:53248
Mvaa 152,104,56
s_add_u32 s34, s34, s96
v_exp_f32 v213, v133
Mvaa 152,108,60
ds_read_b128 a[88:91], v218 offset:53248
CV 162,212,213
v_mfma_f32_16x16x32_bf16 v[156:159], a[124:127], a[60:63], v[188:191]
s_lshl_b32 s96, s38, 9
v_exp_f32 v214, v134
Mvaa 156,120,56
ds_read_b128 a[84:87], v217 offset:53248
Mvaa 156,116,52
s_add_u32 s35, s35, s96
v_exp_f32 v215, v135
Mvaa 156,112,48
ds_read_b128 a[80:83], v216 offset:53248
CV 163,214,215
Mvav 32,192,168
s_lshl_b32 s96, s38, 16
v_exp_f32 v212, v136
Mvav 32,196,172
ds_read_b128 a[96:99], v216 offset:57344
Mvav 36,200,168
s_add_u32 s36, s36, s96
v_exp_f32 v213, v137
Mvav 36,204,172
ds_read_b128 a[100:103], v217 offset:57344
CV 164,212,213
Mvav 40,208,168
s_mov_b32 s38, 0
v_exp_f32 v214, v138
Mvav 40,212,172
ds_read_b128 a[104:107], v218 offset:57344
Mvav 44,216,168
v_exp_f32 v215, v139
Mvav 44,220,172
ds_read_b128 a[108:111], v219 offset:57344
CV 165,214,215
Mvav 48,224,168
v_exp_f32 v212, v140
Mvav 48,228,172
ds_read_b128 a[124:127], v219 offset:61440
Mvav 52,232,168
v_exp_f32 v213, v141
Mvav 52,236,172
ds_read_b128 a[120:123], v218 offset:61440
CV 166,212,213
Mvav 56,240,168
v_exp_f32 v214, v142
Mvav 56,244,172
ds_read_b128 a[116:119], v217 offset:61440
Mvav 60,248,168
v_exp_f32 v215, v143
Mvav 60,252,172
ds_read_b128 a[112:115], v216 offset:61440
CV 167,214,215
v_smfmac_f32_16x16x64_bf16 v[196:199], v[208:211], v[168:175], v239
WL 8
buffer_store_dwordx4 v[242:245], v247, s[72:75], s23 offen offset:2048 sc1
v_mfma_f32_16x16x32_bf16 v[128:131], a[64:67], a[0:3], v[176:179]
v_exp_f32 v212, v144
Mvaa 128,68,4
Mvaa 128,72,8
v_exp_f32 v213, v145
Mvaa 128,76,12
CV 168,212,213
v_mfma_f32_16x16x32_bf16 v[132:135], a[92:95], a[12:15], v[176:179]
v_exp_f32 v214, v146
Mvaa 132,88,8
Mvaa 132,84,4
WL 0
v_exp_f32 v215, v147
Mvaa 132,80,0
TR 144,222,49152
CV 169,214,215
v_mfma_f32_16x16x32_bf16 v[136:139], a[96:99], a[0:3], v[176:179]
v_exp_f32 v212, v148
Mvaa 136,100,4
TR 146,222,53248
Mvaa 136,104,8
v_exp_f32 v213, v149
Mvaa 136,108,12
TR 148,222,57344
CV 170,212,213
v_mfma_f32_16x16x32_bf16 v[140:143], a[124:127], a[12:15], v[176:179]
v_exp_f32 v214, v150
Mvaa 140,120,8
TR 150,222,61440
Mvaa 140,116,4
v_exp_f32 v215, v151
Mvaa 140,112,0
TR 152,223,49152
CV 171,214,215
Mvav 64,192,160
Lstub8_fused_block:
s_cbranch_execz Ltourend_fused_block
v_exp_f32 v212, v152
Mvav 64,196,164
TR 154,223,53248
Mvav 68,200,160
v_exp_f32 v213, v153
Mvav 68,204,164
TR 156,223,57344
CV 172,212,213
Mvav 72,208,160
v_exp_f32 v214, v154
Mvav 72,212,164
TR 158,223,61440
Mvav 76,216,160
v_exp_f32 v215, v155
Mvav 76,220,164
TR 160,224,49152
CV 173,214,215
Mvav 80,224,160
v_exp_f32 v212, v156
Mvav 80,228,164
TR 162,224,53248
Mvav 84,232,160
v_exp_f32 v213, v157
Mvav 84,236,164
TR 164,224,57344
CV 174,212,213
Mvav 88,240,160
v_exp_f32 v214, v158
Mvav 88,244,164
TR 166,224,61440
Mvav 92,248,160
v_exp_f32 v215, v159
Mvav 92,252,164
TR 168,225,49152
CV 175,214,215
v_smfmac_f32_16x16x64_bf16 v[200:203], v[208:211], v[160:167], v239
TR 170,225,53248
v_mfma_f32_16x16x32_bf16 v[144:147], a[64:67], a[16:19], v[180:183]
v_exp_f32 v212, v128
Mvaa 144,68,20
TR 172,225,57344
Mvaa 144,72,24
s_add_u32 s87, s30, 294912
v_exp_f32 v213, v129
Mvaa 144,76,28
TR 174,225,61440
CV 160,212,213
)"
R"(v_mfma_f32_16x16x32_bf16 v[148:151], a[92:95], a[28:31], v[180:183]
s_add_u32 s88, s30, 368640
v_exp_f32 v214, v130
Mvaa 148,88,24
TR 176,226,49152
Mvaa 148,84,20
s_add_u32 s89, s31, 8192
v_exp_f32 v215, v131
Mvaa 148,80,16
TR 178,226,53248
CV 161,214,215
v_mfma_f32_16x16x32_bf16 v[152:155], a[96:99], a[16:19], v[180:183]
v_exp_f32 v212, v132
Mvaa 152,100,20
TR 180,226,57344
Mvaa 152,104,24
v_exp_f32 v213, v133
Mvaa 152,108,28
TR 182,226,61440
CV 162,212,213
v_mfma_f32_16x16x32_bf16 v[156:159], a[124:127], a[28:31], v[180:183]
v_exp_f32 v214, v134
Mvaa 156,120,24
TR 184,227,49152
Mvaa 156,116,20
v_exp_f32 v215, v135
Mvaa 156,112,16
TR 186,227,53248
CV 163,214,215
Mvav 96,192,168
v_exp_f32 v212, v136
Mvav 96,196,172
TR 188,227,57344
Mvav 100,200,168
v_exp_f32 v213, v137
Mvav 100,204,172
TR 190,227,61440
CV 164,212,213
Mvav 104,208,168
v_exp_f32 v214, v138
Mvav 104,212,172
Mvav 108,216,168
v_exp_f32 v215, v139
Mvav 108,220,172
CV 165,214,215
Mvav 112,224,168
v_exp_f32 v212, v140
Mvav 112,228,172
Mvav 116,232,168
v_exp_f32 v213, v141
Mvav 116,236,172
CV 166,212,213
Mvav 120,240,168
v_exp_f32 v214, v142
Mvav 120,244,172
s_waitcnt vmcnt(21) lgkmcnt(0)
s_barrier
Mvav 124,248,168
v_exp_f32 v215, v143
Mvav 124,252,172
CV 167,214,215
v_smfmac_f32_16x16x64_bf16 v[204:207], v[208:211], v[168:175], v239
v_mfma_f32_16x16x32_bf16 v[128:131], a[64:67], a[32:35], v[184:187]
s_add_u32 m0, s84, 49152
v_exp_f32 v212, v144
Mvaa 128,68,36
DM 235,64,87,0
Mvaa 128,72,40
v_exp_f32 v213, v145
Mvaa 128,76,44
TR 192,220,0
CV 168,212,213
v_mfma_f32_16x16x32_bf16 v[132:135], a[92:95], a[44:47], v[184:187]
v_exp_f32 v214, v146
Mvaa 132,88,40
DM 236,64,87,1024
Mvaa 132,84,36
v_exp_f32 v215, v147
Mvaa 132,80,32
TR 194,220,4096
CV 169,214,215
v_mfma_f32_16x16x32_bf16 v[136:139], a[96:99], a[32:35], v[184:187]
v_exp_f32 v212, v148
Mvaa 136,100,36
DM 237,64,87,2048
Mvaa 136,104,40
v_exp_f32 v213, v149
Mvaa 136,108,44
TR 196,220,8192
CV 170,212,213
v_mfma_f32_16x16x32_bf16 v[140:143], a[124:127], a[44:47], v[184:187]
v_exp_f32 v214, v150
Mvaa 140,120,40
DM 238,64,87,3072
Mvaa 140,116,36
v_exp_f32 v215, v151
Mvaa 140,112,32
TR 198,220,12288
CV 171,214,215
Mvav 0,128,160
s_add_u32 m0, s85, 49152
v_exp_f32 v212, v152
Mvav 0,132,164
DM 235,64,88,0
Mvav 4,136,160
v_exp_f32 v213, v153
Mvav 4,140,164
TR 200,221,0
CV 172,212,213
Mvav 8,144,160
v_exp_f32 v214, v154
Mvav 8,148,164
DM 236,64,88,1024
Mvav 12,152,160
v_exp_f32 v215, v155
Mvav 12,156,164
TR 202,221,4096
CV 173,214,215
Mvav 16,160,160
v_exp_f32 v212, v156
Mvav 16,164,164
DM 237,64,88,2048
Mvav 20,168,160
v_exp_f32 v213, v157
Mvav 20,172,164
TR 204,221,8192
CV 174,212,213
Mvav 24,176,160
v_exp_f32 v214, v158
Mvav 24,180,164
DM 238,64,88,3072
Mvav 28,184,160
s_add_u32 m0, s86, 12288
v_exp_f32 v215, v159
Mvav 28,188,164
TR 206,221,12288
CV 175,214,215
v_smfmac_f32_16x16x64_bf16 v[192:195], v[208:211], v[160:167], v239
buffer_load_dwordx4 v234, s[72:75], s89 offen offset:0 sc1 lds
v_mfma_f32_16x16x32_bf16 v[144:147], a[64:67], a[48:51], v[188:191]
v_exp_f32 v212, v128
Mvaa 144,68,52
ds_read_b128 a[64:67], v216 offset:0
Mvaa 144,72,56
v_exp_f32 v213, v129
Mvaa 144,76,60
ds_read_b128 a[68:71], v217 offset:0
CV 160,212,213
v_mfma_f32_16x16x32_bf16 v[148:151], a[92:95], a[60:63], v[188:191]
v_exp_f32 v214, v130
Mvaa 148,88,56
ds_read_b128 a[72:75], v218 offset:0
Mvaa 148,84,52
v_exp_f32 v215, v131
Mvaa 148,80,48
ds_read_b128 a[76:79], v219 offset:0
CV 161,214,215
v_mfma_f32_16x16x32_bf16 v[152:155], a[96:99], a[48:51], v[188:191]
v_exp_f32 v212, v132
Mvaa 152,100,52
ds_read_b128 a[92:95], v219 offset:4096
Mvaa 152,104,56
v_exp_f32 v213, v133
Mvaa 152,108,60
ds_read_b128 a[88:91], v218 offset:4096
CV 162,212,213
v_mfma_f32_16x16x32_bf16 v[156:159], a[124:127], a[60:63], v[188:191]
v_exp_f32 v214, v134
Mvaa 156,120,56
ds_read_b128 a[84:87], v217 offset:4096
Mvaa 156,116,52
v_exp_f32 v215, v135
Mvaa 156,112,48
ds_read_b128 a[80:83], v216 offset:4096
CV 163,214,215
Mvav 32,128,168
v_exp_f32 v212, v136
Mvav 32,132,172
ds_read_b128 a[96:99], v216 offset:8192
Mvav 36,136,168
v_exp_f32 v213, v137
Mvav 36,140,172
ds_read_b128 a[100:103], v217 offset:8192
CV 164,212,213
Mvav 40,144,168
v_exp_f32 v214, v138
Mvav 40,148,172
ds_read_b128 a[104:107], v218 offset:8192
Mvav 44,152,168
v_exp_f32 v215, v139
Mvav 44,156,172
ds_read_b128 a[108:111], v219 offset:8192
CV 165,214,215
Mvav 48,160,168
v_exp_f32 v212, v140
Mvav 48,164,172
ds_read_b128 a[124:127], v219 offset:12288
Mvav 52,168,168
v_exp_f32 v213, v141
Mvav 52,172,172
ds_read_b128 a[120:123], v218 offset:12288
CV 166,212,213
Mvav 56,176,168
v_exp_f32 v214, v142
Mvav 56,180,172
ds_read_b128 a[116:119], v217 offset:12288
Mvav 60,184,168
v_exp_f32 v215, v143
Mvav 60,188,172
ds_read_b128 a[112:115], v216 offset:12288
CV 167,214,215
v_smfmac_f32_16x16x64_bf16 v[196:199], v[208:211], v[168:175], v239
WL 8
buffer_store_dwordx4 v[242:245], v247, s[72:75], s23 offen offset:3072 sc1
v_mfma_f32_16x16x32_bf16 v[128:131], a[64:67], a[0:3], v[176:179]
v_exp_f32 v212, v144
Mvaa 128,68,4
v_subrev_u32 v241, 65536, v220
v_subrev_u32 v242, 65536, v221
v_subrev_u32 v243, 65536, v222
Mvaa 128,72,8
v_exp_f32 v213, v145
v_subrev_u32 v244, 65536, v223
Mvaa 128,76,12
CV 168,212,213
v_subrev_u32 v245, 65536, v224
v_subrev_u32 v246, 65536, v225
v_mfma_f32_16x16x32_bf16 v[132:135], a[92:95], a[12:15], v[176:179]
v_exp_f32 v214, v146
v_subrev_u32 v247, 65536, v226
Mvaa 132,88,8
v_subrev_u32 v248, 65536, v227
Mvaa 132,84,4
WL 0
v_exp_f32 v215, v147
Mvaa 132,80,0
TR 208,222,0
CV 169,214,215
v_mfma_f32_16x16x32_bf16 v[136:139], a[96:99], a[0:3], v[176:179]
v_exp_f32 v212, v148
Mvaa 136,100,4
TR 210,222,4096
Mvaa 136,104,8
v_exp_f32 v213, v149
Mvaa 136,108,12
TR 212,222,8192
CV 170,212,213
v_mfma_f32_16x16x32_bf16 v[140:143], a[124:127], a[12:15], v[176:179]
v_exp_f32 v214, v150
Mvaa 140,120,8
TR 214,222,12288
Mvaa 140,116,4
v_exp_f32 v215, v151
Mvaa 140,112,0
TR 216,223,0
CV 171,214,215
Mvav 64,128,160
v_exp_f32 v212, v152
Mvav 64,132,164
TR 218,223,4096
Mvav 68,136,160
v_exp_f32 v213, v153
Mvav 68,140,164
TR 220,223,8192
CV 172,212,213
Mvav 72,144,160
v_exp_f32 v214, v154
Mvav 72,148,164
TR 222,223,12288
Mvav 76,152,160
v_exp_f32 v215, v155
Mvav 76,156,164
TR 224,224,0
CV 173,214,215
Mvav 80,160,160
v_exp_f32 v212, v156
Mvav 80,164,164
TR 226,224,4096
Mvav 84,168,160
v_exp_f32 v213, v157
Mvav 84,172,164
TR 228,224,8192
CV 174,212,213
Mvav 88,176,160
v_exp_f32 v214, v158
Mvav 88,180,164
TR 230,224,12288
Mvav 92,184,160
v_exp_f32 v215, v159
Mvav 92,188,164
TR 232,225,0
CV 175,214,215
v_smfmac_f32_16x16x64_bf16 v[200:203], v[208:211], v[160:167], v239
TR 234,225,4096
v_mfma_f32_16x16x32_bf16 v[144:147], a[64:67], a[16:19], v[180:183]
v_exp_f32 v212, v128
Mvaa 144,68,20
TR 236,225,8192
Mvaa 144,72,24
s_add_u32 s87, s30, 442368
v_exp_f32 v213, v129
Mvaa 144,76,28
TR 238,225,12288
CV 160,212,213
v_mfma_f32_16x16x32_bf16 v[148:151], a[92:95], a[28:31], v[180:183]
s_add_u32 s88, s30, 516096
v_exp_f32 v214, v130
Mvaa 148,88,24
TR 240,226,0
Mvaa 148,84,20
s_add_u32 s89, s31, 12288
v_exp_f32 v215, v131
Mvaa 148,80,16
TR 242,226,4096
CV 161,214,215
v_mfma_f32_16x16x32_bf16 v[152:155], a[96:99], a[16:19], v[180:183]
v_exp_f32 v212, v132
Mvaa 152,100,20
TR 244,226,8192
Mvaa 152,104,24
v_exp_f32 v213, v133
Mvaa 152,108,28
TR 246,226,12288
CV 162,212,213
v_mfma_f32_16x16x32_bf16 v[156:159], a[124:127], a[28:31], v[180:183]
v_exp_f32 v214, v134
Mvaa 156,120,24
TR 248,227,0
Mvaa 156,116,20
v_exp_f32 v215, v135
Mvaa 156,112,16
TR 250,227,4096
CV 163,214,215
Mvav 96,128,168
v_exp_f32 v212, v136
Mvav 96,132,172
TR 252,227,8192
Mvav 100,136,168
v_exp_f32 v213, v137
Mvav 100,140,172
TR 254,227,12288
CV 164,212,213
Mvav 104,144,168
v_exp_f32 v214, v138
Mvav 104,148,172
Mvav 108,152,168
v_exp_f32 v215, v139
Mvav 108,156,172
CV 165,214,215
Mvav 112,160,168
v_exp_f32 v212, v140
Mvav 112,164,172
Mvav 116,168,168
v_exp_f32 v213, v141
Mvav 116,172,172
CV 166,212,213
Mvav 120,176,168
v_exp_f32 v214, v142
Mvav 120,180,172
s_waitcnt vmcnt(21) lgkmcnt(0)
s_barrier
Mvav 124,184,168
v_exp_f32 v215, v143
Mvav 124,188,172
CV 167,214,215
v_smfmac_f32_16x16x64_bf16 v[204:207], v[208:211], v[168:175], v239
s_setpc_b64 s[6:7]
.p2align 12
Lt2x_fused_block:
s_cbranch_execz Ltour2end_fused_block
Ltail_fused_block:
v_lshlrev_b32 v250, 16, v152
v_and_b32 v251, 0xffff0000, v152
v_pk_add_f32 v[128:129], v[128:129], v[250:251]
v_lshlrev_b32 v250, 16, v153
v_and_b32 v251, 0xffff0000, v153
v_pk_add_f32 v[130:131], v[130:131], v[250:251]
v_lshlrev_b32 v250, 16, v154
v_and_b32 v251, 0xffff0000, v154
v_pk_add_f32 v[132:133], v[132:133], v[250:251]
v_lshlrev_b32 v250, 16, v155
v_and_b32 v251, 0xffff0000, v155
v_pk_add_f32 v[134:135], v[134:135], v[250:251]
v_lshlrev_b32 v250, 16, v156
v_and_b32 v251, 0xffff0000, v156
v_pk_add_f32 v[136:137], v[136:137], v[250:251]
v_lshlrev_b32 v250, 16, v157
v_and_b32 v251, 0xffff0000, v157
v_pk_add_f32 v[138:139], v[138:139], v[250:251]
v_lshlrev_b32 v250, 16, v158
v_and_b32 v251, 0xffff0000, v158
v_pk_add_f32 v[140:141], v[140:141], v[250:251]
v_lshlrev_b32 v250, 16, v159
v_and_b32 v251, 0xffff0000, v159
v_pk_add_f32 v[142:143], v[142:143], v[250:251]
v_lshlrev_b32 v250, 16, v160
v_and_b32 v251, 0xffff0000, v160
v_pk_add_f32 v[144:145], v[144:145], v[250:251]
v_lshlrev_b32 v250, 16, v161
v_and_b32 v251, 0xffff0000, v161
)"
R"(v_pk_add_f32 v[146:147], v[146:147], v[250:251]
v_lshlrev_b32 v250, 16, v162
v_and_b32 v251, 0xffff0000, v162
v_pk_add_f32 v[128:129], v[128:129], v[250:251]
v_lshlrev_b32 v250, 16, v163
v_and_b32 v251, 0xffff0000, v163
v_pk_add_f32 v[130:131], v[130:131], v[250:251]
v_lshlrev_b32 v250, 16, v164
v_and_b32 v251, 0xffff0000, v164
v_pk_add_f32 v[132:133], v[132:133], v[250:251]
v_lshlrev_b32 v250, 16, v165
v_and_b32 v251, 0xffff0000, v165
v_pk_add_f32 v[134:135], v[134:135], v[250:251]
v_lshlrev_b32 v250, 16, v166
v_and_b32 v251, 0xffff0000, v166
v_pk_add_f32 v[136:137], v[136:137], v[250:251]
v_lshlrev_b32 v250, 16, v167
v_and_b32 v251, 0xffff0000, v167
v_pk_add_f32 v[138:139], v[138:139], v[250:251]
v_lshlrev_b32 v250, 16, v168
v_and_b32 v251, 0xffff0000, v168
v_pk_add_f32 v[140:141], v[140:141], v[250:251]
v_lshlrev_b32 v250, 16, v169
v_and_b32 v251, 0xffff0000, v169
v_pk_add_f32 v[142:143], v[142:143], v[250:251]
v_lshlrev_b32 v250, 16, v170
v_and_b32 v251, 0xffff0000, v170
v_pk_add_f32 v[144:145], v[144:145], v[250:251]
v_lshlrev_b32 v250, 16, v171
v_and_b32 v251, 0xffff0000, v171
v_pk_add_f32 v[146:147], v[146:147], v[250:251]
v_lshlrev_b32 v250, 16, v172
v_and_b32 v251, 0xffff0000, v172
v_pk_add_f32 v[128:129], v[128:129], v[250:251]
v_lshlrev_b32 v250, 16, v173
v_and_b32 v251, 0xffff0000, v173
v_pk_add_f32 v[130:131], v[130:131], v[250:251]
v_lshlrev_b32 v250, 16, v174
v_and_b32 v251, 0xffff0000, v174
v_pk_add_f32 v[132:133], v[132:133], v[250:251]
v_lshlrev_b32 v250, 16, v175
v_and_b32 v251, 0xffff0000, v175
v_pk_add_f32 v[134:135], v[134:135], v[250:251]
v_lshlrev_b32 v250, 16, v176
v_and_b32 v251, 0xffff0000, v176
v_pk_add_f32 v[136:137], v[136:137], v[250:251]
v_lshlrev_b32 v250, 16, v177
v_and_b32 v251, 0xffff0000, v177
v_pk_add_f32 v[138:139], v[138:139], v[250:251]
v_lshlrev_b32 v250, 16, v178
v_and_b32 v251, 0xffff0000, v178
v_pk_add_f32 v[140:141], v[140:141], v[250:251]
v_lshlrev_b32 v250, 16, v179
v_and_b32 v251, 0xffff0000, v179
v_pk_add_f32 v[142:143], v[142:143], v[250:251]
v_lshlrev_b32 v250, 16, v180
v_and_b32 v251, 0xffff0000, v180
v_pk_add_f32 v[144:145], v[144:145], v[250:251]
v_lshlrev_b32 v250, 16, v181
v_and_b32 v251, 0xffff0000, v181
v_pk_add_f32 v[146:147], v[146:147], v[250:251]
v_mul_f32 v184, 0xc13504f3, v128
v_mul_f32 v185, 0xc13504f3, v129
v_mul_f32 v186, 0xc13504f3, v130
v_mul_f32 v187, 0xc13504f3, v131
v_mul_f32 v188, 0xc13504f3, v132
v_mul_f32 v189, 0xc13504f3, v133
v_mul_f32 v190, 0xc13504f3, v134
v_mul_f32 v191, 0xc13504f3, v135
v_mul_f32 v192, 0xc13504f3, v136
v_mul_f32 v193, 0xc13504f3, v137
v_mul_f32 v194, 0xc13504f3, v138
v_mul_f32 v195, 0xc13504f3, v139
v_mul_f32 v196, 0xc13504f3, v140
v_mul_f32 v197, 0xc13504f3, v141
v_mul_f32 v198, 0xc13504f3, v142
v_mul_f32 v199, 0xc13504f3, v143
v_mul_f32 v200, 0xc13504f3, v144
v_mul_f32 v201, 0xc13504f3, v145
v_mul_f32 v202, 0xc13504f3, v146
v_mul_f32 v203, 0xc13504f3, v147
v_exp_f32 v184, v184
v_exp_f32 v185, v185
v_exp_f32 v186, v186
v_exp_f32 v187, v187
v_exp_f32 v188, v188
v_exp_f32 v189, v189
v_exp_f32 v190, v190
v_exp_f32 v191, v191
v_exp_f32 v192, v192
v_exp_f32 v193, v193
v_exp_f32 v194, v194
v_exp_f32 v195, v195
v_exp_f32 v196, v196
v_exp_f32 v197, v197
v_exp_f32 v198, v198
v_exp_f32 v199, v199
v_exp_f32 v200, v200
v_exp_f32 v201, v201
v_exp_f32 v202, v202
v_exp_f32 v203, v203
v_mov_b32 v250, 0x3e0293ee
v_pk_fma_f32 v[184:185], v[184:185], v[250:251], v[250:251] op_sel_hi:[1,0,0]
v_pk_fma_f32 v[186:187], v[186:187], v[250:251], v[250:251] op_sel_hi:[1,0,0]
v_pk_fma_f32 v[188:189], v[188:189], v[250:251], v[250:251] op_sel_hi:[1,0,0]
v_pk_fma_f32 v[190:191], v[190:191], v[250:251], v[250:251] op_sel_hi:[1,0,0]
v_pk_fma_f32 v[192:193], v[192:193], v[250:251], v[250:251] op_sel_hi:[1,0,0]
v_pk_fma_f32 v[194:195], v[194:195], v[250:251], v[250:251] op_sel_hi:[1,0,0]
v_pk_fma_f32 v[196:197], v[196:197], v[250:251], v[250:251] op_sel_hi:[1,0,0]
v_pk_fma_f32 v[198:199], v[198:199], v[250:251], v[250:251] op_sel_hi:[1,0,0]
v_pk_fma_f32 v[200:201], v[200:201], v[250:251], v[250:251] op_sel_hi:[1,0,0]
v_pk_fma_f32 v[202:203], v[202:203], v[250:251], v[250:251] op_sel_hi:[1,0,0]
v_rcp_f32 v184, v184
v_rcp_f32 v185, v185
v_rcp_f32 v186, v186
v_rcp_f32 v187, v187
v_rcp_f32 v188, v188
v_rcp_f32 v189, v189
v_rcp_f32 v190, v190
v_rcp_f32 v191, v191
v_rcp_f32 v192, v192
v_rcp_f32 v193, v193
v_rcp_f32 v194, v194
v_rcp_f32 v195, v195
v_rcp_f32 v196, v196
v_rcp_f32 v197, v197
v_rcp_f32 v198, v198
v_rcp_f32 v199, v199
v_rcp_f32 v200, v200
v_rcp_f32 v201, v201
v_rcp_f32 v202, v202
v_rcp_f32 v203, v203
s_mov_b32 s99, 0x20000
v_lshlrev_b32 v24, 16, v80
v_and_b32 v25, 0xffff0000, v80
v_lshlrev_b32 v26, 16, v81
v_and_b32 v27, 0xffff0000, v81
v_lshlrev_b32 v28, 16, v82
v_and_b32 v29, 0xffff0000, v82
v_lshlrev_b32 v30, 16, v83
v_and_b32 v31, 0xffff0000, v83
v_lshlrev_b32 v32, 16, v84
v_and_b32 v33, 0xffff0000, v84
v_lshlrev_b32 v34, 16, v85
v_and_b32 v35, 0xffff0000, v85
v_lshlrev_b32 v36, 16, v86
v_and_b32 v37, 0xffff0000, v86
v_lshlrev_b32 v38, 16, v87
v_and_b32 v39, 0xffff0000, v87
v_lshlrev_b32 v40, 16, v88
v_and_b32 v41, 0xffff0000, v88
v_lshlrev_b32 v42, 16, v89
v_and_b32 v43, 0xffff0000, v89
v_pk_mul_f32 v[128:129], v[128:129], v[184:185]
v_pk_mul_f32 v[130:131], v[130:131], v[186:187]
v_pk_mul_f32 v[132:133], v[132:133], v[188:189]
v_pk_mul_f32 v[134:135], v[134:135], v[190:191]
v_pk_mul_f32 v[136:137], v[136:137], v[192:193]
v_pk_mul_f32 v[138:139], v[138:139], v[194:195]
v_pk_mul_f32 v[140:141], v[140:141], v[196:197]
v_pk_mul_f32 v[142:143], v[142:143], v[198:199]
v_pk_mul_f32 v[144:145], v[144:145], v[200:201]
v_pk_mul_f32 v[146:147], v[146:147], v[202:203]
Lschk0_fused_block:
s_waitcnt vmcnt(0)
v_max_u32 v249, v96, v97
v_max3_u32 v249, v249, v98, v99
v_max3_u32 v249, v249, v100, v101
v_max3_u32 v249, v249, v102, v103
v_max3_u32 v249, v249, v104, v105
v_cmp_eq_u32 vcc, -1, v249
s_sub_u32 s99, s99, 1
s_cbranch_scc1 Lsok0_fused_block
s_nop 2
s_cbranch_vccnz Lsre0_fused_block
Lsok0_fused_block:
v_lshlrev_b32 v250, 16, v96
v_and_b32 v251, 0xffff0000, v96
v_pk_add_f32 v[0:1], v[0:1], v[250:251]
v_lshlrev_b32 v250, 16, v97
v_and_b32 v251, 0xffff0000, v97
v_pk_add_f32 v[2:3], v[2:3], v[250:251]
v_lshlrev_b32 v250, 16, v98
v_and_b32 v251, 0xffff0000, v98
v_pk_add_f32 v[4:5], v[4:5], v[250:251]
v_lshlrev_b32 v250, 16, v99
v_and_b32 v251, 0xffff0000, v99
v_pk_add_f32 v[6:7], v[6:7], v[250:251]
v_lshlrev_b32 v250, 16, v100
v_and_b32 v251, 0xffff0000, v100
v_pk_add_f32 v[8:9], v[8:9], v[250:251]
v_lshlrev_b32 v250, 16, v101
v_and_b32 v251, 0xffff0000, v101
v_pk_add_f32 v[10:11], v[10:11], v[250:251]
v_lshlrev_b32 v250, 16, v102
v_and_b32 v251, 0xffff0000, v102
v_pk_add_f32 v[12:13], v[12:13], v[250:251]
v_lshlrev_b32 v250, 16, v103
v_and_b32 v251, 0xffff0000, v103
v_pk_add_f32 v[14:15], v[14:15], v[250:251]
v_lshlrev_b32 v250, 16, v104
v_and_b32 v251, 0xffff0000, v104
v_pk_add_f32 v[16:17], v[16:17], v[250:251]
v_lshlrev_b32 v250, 16, v105
v_and_b32 v251, 0xffff0000, v105
v_pk_add_f32 v[18:19], v[18:19], v[250:251]
s_mov_b32 s99, 0x20000
Lschk1_fused_block:
s_waitcnt vmcnt(0)
v_max_u32 v249, v106, v107
v_max3_u32 v249, v249, v108, v109
v_max3_u32 v249, v249, v110, v111
v_max3_u32 v249, v249, v112, v113
v_max3_u32 v249, v249, v114, v115
v_cmp_eq_u32 vcc, -1, v249
s_sub_u32 s99, s99, 1
s_cbranch_scc1 Lsok1_fused_block
s_nop 2
s_cbranch_vccnz Lsre1_fused_block
Lsok1_fused_block:
v_lshlrev_b32 v250, 16, v106
v_and_b32 v251, 0xffff0000, v106
v_pk_add_f32 v[0:1], v[0:1], v[250:251]
v_lshlrev_b32 v250, 16, v107
v_and_b32 v251, 0xffff0000, v107
v_pk_add_f32 v[2:3], v[2:3], v[250:251]
v_lshlrev_b32 v250, 16, v108
v_and_b32 v251, 0xffff0000, v108
v_pk_add_f32 v[4:5], v[4:5], v[250:251]
v_lshlrev_b32 v250, 16, v109
v_and_b32 v251, 0xffff0000, v109
v_pk_add_f32 v[6:7], v[6:7], v[250:251]
v_lshlrev_b32 v250, 16, v110
v_and_b32 v251, 0xffff0000, v110
v_pk_add_f32 v[8:9], v[8:9], v[250:251]
v_lshlrev_b32 v250, 16, v111
v_and_b32 v251, 0xffff0000, v111
v_pk_add_f32 v[10:11], v[10:11], v[250:251]
v_lshlrev_b32 v250, 16, v112
v_and_b32 v251, 0xffff0000, v112
v_pk_add_f32 v[12:13], v[12:13], v[250:251]
v_lshlrev_b32 v250, 16, v113
v_and_b32 v251, 0xffff0000, v113
v_pk_add_f32 v[14:15], v[14:15], v[250:251]
v_lshlrev_b32 v250, 16, v114
v_and_b32 v251, 0xffff0000, v114
v_pk_add_f32 v[16:17], v[16:17], v[250:251]
v_lshlrev_b32 v250, 16, v115
v_and_b32 v251, 0xffff0000, v115
v_pk_add_f32 v[18:19], v[18:19], v[250:251]
s_mov_b32 s99, 0x20000
Lschk2_fused_block:
s_waitcnt vmcnt(0)
v_max_u32 v249, v116, v117
v_max3_u32 v249, v249, v118, v119
v_max3_u32 v249, v249, v120, v121
v_max3_u32 v249, v249, v122, v123
v_max3_u32 v249, v249, v124, v125
v_cmp_eq_u32 vcc, -1, v249
s_sub_u32 s99, s99, 1
s_cbranch_scc1 Lsok2_fused_block
s_nop 2
s_cbranch_vccnz Lsre2_fused_block
Lsok2_fused_block:
v_lshlrev_b32 v250, 16, v116
v_and_b32 v251, 0xffff0000, v116
v_pk_add_f32 v[0:1], v[0:1], v[250:251]
v_lshlrev_b32 v250, 16, v117
v_and_b32 v251, 0xffff0000, v117
v_pk_add_f32 v[2:3], v[2:3], v[250:251]
v_lshlrev_b32 v250, 16, v118
v_and_b32 v251, 0xffff0000, v118
v_pk_add_f32 v[4:5], v[4:5], v[250:251]
v_lshlrev_b32 v250, 16, v119
v_and_b32 v251, 0xffff0000, v119
v_pk_add_f32 v[6:7], v[6:7], v[250:251]
v_lshlrev_b32 v250, 16, v120
v_and_b32 v251, 0xffff0000, v120
v_pk_add_f32 v[8:9], v[8:9], v[250:251]
v_lshlrev_b32 v250, 16, v121
v_and_b32 v251, 0xffff0000, v121
v_pk_add_f32 v[10:11], v[10:11], v[250:251]
v_lshlrev_b32 v250, 16, v122
v_and_b32 v251, 0xffff0000, v122
v_pk_add_f32 v[12:13], v[12:13], v[250:251]
v_lshlrev_b32 v250, 16, v123
v_and_b32 v251, 0xffff0000, v123
v_pk_add_f32 v[14:15], v[14:15], v[250:251]
v_lshlrev_b32 v250, 16, v124
v_and_b32 v251, 0xffff0000, v124
v_pk_add_f32 v[16:17], v[16:17], v[250:251]
v_lshlrev_b32 v250, 16, v125
v_and_b32 v251, 0xffff0000, v125
v_pk_add_f32 v[18:19], v[18:19], v[250:251]
v_pk_fma_f32 v[0:1], v[0:1], v[128:129], v[24:25]
v_pk_fma_f32 v[2:3], v[2:3], v[130:131], v[26:27]
v_pk_fma_f32 v[4:5], v[4:5], v[132:133], v[28:29]
v_pk_fma_f32 v[6:7], v[6:7], v[134:135], v[30:31]
v_pk_fma_f32 v[8:9], v[8:9], v[136:137], v[32:33]
v_pk_fma_f32 v[10:11], v[10:11], v[138:139], v[34:35]
v_pk_fma_f32 v[12:13], v[12:13], v[140:141], v[36:37]
v_pk_fma_f32 v[14:15], v[14:15], v[142:143], v[38:39]
v_pk_fma_f32 v[16:17], v[16:17], v[144:145], v[40:41]
v_pk_fma_f32 v[18:19], v[18:19], v[146:147], v[42:43]
CV 48,0,1
CV 49,2,3
CV 52,4,5
CV 53,6,7
CV 50,8,9
CV 51,10,11
CV 54,12,13
CV 55,14,15
CV 56,16,17
CV 57,18,19
v_permlane16_swap_b32 v48, v50
v_permlane16_swap_b32 v49, v51
v_permlane16_swap_b32 v52, v54
v_permlane16_swap_b32 v53, v55
buffer_store_dwordx4 v[48:51], v77, s[68:71], s25 offen
buffer_store_dwordx4 v[52:55], v77, s[68:71], s32 offen
buffer_store_dwordx2 v[56:57], v76, s[68:71], s31 offen
s_endpgm
Lsre0_fused_block:
buffer_load_dwordx4 v[96:99], v234, s[72:75], s20 offen offset:0 sc1
buffer_load_dwordx4 v[100:103], v234, s[72:75], s20 offen offset:1024 sc1
buffer_load_dwordx2 v[104:105], v74, s[72:75], s20 offen offset:2048 sc1
s_branch Lschk0_fused_block
Lsre1_fused_block:
buffer_load_dwordx4 v[106:109], v234, s[72:75], s21 offen offset:0 sc1
buffer_load_dwordx4 v[110:113], v234, s[72:75], s21 offen offset:1024 sc1
buffer_load_dwordx2 v[114:115], v74, s[72:75], s21 offen offset:2048 sc1
s_branch Lschk1_fused_block
Lsre2_fused_block:
buffer_load_dwordx4 v[116:119], v234, s[72:75], s22 offen offset:0 sc1
buffer_load_dwordx4 v[120:123], v234, s[72:75], s22 offen offset:1024 sc1
buffer_load_dwordx2 v[124:125], v74, s[72:75], s22 offen offset:2048 sc1
s_branch Lschk2_fused_block
Lstub9_fused_block:
s_cbranch_execz Ltourend_fused_block
Lhook_fused_block:
v_add_u32 v246, 16384, v246
v_add_u32 v247, 2048, v247
s_mul_i32 s95, s27, 5120
v_min_u32 v252, 19, v255
v_lshlrev_b32 v252, 7, v252
buffer_load_dword v253, v252, s[64:67], s95 offen offset:0
s_and_b32 s39, s76, 7
s_lshl_b32 s39, s39, 3
s_add_u32 s39, s39, s77
s_lshl_b32 s95, s19, 6
s_add_u32 s39, s39, s95
s_lshl_b32 s95, s94, 8
s_add_u32 s39, s39, s95
s_cmp_lt_u32 s39, 68
s_cbranch_scc0 Lhy0_fused_block
s_lshr_b32 s96, s76, 3
s_and_b32 s95, s39, 31
s_lshl_b32 s95, s95, 5
s_lshl_b32 s97, s96, 10
s_lshl_b32 s96, s96, 7
s_add_u32 s96, s96, 4096
s_cmp_lt_u32 s39, 64
s_cselect_b32 s96, s97, s96
s_add_u32 s95, s95, s96
s_bitcmp1_b32 s39, 5
s_cselect_b32 s12, s56, s64
s_cselect_b32 s13, s57, s65
s_cselect_b32 s14, s58, s66
s_cselect_b32 s38, 4608, 0
s_mov_b32 s15, 0x20000
s_add_u32 s38, s38, s95
s_mul_i32 s95, s95, 1152
s_add_u32 s95, s95, 1024
v_lshrrev_b32 v251, 1, v255
v_mul_u32_u24 v251, 1152, v251
v_and_b32 v252, 1, v255
v_lshl_add_u32 v249, v252, 4, v251
s_add_u32 m0, s84, 131072
s_nop 0
DM 249,12,95,0
s_add_u32 s95, s95, 32
s_add_u32 m0, s84, 132096
s_nop 0
DM 249,12,95,0
s_add_u32 s95, s95, 32
s_add_u32 m0, s84, 133120
s_nop 0
DM 249,12,95,0
s_add_u32 s95, s95, 32
s_add_u32 m0, s84, 134144
s_nop 0
DM 249,12,95,0
s_add_u32 s95, s95, 32
Lhy0_fused_block:
s_swappc_b64 s[8:9], s[10:11]
v_mov_b32 v247, 0x7ffffff0
s_mul_i32 s95, s27, 5120
v_min_u32 v252, 19, v255
v_lshlrev_b32 v252, 7, v252
buffer_load_dword v253, v252, s[64:67], s95 offen offset:2560
)"
R"(s_cmp_lt_u32 s39, 68
s_cbranch_scc0 Lhy1_fused_block
v_mov_b32 v247, v234
v_subrev_u32 v246, 16384, v246
s_lshl_b32 s95, s38, 5
s_add_u32 s20, s95, 39059456
s_add_u32 s21, s95, 39337984
s_add_u32 s22, s95, 39616512
s_add_u32 s23, s95, 39895040
Lhy1_fused_block:
s_swappc_b64 s[8:9], s[10:11]
v_mov_b32 v247, 0x7ffffff0
v_mov_b32 v242, -1
v_mov_b32 v243, -1
v_mov_b32 v244, -1
v_mov_b32 v245, -1
ds_write_b128 v246, v[242:245] offset:0
ds_write_b128 v246, v[242:245] offset:1024
ds_write_b128 v246, v[242:245] offset:2048
ds_write_b128 v246, v[242:245] offset:3072
s_mul_i32 s95, s27, 4608
v_min_u32 v252, 17, v255
v_lshlrev_b32 v252, 7, v252
buffer_load_dword v253, v252, s[56:59], s95 offen offset:0
Lhy2_fused_block:
s_swappc_b64 s[8:9], s[10:11]
s_mul_i32 s95, s27, 4608
v_min_u32 v252, 17, v255
v_lshlrev_b32 v252, 7, v252
buffer_load_dword v253, v252, s[56:59], s95 offen offset:2304
s_or_b32 s95, s19, s94
s_cbranch_scc1 Lhy3_fused_block
s_memrealtime s[96:97]
v_mov_b32 v250, s82
v_mov_b32 v251, s83
v_lshlrev_b32 v249, 3, v255
s_lshl_b32 s98, s18, 7
s_add_u32 s98, s98, 131072
s_waitcnt vmcnt(28) lgkmcnt(0)
s_mov_b64 exec, 0xff00
v_mov_b32 v250, s96
v_mov_b32 v251, s82
s_mov_b64 exec, 0xffff
buffer_store_dwordx2 v[250:251], v249, s[72:75], s98 offen sc1
s_mov_b64 exec, -1
Lhy3_fused_block:
s_swappc_b64 s[8:9], s[10:11]
s_lshl_b32 s98, s19, 13
s_add_u32 s98, s98, 131072
v_lshlrev_b32 v249, 7, v255
buffer_load_dwordx2 v[250:251], v249, s[72:75], s98 offen sc1
v_and_b32 v242, 7, v255
v_lshlrev_b32 v242, 7, v242
s_mov_b32 s95, 131072
buffer_load_dwordx2 v[252:253], v242, s[72:75], s95 offen offset:64 sc1
Lhy4_fused_block:
s_swappc_b64 s[8:9], s[10:11]
s_cmp_lg_u32 s94, 0
s_cbranch_scc1 Lhy5_fused_block
s_mov_b32 s99, 0x20000
s_waitcnt vmcnt(27)
Lfhchk_fused_block:
v_cmp_ne_u64 vcc, s[82:83], v[250:251]
v_cmp_ne_u32 s[96:97], s82, v253
s_sub_u32 s99, s99, 1
s_cbranch_scc1 Lfhok_fused_block
s_nop 1
s_or_b64 vcc, vcc, s[96:97]
s_cbranch_scc0 Lfhok_fused_block
s_lshl_b32 s98, s19, 13
s_add_u32 s98, s98, 131072
v_lshlrev_b32 v249, 7, v255
buffer_load_dwordx2 v[250:251], v249, s[72:75], s98 offen sc1
v_and_b32 v242, 7, v255
v_lshlrev_b32 v242, 7, v242
s_mov_b32 s95, 131072
buffer_load_dwordx2 v[252:253], v242, s[72:75], s95 offen offset:64 sc1
s_waitcnt vmcnt(0)
s_branch Lfhchk_fused_block
Lfhok_fused_block:
s_and_b32 s95, s18, 7
v_readlane_b32 s96, v252, 0
v_and_b32 v249, 7, v255
s_nop 0
v_subrev_u32 v252, s96, v252
v_lshl_add_u32 v252, v252, 3, v249
s_nop 0
v_readlane_b32 s96, v252, s95
s_nop 1
v_cmp_gt_i32 vcc, s96, v252
s_nop 1
s_and_b32 s95, vcc_lo, 0xff
s_bcnt1_i32_b32 s95, s95
Lrole_fused_block:
s_lshr_b32 s78, s95, 1
s_and_b32 s38, s95, 1
s_getpc_b64 s[6:7]
Lvb_fused_block:
s_mov_b32 s95, Lvar0_fused_block-Lvb_fused_block
s_mov_b32 s96, 0
s_cmp_eq_u32 s78, 1
s_cselect_b32 s95, Lvar1_fused_block-Lvb_fused_block, s95
s_cselect_b32 s96, 10, s96
s_cmp_eq_u32 s78, 2
s_cselect_b32 s95, Lvar2_fused_block-Lvb_fused_block, s95
s_cselect_b32 s96, 19, s96
s_cmp_eq_u32 s78, 3
s_cselect_b32 s95, Lvar3_fused_block-Lvb_fused_block, s95
s_cselect_b32 s96, 26, s96
s_add_u32 s6, s6, s95
s_addc_u32 s7, s7, 0
s_mul_i32 s30, s96, 147456
s_add_u32 s30, s30, s28
s_lshl_b32 s31, s96, 12
s_add_u32 s31, s31, s29
s_lshl_b32 s96, s38, 9
s_add_u32 s96, s96, s35
s_add_u32 s96, s96, s78
s_lshl_b32 s96, s96, 13
s_add_u32 s20, s96, 48562176
s_add_u32 s21, s96, 48563200
s_add_u32 s22, s96, 48564224
s_add_u32 s23, s96, 48565248
v_mov_b32 v247, v234
s_sub_u32 s95, Lend_fused_block-Lvb_fused_block, s95
s_lshl_b32 s96, s81, 8
v_and_b32 v252, 1, v255
v_lshlrev_b32 v252, 7, v252
v_add_u32 v252, s96, v252
v_min_u32 v252, s95, v252
global_load_dword v253, v252, s[6:7]
Lhy5_fused_block:
s_swappc_b64 s[8:9], s[10:11]
s_lshl_b32 s95, s19, 2
s_sub_u32 s95, 16, s95
s_sub_u32 s96, s6, s95
s_subb_u32 s97, s7, 0
s_mov_b64 exec, 0
s_setpc_b64 s[96:97]
Ltour2end_fused_block:
s_mov_b64 exec, -1
s_cmp_lg_u32 s27, 0
s_cbranch_scc1 Lhy6_fused_block
s_mov_b32 s97, 0
s_add_u32 s98, s97, 655360
v_mov_b32 v250, s82
v_mov_b32 v251, s83
v_lshlrev_b32 v249, 3, v255
s_mov_b64 exec, 0xffff
buffer_store_dwordx2 v[250:251], v249, s[72:75], s98 offen sc1
s_mov_b64 exec, -1
Lhy6_fused_block:
s_swappc_b64 s[8:9], s[10:11]
Lretry_fused_block:
s_mov_b32 s94, 1
s_waitcnt vmcnt(0) lgkmcnt(0)
s_barrier
s_nop 7
v_mov_b32 v160, 0xff800000
v_mov_b32 v161, 0xff800000
v_mov_b32 v162, 0xff800000
v_mov_b32 v163, 0xff800000
s_mov_b32 s87, s24
s_mov_b32 s93, 32
Lmax_fused_block:
s_add_u32 m0, s84, 0
s_nop 0
DM 228,44,87,0
DM 229,44,87,1024
DM 230,44,87,2048
DM 231,44,87,3072
s_waitcnt vmcnt(0)
s_barrier
ds_read_b128 a[64:67], v216 offset:0
ds_read_b128 a[68:71], v217 offset:0
ds_read_b128 a[72:75], v218 offset:0
ds_read_b128 a[76:79], v219 offset:0
ds_read_b128 a[80:83], v216 offset:4096
ds_read_b128 a[84:87], v217 offset:4096
ds_read_b128 a[88:91], v218 offset:4096
ds_read_b128 a[92:95], v219 offset:4096
ds_read_b128 a[96:99], v216 offset:8192
ds_read_b128 a[100:103], v217 offset:8192
ds_read_b128 a[104:107], v218 offset:8192
ds_read_b128 a[108:111], v219 offset:8192
ds_read_b128 a[112:115], v216 offset:12288
ds_read_b128 a[116:119], v217 offset:12288
ds_read_b128 a[120:123], v218 offset:12288
ds_read_b128 a[124:127], v219 offset:12288
WL 0
s_barrier
v_mfma_f32_16x16x32_bf16 v[128:131], a[64:67], a[0:3], 0
Mvaa 128,68,4
Mvaa 128,72,8
Mvaa 128,76,12
v_mfma_f32_16x16x32_bf16 v[132:135], a[92:95], a[12:15], 0
Mvaa 132,88,8
Mvaa 132,84,4
Mvaa 132,80,0
v_mfma_f32_16x16x32_bf16 v[136:139], a[96:99], a[0:3], 0
Mvaa 136,100,4
Mvaa 136,104,8
Mvaa 136,108,12
v_mfma_f32_16x16x32_bf16 v[140:143], a[124:127], a[12:15], 0
Mvaa 140,120,8
Mvaa 140,116,4
Mvaa 140,112,0
s_nop 7
v_max3_f32 v160, v160, v128, v129
v_max3_f32 v160, v160, v130, v131
v_max3_f32 v160, v160, v132, v133
v_max3_f32 v160, v160, v134, v135
v_max3_f32 v160, v160, v136, v137
v_max3_f32 v160, v160, v138, v139
v_max3_f32 v160, v160, v140, v141
v_max3_f32 v160, v160, v142, v143
v_mfma_f32_16x16x32_bf16 v[128:131], a[64:67], a[16:19], 0
Mvaa 128,68,20
Mvaa 128,72,24
Mvaa 128,76,28
v_mfma_f32_16x16x32_bf16 v[132:135], a[92:95], a[28:31], 0
Mvaa 132,88,24
Mvaa 132,84,20
Mvaa 132,80,16
v_mfma_f32_16x16x32_bf16 v[136:139], a[96:99], a[16:19], 0
Mvaa 136,100,20
Mvaa 136,104,24
Mvaa 136,108,28
v_mfma_f32_16x16x32_bf16 v[140:143], a[124:127], a[28:31], 0
Mvaa 140,120,24
Mvaa 140,116,20
Mvaa 140,112,16
s_nop 7
v_max3_f32 v161, v161, v128, v129
v_max3_f32 v161, v161, v130, v131
v_max3_f32 v161, v161, v132, v133
v_max3_f32 v161, v161, v134, v135
v_max3_f32 v161, v161, v136, v137
v_max3_f32 v161, v161, v138, v139
v_max3_f32 v161, v161, v140, v141
v_max3_f32 v161, v161, v142, v143
v_mfma_f32_16x16x32_bf16 v[128:131], a[64:67], a[32:35], 0
Mvaa 128,68,36
Mvaa 128,72,40
Mvaa 128,76,44
v_mfma_f32_16x16x32_bf16 v[132:135], a[92:95], a[44:47], 0
Mvaa 132,88,40
Mvaa 132,84,36
Mvaa 132,80,32
v_mfma_f32_16x16x32_bf16 v[136:139], a[96:99], a[32:35], 0
Mvaa 136,100,36
Mvaa 136,104,40
Mvaa 136,108,44
v_mfma_f32_16x16x32_bf16 v[140:143], a[124:127], a[44:47], 0
Mvaa 140,120,40
Mvaa 140,116,36
Mvaa 140,112,32
s_nop 7
v_max3_f32 v162, v162, v128, v129
v_max3_f32 v162, v162, v130, v131
v_max3_f32 v162, v162, v132, v133
v_max3_f32 v162, v162, v134, v135
v_max3_f32 v162, v162, v136, v137
v_max3_f32 v162, v162, v138, v139
v_max3_f32 v162, v162, v140, v141
v_max3_f32 v162, v162, v142, v143
v_mfma_f32_16x16x32_bf16 v[128:131], a[64:67], a[48:51], 0
Mvaa 128,68,52
Mvaa 128,72,56
Mvaa 128,76,60
v_mfma_f32_16x16x32_bf16 v[132:135], a[92:95], a[60:63], 0
Mvaa 132,88,56
Mvaa 132,84,52
Mvaa 132,80,48
v_mfma_f32_16x16x32_bf16 v[136:139], a[96:99], a[48:51], 0
Mvaa 136,100,52
Mvaa 136,104,56
Mvaa 136,108,60
v_mfma_f32_16x16x32_bf16 v[140:143], a[124:127], a[60:63], 0
Mvaa 140,120,56
Mvaa 140,116,52
Mvaa 140,112,48
s_nop 7
v_max3_f32 v163, v163, v128, v129
v_max3_f32 v163, v163, v130, v131
v_max3_f32 v163, v163, v132, v133
v_max3_f32 v163, v163, v134, v135
v_max3_f32 v163, v163, v136, v137
v_max3_f32 v163, v163, v138, v139
v_max3_f32 v163, v163, v140, v141
v_max3_f32 v163, v163, v142, v143
s_add_u32 s87, s87, 16384
s_sub_u32 s93, s93, 1
s_cmp_lg_u32 s93, 0
s_cbranch_scc1 Lmax_fused_block
v_mov_b32 v250, v160
s_nop 1
v_permlane32_swap_b32 v160, v250
v_max_f32 v160, v160, v250
v_mov_b32 v250, v160
s_nop 1
v_permlane16_swap_b32 v160, v250
v_max_f32 v160, v160, v250
v_sub_f32 v176, 0, v160
v_sub_f32 v177, 0, v160
v_sub_f32 v178, 0, v160
v_sub_f32 v179, 0, v160
v_mov_b32 v250, v161
s_nop 1
v_permlane32_swap_b32 v161, v250
v_max_f32 v161, v161, v250
v_mov_b32 v250, v161
s_nop 1
v_permlane16_swap_b32 v161, v250
v_max_f32 v161, v161, v250
v_sub_f32 v180, 0, v161
v_sub_f32 v181, 0, v161
v_sub_f32 v182, 0, v161
v_sub_f32 v183, 0, v161
v_mov_b32 v250, v162
s_nop 1
v_permlane32_swap_b32 v162, v250
v_max_f32 v162, v162, v250
v_mov_b32 v250, v162
s_nop 1
v_permlane16_swap_b32 v162, v250
v_max_f32 v162, v162, v250
v_sub_f32 v184, 0, v162
v_sub_f32 v185, 0, v162
v_sub_f32 v186, 0, v162
v_sub_f32 v187, 0, v162
v_mov_b32 v250, v163
s_nop 1
v_permlane32_swap_b32 v163, v250
v_max_f32 v163, v163, v250
v_mov_b32 v250, v163
s_nop 1
v_permlane16_swap_b32 v163, v250
v_max_f32 v163, v163, v250
v_sub_f32 v188, 0, v163
v_sub_f32 v189, 0, v163
v_sub_f32 v190, 0, v163
v_sub_f32 v191, 0, v163
v_mov_b32 v247, 0x7ffffff0
v_add_u32 v246, s84, v234
v_add_u32 v246, 131072, v246
s_add_u32 s87, s24, 0
s_add_u32 m0, s84, 16384
s_nop 0
DM 228,44,87,0
DM 229,44,87,1024
DM 230,44,87,2048
DM 231,44,87,3072
s_add_u32 m0, s85, 16384
s_nop 0
DM 232,48,87,0
DM 233,48,87,1024
DM 232,48,87,2048
DM 233,48,87,3072
s_add_u32 s87, s24, 16384
s_add_u32 m0, s84, 32768
s_nop 0
DM 228,44,87,0
DM 229,44,87,1024
DM 230,44,87,2048
DM 231,44,87,3072
s_add_u32 m0, s85, 32768
s_nop 0
DM 232,48,87,0
DM 233,48,87,1024
DM 232,48,87,2048
DM 233,48,87,3072
s_add_u32 s87, s24, 32768
s_add_u32 m0, s84, 49152
s_nop 0
DM 228,44,87,0
DM 229,44,87,1024
DM 230,44,87,2048
DM 231,44,87,3072
s_add_u32 m0, s85, 49152
s_nop 0
DM 232,48,87,0
DM 233,48,87,1024
DM 232,48,87,2048
DM 233,48,87,3072
s_add_u32 s87, s24, 49152
s_add_u32 m0, s84, 0
s_nop 0
DM 228,44,87,0
DM 229,44,87,1024
DM 230,44,87,2048
DM 231,44,87,3072
s_add_u32 m0, s85, 0
s_nop 0
DM 232,48,87,0
DM 233,48,87,1024
DM 232,48,87,2048
DM 233,48,87,3072
v_mov_b32 v212, 0
v_mov_b32 v213, 0
)"
R"(v_mov_b32 v214, 0
v_mov_b32 v215, 0
s_waitcnt vmcnt(0)
s_barrier
ds_read_b128 a[64:67], v216 offset:16384
ds_read_b128 a[68:71], v217 offset:16384
ds_read_b128 a[72:75], v218 offset:16384
ds_read_b128 a[76:79], v219 offset:16384
ds_read_b128 a[80:83], v216 offset:20480
ds_read_b128 a[84:87], v217 offset:20480
ds_read_b128 a[88:91], v218 offset:20480
ds_read_b128 a[92:95], v219 offset:20480
ds_read_b128 a[96:99], v216 offset:24576
ds_read_b128 a[100:103], v217 offset:24576
ds_read_b128 a[104:107], v218 offset:24576
ds_read_b128 a[108:111], v219 offset:24576
ds_read_b128 a[112:115], v216 offset:28672
ds_read_b128 a[116:119], v217 offset:28672
ds_read_b128 a[120:123], v218 offset:28672
ds_read_b128 a[124:127], v219 offset:28672
v_mfma_f32_32x32x16_bf16 v[0:15], v[212:215], v[212:215], 0
v_mfma_f32_32x32x16_bf16 v[16:31], v[212:215], v[212:215], 0
v_mfma_f32_32x32x16_bf16 v[32:47], v[212:215], v[212:215], 0
v_mfma_f32_32x32x16_bf16 v[48:63], v[212:215], v[212:215], 0
v_mfma_f32_32x32x16_bf16 v[64:79], v[212:215], v[212:215], 0
v_mfma_f32_32x32x16_bf16 v[80:95], v[212:215], v[212:215], 0
v_mfma_f32_32x32x16_bf16 v[96:111], v[212:215], v[212:215], 0
v_mfma_f32_32x32x16_bf16 v[112:127], v[212:215], v[212:215], 0
v_mfma_f32_32x32x16_bf16 v[192:207], v[212:215], v[212:215], 0
WL 0
TR 128,220,16384
TR 130,220,20480
TR 132,220,24576
TR 134,220,28672
TR 136,221,16384
TR 138,221,20480
TR 140,221,24576
TR 142,221,28672
TR 144,222,16384
TR 146,222,20480
TR 148,222,24576
TR 150,222,28672
TR 152,223,16384
TR 154,223,20480
TR 156,223,24576
TR 158,223,28672
WL 0
s_branch Latt_fused_block
Lcommon_fused_block:
.p2align 6
s_branch Lv0stub0_fused_block
s_branch Lv0stub1_fused_block
s_branch Lv0stub2_fused_block
s_branch Lv0stub3_fused_block
Lvar0_fused_block:
v_mfma_f32_16x16x32_bf16 v[128:131], a[64:67], a[32:35], v[184:187]
v_exp_f32 v212, v144
Mvaa 128,68,36
s_add_u32 s90, s34, 0
GL 128,234,72,90,0
Mvaa 128,72,40
s_add_u32 s20, s25, 16777216
v_exp_f32 v213, v145
Mvaa 128,76,44
GL 132,234,72,90,1024
CV 168,212,213
v_mfma_f32_16x16x32_bf16 v[132:135], a[92:95], a[44:47], v[184:187]
v_exp_f32 v214, v146
Mvaa 132,88,40
GL 136,234,72,90,2048
Mvaa 132,84,36
s_add_u32 s21, s25, 16908288
v_exp_f32 v215, v147
Mvaa 132,80,32
GL 140,234,72,90,3072
CV 169,214,215
v_mfma_f32_16x16x32_bf16 v[136:139], a[96:99], a[32:35], v[184:187]
s_add_u32 s90, s34, 131072
GL 144,234,72,90,0
v_exp_f32 v212, v148
Mvaa 136,100,36
Mvaa 136,104,40
s_add_u32 s22, s26, 16777216
GL 148,234,72,90,1024
v_exp_f32 v213, v149
Mvaa 136,108,44
CV 170,212,213
v_mfma_f32_16x16x32_bf16 v[140:143], a[124:127], a[44:47], v[184:187]
GL 152,234,72,90,2048
v_exp_f32 v214, v150
Mvaa 140,120,40
Mvaa 140,116,36
s_add_u32 s23, s26, 16908288
GL 156,234,72,90,3072
v_exp_f32 v215, v151
Mvaa 140,112,32
s_add_u32 s90, s34, 4096
GL 160,234,72,90,0
CV 171,214,215
Mvav 0,192,160
v_exp_f32 v212, v152
Mvav 0,196,164
GL 164,234,72,90,1024
Mvav 4,200,160
v_exp_f32 v213, v153
Mvav 4,204,164
GL 168,234,72,90,2048
CV 172,212,213
Mvav 8,208,160
v_exp_f32 v214, v154
Mvav 8,212,164
GL 172,234,72,90,3072
Mvav 12,216,160
s_add_u32 s90, s34, 135168
GL 176,234,72,90,0
v_exp_f32 v215, v155
Mvav 12,220,164
CV 173,214,215
Mvav 16,224,160
GL 180,234,72,90,1024
v_exp_f32 v212, v156
Mvav 16,228,164
Mvav 20,232,160
GL 184,234,72,90,2048
v_exp_f32 v213, v157
Mvav 20,236,164
CV 174,212,213
Mvav 24,240,160
GL 188,234,72,90,3072
v_exp_f32 v214, v158
Mvav 24,244,164
Mvav 28,248,160
v_exp_f32 v215, v159
Mvav 28,252,164
CV 175,214,215
v_smfmac_f32_16x16x64_bf16 v[192:195], v[208:211], v[160:167], v239
v_mfma_f32_16x16x32_bf16 v[144:147], a[64:67], a[48:51], v[188:191]
v_exp_f32 v212, v128
Mvaa 144,68,52
Mvaa 144,72,56
v_exp_f32 v213, v129
Mvaa 144,76,60
CV 160,212,213
v_mfma_f32_16x16x32_bf16 v[148:151], a[92:95], a[60:63], v[188:191]
v_exp_f32 v214, v130
Mvaa 148,88,56
Mvaa 148,84,52
v_exp_f32 v215, v131
v_add_f32 v192, v192, v193
Mvaa 148,80,48
CV 161,214,215
v_rcp_f32 v193, v192
v_mfma_f32_16x16x32_bf16 v[152:155], a[96:99], a[48:51], v[188:191]
v_exp_f32 v212, v132
v_mov_b32 v194, v194
Mvaa 152,100,52
v_mul_f32 v0, v0, v193
v_mul_f32 v1, v1, v193
v_mul_f32 v2, v2, v193
Mvaa 152,104,56
v_exp_f32 v213, v133
v_mul_f32 v3, v3, v193
Mvaa 152,108,60
CV 162,212,213
v_mul_f32 v4, v4, v193
v_mul_f32 v5, v5, v193
v_mfma_f32_16x16x32_bf16 v[156:159], a[124:127], a[60:63], v[188:191]
v_exp_f32 v214, v134
v_mul_f32 v6, v6, v193
Mvaa 156,120,56
v_mul_f32 v7, v7, v193
CV 0,0,1
CV 1,2,3
Mvaa 156,116,52
v_exp_f32 v215, v135
CV 2,4,5
Mvaa 156,112,48
CV 163,214,215
CV 3,6,7
v_mul_f32 v8, v8, v193
Mvav 32,192,168
buffer_store_dwordx4 v[0:3], v234, s[72:75], s20 offen offset:0 sc1
v_exp_f32 v212, v136
v_mul_f32 v9, v9, v193
Mvav 32,196,172
v_mul_f32 v10, v10, v193
v_mul_f32 v11, v11, v193
v_mul_f32 v12, v12, v193
Mvav 36,200,168
v_exp_f32 v213, v137
v_mul_f32 v13, v13, v193
Mvav 36,204,172
CV 164,212,213
v_mul_f32 v14, v14, v193
v_mul_f32 v15, v15, v193
Mvav 40,208,168
v_exp_f32 v214, v138
CV 8,8,9
Mvav 40,212,172
CV 9,10,11
CV 10,12,13
CV 11,14,15
Mvav 44,216,168
buffer_store_dwordx4 v[8:11], v234, s[72:75], s20 offen offset:1024 sc1
v_exp_f32 v215, v139
v_mul_f32 v16, v16, v193
Mvav 44,220,172
CV 165,214,215
v_mul_f32 v17, v17, v193
v_mul_f32 v18, v18, v193
Mvav 48,224,168
v_exp_f32 v212, v140
v_mul_f32 v19, v19, v193
Mvav 48,228,172
v_mul_f32 v20, v20, v193
v_mul_f32 v21, v21, v193
v_mul_f32 v22, v22, v193
Mvav 52,232,168
v_exp_f32 v213, v141
v_mul_f32 v23, v23, v193
Mvav 52,236,172
CV 166,212,213
CV 16,16,17
CV 17,18,19
Mvav 56,240,168
v_exp_f32 v214, v142
CV 18,20,21
Mvav 56,244,172
CV 19,22,23
v_mul_f32 v24, v24, v193
v_mul_f32 v25, v25, v193
Mvav 60,248,168
buffer_store_dwordx4 v[16:19], v234, s[72:75], s20 offen offset:2048 sc1
v_exp_f32 v215, v143
v_mul_f32 v26, v26, v193
Mvav 60,252,172
CV 167,214,215
v_mul_f32 v27, v27, v193
v_mul_f32 v28, v28, v193
v_smfmac_f32_16x16x64_bf16 v[196:199], v[208:211], v[168:175], v239
v_mul_f32 v29, v29, v193
v_mul_f32 v30, v30, v193
v_mul_f32 v31, v31, v193
Mvav 64,192,160
v_exp_f32 v212, v144
CV 24,24,25
Mvav 64,196,164
v_exp_f32 v213, v145
CV 25,26,27
Mvav 68,200,160
v_exp_f32 v214, v146
CV 168,212,213
Mvav 68,204,164
v_exp_f32 v215, v147
CV 26,28,29
Mvav 72,208,160
v_exp_f32 v212, v148
CV 169,214,215
Mvav 72,212,164
v_exp_f32 v213, v149
CV 27,30,31
Mvav 76,216,160
buffer_store_dwordx4 v[24:27], v234, s[72:75], s20 offen offset:3072 sc1
v_exp_f32 v214, v150
CV 170,212,213
Mvav 76,220,164
s_add_u32 s90, s34, 8192
GL 96,234,72,90,0
v_exp_f32 v215, v151
v_add_f32 v196, v196, v197
Mvav 80,224,160
v_exp_f32 v212, v152
CV 171,214,215
Mvav 80,228,164
GL 100,234,72,90,1024
v_exp_f32 v213, v153
Mvav 84,232,160
v_exp_f32 v214, v154
CV 172,212,213
Mvav 84,236,164
GL 104,234,72,90,2048
v_exp_f32 v215, v155
Mvav 88,240,160
v_exp_f32 v212, v156
CV 173,214,215
Mvav 88,244,164
GL 108,234,72,90,3072
v_exp_f32 v213, v157
Mvav 92,248,160
s_add_u32 s90, s34, 139264
GL 112,234,72,90,0
v_exp_f32 v214, v158
CV 174,212,213
Mvav 92,252,164
v_exp_f32 v215, v159
v_smfmac_f32_16x16x64_bf16 v[200:203], v[208:211], v[160:167], v239
GL 116,234,72,90,1024
CV 175,214,215
s_nop 0
Mvav 96,192,168
v_rcp_f32 v197, v196
v_add_f32 v192, v192, v196
Mvav 96,196,172
GL 120,234,72,90,2048
v_mul_f32 v32, v32, v197
v_mul_f32 v33, v33, v197
v_mul_f32 v34, v34, v197
Mvav 100,200,168
v_mul_f32 v35, v35, v197
v_mul_f32 v36, v36, v197
v_mul_f32 v37, v37, v197
Mvav 100,204,172
GL 124,234,72,90,3072
v_mul_f32 v38, v38, v197
v_mul_f32 v39, v39, v197
CV 32,32,33
Mvav 104,208,168
CV 33,34,35
CV 34,36,37
CV 35,38,39
Mvav 104,212,172
buffer_store_dwordx4 v[32:35], v234, s[72:75], s21 offen offset:0 sc1
TR 64,241,16384
v_mul_f32 v40, v40, v197
v_mul_f32 v41, v41, v197
Mvav 108,216,168
TR 66,241,20480
v_mul_f32 v42, v42, v197
v_mul_f32 v43, v43, v197
Mvav 108,220,172
TR 68,241,24576
v_mul_f32 v44, v44, v197
v_mul_f32 v45, v45, v197
Mvav 112,224,168
TR 70,241,28672
v_mul_f32 v46, v46, v197
v_mul_f32 v47, v47, v197
Mvav 112,228,172
TR 72,242,16384
CV 40,40,41
CV 41,42,43
Mvav 116,232,168
TR 74,242,20480
CV 42,44,45
CV 43,46,47
Mvav 116,236,172
TR 76,242,24576
v_mul_f32 v48, v48, v197
v_mul_f32 v49, v49, v197
Mvav 120,240,168
buffer_store_dwordx4 v[40:43], v234, s[72:75], s21 offen offset:1024 sc1
TR 78,242,28672
v_mul_f32 v50, v50, v197
v_mul_f32 v51, v51, v197
Mvav 120,244,172
TR 80,243,16384
v_mul_f32 v52, v52, v197
v_mul_f32 v53, v53, v197
Mvav 124,248,168
TR 82,243,20480
v_mul_f32 v54, v54, v197
v_mul_f32 v55, v55, v197
Mvav 124,252,172
TR 84,243,24576
CV 48,48,49
CV 49,50,51
v_smfmac_f32_16x16x64_bf16 v[204:207], v[208:211], v[168:175], v239
s_waitcnt vmcnt(22) lgkmcnt(7)
TR 86,243,28672
CV 50,52,53
v_mfma_f32_16x16x32_bf16 v[128:131], a[64:67], a[128:131], 0
TR 88,244,16384
CV 51,54,55
v_mul_f32 v56, v56, v197
Mvaa 128,68,132
TR 90,244,20480
v_mul_f32 v57, v57, v197
v_mul_f32 v58, v58, v197
v_mfma_f32_16x16x32_bf16 v[132:135], a[64:67], a[144:147], 0
TR 92,244,24576
v_mul_f32 v59, v59, v197
v_mul_f32 v60, v60, v197
Mvaa 132,68,148
WL 7
TR 94,244,28672
v_mul_f32 v61, v61, v197
v_mfma_f32_16x16x32_bf16 v[140:143], a[72:75], a[144:147], 0
TR 64,245,16384
v_mul_f32 v62, v62, v197
v_mul_f32 v63, v63, v197
Mvaa 140,76,148
TR 66,245,20480
CV 56,56,57
CV 57,58,59
v_mfma_f32_16x16x32_bf16 v[136:139], a[72:75], a[128:131], 0
buffer_store_dwordx4 v[48:51], v234, s[72:75], s21 offen offset:2048 sc1
TR 68,245,24576
CV 58,60,61
CV 59,62,63
Mvaa 136,76,132
)"
R"(WL 7
TR 70,245,28672
v_add_f32 v200, v200, v201
v_mfma_f32_16x16x32_bf16 v[152:155], a[80:83], a[128:131], 0
TR 72,246,16384
v_rcp_f32 v201, v200
Mvaa 152,84,132
TR 74,246,20480
v_add_f32 v192, v192, v200
v_mul_f32 v64, v64, v201
v_mfma_f32_16x16x32_bf16 v[156:159], a[80:83], a[144:147], 0
TR 76,246,24576
v_mul_f32 v65, v65, v201
v_mul_f32 v66, v66, v201
Mvaa 156,84,148
WL 7
TR 78,246,28672
v_mul_f32 v67, v67, v201
v_mfma_f32_16x16x32_bf16 v[164:167], a[88:91], a[144:147], 0
TR 80,247,16384
v_mul_f32 v68, v68, v201
v_mul_f32 v69, v69, v201
Mvaa 164,92,148
TR 82,247,20480
v_mul_f32 v70, v70, v201
v_mul_f32 v71, v71, v201
v_mfma_f32_16x16x32_bf16 v[160:163], a[88:91], a[128:131], 0
TR 84,247,24576
CV 64,64,65
CV 65,66,67
Mvaa 160,92,132
WL 7
TR 86,247,28672
CV 66,68,69
v_mfma_f32_16x16x32_bf16 v[168:171], a[64:67], a[128:131], 0
TR 88,248,16384
CV 67,70,71
v_mul_f32 v72, v72, v201
Mvaa 168,68,132
buffer_store_dwordx4 v[56:59], v234, s[72:75], s21 offen offset:3072 sc1
TR 90,248,20480
v_mul_f32 v73, v73, v201
v_mul_f32 v74, v74, v201
v_mfma_f32_16x16x32_bf16 v[172:175], a[64:67], a[144:147], 0
TR 92,248,24576
v_mul_f32 v75, v75, v201
v_mul_f32 v76, v76, v201
Mvaa 172,68,148
WL 7
TR 94,248,28672
v_mul_f32 v77, v77, v201
v_mfma_f32_16x16x32_bf16 v[180:183], a[72:75], a[144:147], 0
TR 64,240,4096
v_mul_f32 v78, v78, v201
v_mul_f32 v79, v79, v201
Mvaa 180,76,148
TR 66,240,4608
CV 72,72,73
CV 73,74,75
v_mfma_f32_16x16x32_bf16 v[176:179], a[72:75], a[128:131], 0
TR 68,240,5120
CV 74,76,77
CV 75,78,79
Mvaa 176,76,132
WL 7
TR 70,240,5632
v_mul_f32 v80, v80, v201
v_mfma_f32_16x16x32_bf16 v[184:187], a[80:83], a[128:131], 0
TR 72,220,16384
v_mul_f32 v81, v81, v201
v_mul_f32 v82, v82, v201
Mvaa 184,84,132
Lv0stub0_fused_block:
s_cbranch_execz Lv0stub4_fused_block
TR 74,220,20480
v_mul_f32 v83, v83, v201
v_mul_f32 v84, v84, v201
v_mfma_f32_16x16x32_bf16 v[188:191], a[80:83], a[144:147], 0
TR 76,220,24576
v_mul_f32 v85, v85, v201
v_mul_f32 v86, v86, v201
Mvaa 188,84,148
WL 7
TR 78,220,28672
v_mul_f32 v87, v87, v201
v_mfma_f32_16x16x32_bf16 v[212:215], a[88:91], a[144:147], 0
s_add_u32 m0, s84, 0
TR 80,221,16384
CV 80,80,81
CV 81,82,83
Mvaa 212,92,148
DM 235,64,87,0
TR 82,221,20480
CV 82,84,85
CV 83,86,87
v_mfma_f32_16x16x32_bf16 v[208:211], a[88:91], a[128:131], 0
TR 84,221,24576
v_mul_f32 v88, v88, v201
v_mul_f32 v89, v89, v201
Mvaa 208,92,132
WL 7
DM 236,64,87,1024
TR 86,221,28672
v_mul_f32 v90, v90, v201
v_mfma_f32_16x16x32_bf16 v[144:147], a[64:67], a[128:131], 0
TR 88,222,16384
v_mul_f32 v91, v91, v201
v_mul_f32 v92, v92, v201
Mvaa 144,68,132
TR 90,222,20480
v_mul_f32 v93, v93, v201
v_mul_f32 v94, v94, v201
v_mfma_f32_16x16x32_bf16 v[148:151], a[64:67], a[144:147], 0
TR 92,222,24576
v_mul_f32 v95, v95, v201
CV 88,88,89
Mvaa 148,68,148
WL 7
DM 237,64,87,2048
TR 94,222,28672
CV 89,90,91
Mvaa 132,72,152
TR 64,223,16384
CV 90,92,93
CV 91,94,95
Mvaa 132,76,156
TR 66,223,20480
v_add_f32 v204, v204, v205
Mvaa 128,72,136
DM 238,64,87,3072
TR 68,223,24576
v_rcp_f32 v205, v204
Mvaa 128,76,140
WL 7
TR 70,223,28672
v_add_f32 v192, v192, v204
Mvaa 136,80,136
TR 72,224,16384
v_cmp_nge_f32 vcc, 0x53800000, v192
s_lshl_b32 s96, s19, 2
s_add_u32 s96, s96, 147456
s_cmp_lg_u64 vcc, 0
s_cselect_b32 s95, 1, 0
v_mov_b32 v249, s96
v_mov_b32 v250, s95
ds_write_b32 v249, v250
v_mov_b32 v249, 147456
Mvaa 136,84,140
TR 74,224,20480
v_mul_f32 v96, v96, v205
v_mul_f32 v97, v97, v205
Mvaa 140,80,152
s_add_u32 m0, s85, 0
TR 76,224,24576
v_mul_f32 v98, v98, v205
v_mul_f32 v99, v99, v205
Mvaa 140,84,156
WL 8
DM 235,64,88,0
TR 78,224,28672
v_mul_f32 v100, v100, v205
Mvaa 156,88,152
TR 80,225,16384
v_mul_f32 v101, v101, v205
v_mul_f32 v102, v102, v205
Mvaa 156,92,156
DM 236,64,88,1024
TR 82,225,20480
v_mul_f32 v103, v103, v205
CV 96,96,97
Mvaa 152,88,136
TR 84,225,24576
CV 97,98,99
CV 98,100,101
Mvaa 152,92,140
WL 8
TR 86,225,28672
CV 99,102,103
Mvaa 160,64,136
TR 88,226,16384
v_mul_f32 v104, v104, v205
v_mul_f32 v105, v105, v205
Mvaa 160,68,140
DM 237,64,88,2048
TR 90,226,20480
v_mul_f32 v106, v106, v205
v_mul_f32 v107, v107, v205
Mvaa 164,64,152
TR 92,226,24576
v_mul_f32 v108, v108, v205
v_mul_f32 v109, v109, v205
Mvaa 164,68,156
WL 7
TR 94,226,28672
v_mul_f32 v110, v110, v205
Mvaa 172,72,152
DM 238,64,88,3072
TR 64,227,16384
v_mul_f32 v111, v111, v205
CV 104,104,105
Mvaa 172,76,156
TR 66,227,20480
CV 105,106,107
CV 106,108,109
Mvaa 168,72,136
TR 68,227,24576
CV 107,110,111
v_mul_f32 v112, v112, v205
Mvaa 168,76,140
WL 7
TR 70,227,28672
v_mul_f32 v113, v113, v205
Mvaa 176,80,136
s_add_u32 m0, s86, 0
TR 72,240,6144
v_mul_f32 v114, v114, v205
v_mul_f32 v115, v115, v205
Mvaa 176,84,140
buffer_load_dwordx4 v234, s[72:75], s89 offen offset:0 sc1 lds
TR 74,240,6656
v_mul_f32 v116, v116, v205
v_mul_f32 v117, v117, v205
Mvaa 180,80,152
TR 76,240,7168
v_mul_f32 v118, v118, v205
v_mul_f32 v119, v119, v205
Mvaa 180,84,156
WL 7
s_add_u32 s90, s34, 12288
GL 192,234,72,90,0
TR 78,240,7680
CV 112,112,113
Mvaa 188,88,152
WL 0
s_barrier
TR 80,241,32768
CV 113,114,115
Mvaa 188,92,156
TR 82,241,36864
CV 114,116,117
CV 115,118,119
Mvaa 184,88,136
TR 84,241,40960
ds_read_b128 v[4:7], v249
v_mul_f32 v120, v120, v205
v_mul_f32 v121, v121, v205
Mvaa 184,92,140
GL 196,234,72,90,1024
TR 86,241,45056
v_mul_f32 v122, v122, v205
Mvaa 208,64,136
TR 88,242,32768
v_mul_f32 v123, v123, v205
v_mul_f32 v124, v124, v205
Mvaa 208,68,140
TR 90,242,36864
v_mul_f32 v125, v125, v205
v_mul_f32 v126, v126, v205
Mvaa 212,64,152
GL 200,234,72,90,2048
TR 92,242,40960
v_mul_f32 v127, v127, v205
CV 120,120,121
Mvaa 212,68,156
TR 94,242,45056
CV 121,122,123
Mvaa 148,72,152
TR 64,243,32768
CV 122,124,125
CV 123,126,127
Mvaa 148,76,156
TR 66,243,36864
Mvaa 144,72,136
GL 204,234,72,90,3072
TR 68,243,40960
Mvaa 144,76,140
s_waitcnt vmcnt(29) lgkmcnt(7)
TR 70,243,45056
Mvaa 128,80,160
TR 72,244,32768
Mvaa 128,84,164
s_add_u32 s90, s34, 143360
GL 208,234,72,90,0
TR 74,244,36864
Mvaa 132,80,176
TR 76,244,40960
v_or3_b32 v249, v4, v5, v6
v_or_b32 v249, v249, v7
s_nop 0
v_readfirstlane_b32 s97, v249
s_nop 3
s_andn2_b32 s97, s97, s94
s_cbranch_scc1 Lretry_fused_block
Mvaa 132,84,180
WL 7
TR 78,244,45056
Mvaa 140,88,176
TR 80,245,32768
Mvaa 140,92,180
GL 212,234,72,90,1024
TR 82,245,36864
Mvaa 136,88,160
TR 84,245,40960
Mvaa 136,92,164
WL 7
TR 86,245,45056
Mvaa 152,64,160
GL 216,234,72,90,2048
TR 88,246,32768
Mvaa 152,68,164
TR 90,246,36864
Mvaa 156,64,176
TR 92,246,40960
Mvaa 156,68,180
WL 7
TR 94,246,45056
Mvaa 164,72,176
GL 220,234,72,90,3072
TR 64,247,32768
Mvaa 164,76,180
TR 66,247,36864
Mvaa 160,72,160
TR 68,247,40960
Mvaa 160,76,164
WL 7
buffer_store_dwordx4 v[64:67], v234, s[72:75], s22 offen offset:0 sc1
TR 70,247,45056
Mvaa 168,80,160
TR 72,248,32768
Mvaa 168,84,164
TR 74,248,36864
Mvaa 172,80,176
TR 76,248,40960
Mvaa 172,84,180
WL 7
TR 78,248,45056
Mvaa 180,88,176
TR 80,240,8192
Mvaa 180,92,180
TR 82,240,8704
Mvaa 176,88,160
TR 84,240,9216
Mvaa 176,92,164
WL 7
TR 86,240,9728
Mvaa 184,64,160
TR 88,220,32768
Mvaa 184,68,164
TR 90,220,36864
Mvaa 188,64,176
buffer_store_dwordx4 v[72:75], v234, s[72:75], s22 offen offset:1024 sc1
TR 92,220,40960
Mvaa 188,68,180
WL 7
TR 94,220,45056
Mvaa 212,72,176
TR 64,221,32768
Mvaa 212,76,180
TR 66,221,36864
Mvaa 208,72,160
TR 68,221,40960
Mvaa 208,76,164
WL 7
TR 70,221,45056
Mvaa 144,80,160
TR 72,222,32768
Mvaa 144,84,164
TR 74,222,36864
Mvaa 148,80,176
TR 76,222,40960
Mvaa 148,84,180
WL 7
TR 78,222,45056
Mvaa 132,88,184
TR 80,223,32768
Mvaa 132,92,188
buffer_store_dwordx4 v[80:83], v234, s[72:75], s22 offen offset:2048 sc1
TR 82,223,36864
Mvaa 128,88,168
TR 84,223,40960
Mvaa 128,92,172
WL 7
TR 86,223,45056
Mvaa 136,64,168
TR 88,224,32768
Mvaa 136,68,172
TR 90,224,36864
Mvaa 140,64,184
TR 92,224,40960
Mvaa 140,68,188
WL 7
TR 94,224,45056
Mvaa 156,72,184
TR 64,225,32768
Mvaa 156,76,188
TR 66,225,36864
Mvaa 152,72,168
TR 68,225,40960
Mvaa 152,76,172
WL 7
)"
R"(TR 70,225,45056
Mvaa 160,80,168
buffer_store_dwordx4 v[88:91], v234, s[72:75], s22 offen offset:3072 sc1
TR 72,226,32768
Mvaa 160,84,172
TR 74,226,36864
Mvaa 164,80,184
TR 76,226,40960
Mvaa 164,84,188
WL 7
TR 78,226,45056
Mvaa 172,88,184
TR 80,227,32768
Mvaa 172,92,188
TR 82,227,36864
Mvaa 168,88,168
TR 84,227,40960
Mvaa 168,92,172
WL 7
TR 86,227,45056
Mvaa 176,64,168
TR 88,240,10240
Mvaa 176,68,172
TR 90,240,10752
Mvaa 180,64,184
TR 92,240,11264
Mvaa 180,68,188
WL 7
s_add_u32 s87, s28, 589824
s_add_u32 s88, s28, 663552
TR 94,240,11776
Mvaa 188,72,184
WL 0
s_barrier
s_add_u32 s89, s29, 16384
TR 64,241,49152
Mvaa 188,76,188
s_add_u32 m0, s84, 16384
TR 66,241,53248
Mvaa 184,72,168
DM 235,64,87,0
TR 68,241,57344
Mvaa 184,76,172
TR 70,241,61440
Mvaa 208,80,168
DM 236,64,87,1024
TR 72,242,49152
Mvaa 208,84,172
TR 74,242,53248
Mvaa 212,80,184
DM 237,64,87,2048
TR 76,242,57344
Mvaa 212,84,188
TR 78,242,61440
Mvaa 148,88,184
TR 80,243,49152
Mvaa 148,92,188
TR 82,243,53248
Mvaa 144,88,168
DM 238,64,87,3072
TR 84,243,57344
Mvaa 144,92,172
s_waitcnt vmcnt(29) lgkmcnt(7)
TR 86,243,61440
Mvaa 128,64,96
TR 88,244,49152
Mvaa 128,68,100
s_add_u32 m0, s85, 16384
TR 90,244,53248
Mvaa 132,64,112
DM 235,64,88,0
TR 92,244,57344
Mvaa 132,68,116
WL 7
TR 94,244,61440
Mvaa 140,72,112
TR 64,245,49152
Mvaa 140,76,116
DM 236,64,88,1024
TR 66,245,53248
Mvaa 136,72,96
TR 68,245,57344
Mvaa 136,76,100
WL 7
TR 70,245,61440
Mvaa 152,80,96
DM 237,64,88,2048
TR 72,246,49152
Mvaa 152,84,100
TR 74,246,53248
Mvaa 156,80,112
TR 76,246,57344
Mvaa 156,84,116
WL 7
TR 78,246,61440
Mvaa 164,88,112
DM 238,64,88,3072
TR 80,247,49152
Mvaa 164,92,116
TR 82,247,53248
Mvaa 160,88,96
TR 84,247,57344
Mvaa 160,92,100
WL 7
s_add_u32 m0, s86, 4096
TR 86,247,61440
Mvaa 168,64,96
buffer_load_dwordx4 v234, s[72:75], s89 offen offset:0 sc1 lds
TR 88,248,49152
Mvaa 168,68,100
TR 90,248,53248
Mvaa 172,64,112
TR 92,248,57344
Mvaa 172,68,116
WL 7
s_add_u32 s90, s34, 16384
GL 224,234,72,90,0
TR 94,248,61440
Mvaa 180,72,112
TR 64,240,12288
Mvaa 180,76,116
TR 66,240,12800
Mvaa 176,72,96
GL 228,234,72,90,1024
TR 68,240,13312
Mvaa 176,76,100
WL 7
TR 70,240,13824
Mvaa 184,80,96
TR 72,220,49152
Mvaa 184,84,100
TR 74,220,53248
Mvaa 188,80,112
GL 232,234,72,90,2048
TR 76,220,57344
Mvaa 188,84,116
WL 7
TR 78,220,61440
Mvaa 212,88,112
TR 80,221,49152
Mvaa 212,92,116
GL 236,234,72,90,3072
TR 82,221,53248
Mvaa 208,88,96
TR 84,221,57344
Mvaa 208,92,100
WL 7
TR 86,221,61440
Mvaa 144,64,96
TR 88,222,49152
Mvaa 144,68,100
s_add_u32 s90, s34, 147456
GL 240,234,72,90,0
TR 90,222,53248
Mvaa 148,64,112
TR 92,222,57344
Mvaa 148,68,116
WL 7
TR 94,222,61440
Mvaa 132,72,120
GL 244,234,72,90,1024
TR 64,223,49152
Mvaa 132,76,124
TR 66,223,53248
Mvaa 128,72,104
TR 68,223,57344
Mvaa 128,76,108
WL 7
TR 70,223,61440
Mvaa 136,80,104
GL 248,234,72,90,2048
TR 72,224,49152
Mvaa 136,84,108
TR 74,224,53248
Mvaa 140,80,120
TR 76,224,57344
Mvaa 140,84,124
WL 7
GL 252,234,72,90,3072
TR 78,224,61440
Mvaa 156,88,120
TR 80,225,49152
Mvaa 156,92,124
TR 82,225,53248
Mvaa 152,88,104
TR 84,225,57344
Mvaa 152,92,108
WL 7
buffer_store_dwordx4 v[96:99], v234, s[72:75], s23 offen offset:0 sc1
TR 86,225,61440
Mvaa 160,64,104
TR 88,226,49152
Mvaa 160,68,108
TR 90,226,53248
Mvaa 164,64,120
TR 92,226,57344
Mvaa 164,68,124
WL 7
TR 94,226,61440
Mvaa 172,72,120
TR 64,227,49152
Mvaa 172,76,124
TR 66,227,53248
Mvaa 168,72,104
TR 68,227,57344
Mvaa 168,76,108
WL 7
TR 70,227,61440
Mvaa 176,80,104
TR 72,240,14336
Mvaa 176,84,108
TR 74,240,14848
Mvaa 180,80,120
buffer_store_dwordx4 v[104:107], v234, s[72:75], s23 offen offset:1024 sc1
TR 76,240,15360
Mvaa 180,84,124
WL 7
TR 78,240,15872
Mvaa 188,88,120
s_waitcnt vmcnt(31) lgkmcnt(0)
s_barrier
TR 80,241,0
Mvaa 188,92,124
TR 82,241,4096
Mvaa 184,88,104
TR 84,241,8192
Mvaa 184,92,108
TR 86,241,12288
Mvaa 208,64,104
TR 88,242,0
Mvaa 208,68,108
TR 90,242,4096
Mvaa 212,64,120
TR 92,242,8192
Mvaa 212,68,124
TR 94,242,12288
Mvaa 148,72,120
TR 64,243,0
Mvaa 148,76,124
buffer_store_dwordx4 v[112:115], v234, s[72:75], s23 offen offset:2048 sc1
TR 66,243,4096
Mvaa 144,72,104
Lv0stub1_fused_block:
s_cbranch_execz Lv0stub5_fused_block
TR 68,243,8192
Mvaa 144,76,108
s_waitcnt vmcnt(24) lgkmcnt(7)
TR 70,243,12288
Mvaa 128,80,192
TR 72,244,0
Mvaa 128,84,196
TR 74,244,4096
Mvaa 132,80,208
TR 76,244,8192
Mvaa 132,84,212
WL 7
TR 78,244,12288
Mvaa 140,88,208
TR 80,245,0
Mvaa 140,92,212
TR 82,245,4096
Mvaa 136,88,192
TR 84,245,8192
Mvaa 136,92,196
WL 7
TR 86,245,12288
Mvaa 152,64,192
buffer_store_dwordx4 v[120:123], v234, s[72:75], s23 offen offset:3072 sc1
TR 88,246,0
Mvaa 152,68,196
TR 90,246,4096
Mvaa 156,64,208
TR 92,246,8192
Mvaa 156,68,212
WL 7
TR 94,246,12288
Mvaa 164,72,208
TR 64,247,0
Mvaa 164,76,212
TR 66,247,4096
Mvaa 160,72,192
TR 68,247,8192
Mvaa 160,76,196
WL 7
TR 70,247,12288
Mvaa 168,80,192
TR 72,248,0
Mvaa 168,84,196
TR 74,248,4096
Mvaa 172,80,208
TR 76,248,8192
Mvaa 172,84,212
WL 7
s_add_u32 s87, s28, 737280
s_add_u32 s88, s28, 811008
TR 78,248,12288
Mvaa 180,88,208
s_add_u32 s89, s29, 20480
s_add_u32 m0, s84, 32768
TR 80,240,0
Mvaa 180,92,212
DM 235,64,87,0
TR 82,240,512
Mvaa 176,88,192
TR 84,240,1024
Mvaa 176,92,196
WL 7
DM 236,64,87,1024
TR 86,240,1536
Mvaa 184,64,192
TR 88,220,0
Mvaa 184,68,196
TR 90,220,4096
Mvaa 188,64,208
DM 237,64,87,2048
TR 92,220,8192
Mvaa 188,68,212
WL 7
TR 94,220,12288
Mvaa 212,72,208
TR 64,221,0
Mvaa 212,76,212
DM 238,64,87,3072
TR 66,221,4096
Mvaa 208,72,192
TR 68,221,8192
Mvaa 208,76,196
WL 7
TR 70,221,12288
Mvaa 144,80,192
TR 72,222,0
Mvaa 144,84,196
s_add_u32 m0, s85, 32768
TR 74,222,4096
Mvaa 148,80,208
DM 235,64,88,0
TR 76,222,8192
Mvaa 148,84,212
WL 7
TR 78,222,12288
Mvaa 132,88,216
DM 236,64,88,1024
TR 80,223,0
Mvaa 132,92,220
TR 82,223,4096
Mvaa 128,88,200
TR 84,223,8192
Mvaa 128,92,204
WL 7
TR 86,223,12288
Mvaa 136,64,200
DM 237,64,88,2048
TR 88,224,0
Mvaa 136,68,204
TR 90,224,4096
Mvaa 140,64,216
TR 92,224,8192
Mvaa 140,68,220
WL 7
DM 238,64,88,3072
TR 94,224,12288
Mvaa 156,72,216
TR 64,225,0
Mvaa 156,76,220
TR 66,225,4096
Mvaa 152,72,200
TR 68,225,8192
Mvaa 152,76,204
WL 7
s_add_u32 m0, s86, 8192
TR 70,225,12288
Mvaa 160,80,200
buffer_load_dwordx4 v234, s[72:75], s89 offen offset:0 sc1 lds
TR 72,226,0
Mvaa 160,84,204
TR 74,226,4096
Mvaa 164,80,216
s_add_u32 s90, s34, 20480
GL 128,234,72,90,0
TR 76,226,8192
Mvaa 164,84,220
WL 7
TR 78,226,12288
Mvaa 172,88,216
TR 80,227,0
Mvaa 172,92,220
TR 82,227,4096
Mvaa 168,88,200
GL 132,234,72,90,1024
TR 84,227,8192
Mvaa 168,92,204
WL 7
TR 86,227,12288
Mvaa 176,64,200
TR 88,240,2048
Mvaa 176,68,204
GL 136,234,72,90,2048
TR 90,240,2560
Mvaa 180,64,216
TR 92,240,3072
)"
R"(Mvaa 180,68,220
WL 7
TR 94,240,3584
Mvaa 188,72,216
s_waitcnt vmcnt(24) lgkmcnt(0)
s_barrier
TR 64,241,16384
Mvaa 188,76,220
GL 140,234,72,90,3072
TR 66,241,20480
Mvaa 184,72,200
TR 68,241,24576
Mvaa 184,76,204
TR 70,241,28672
Mvaa 208,80,200
s_add_u32 s90, s34, 151552
GL 144,234,72,90,0
TR 72,242,16384
Mvaa 208,84,204
TR 74,242,20480
Mvaa 212,80,216
TR 76,242,24576
Mvaa 212,84,220
TR 78,242,28672
Mvaa 148,88,216
GL 148,234,72,90,1024
TR 80,243,16384
Mvaa 148,92,220
TR 82,243,20480
Mvaa 144,88,200
TR 84,243,24576
Mvaa 144,92,204
s_waitcnt vmcnt(19) lgkmcnt(7)
GL 152,234,72,90,2048
TR 86,243,28672
Mvaa 128,64,224
TR 88,244,16384
Mvaa 128,68,228
TR 90,244,20480
Mvaa 132,64,240
TR 92,244,24576
Mvaa 132,68,244
WL 7
GL 156,234,72,90,3072
TR 94,244,28672
Mvaa 140,72,240
TR 64,245,16384
Mvaa 140,76,244
TR 66,245,20480
Mvaa 136,72,224
s_waitcnt vmcnt(17)
s_add_u32 s98, s37, 262144
TR 68,245,24576
v_mov_b32 v250, s82
Mvaa 136,76,228
WL 7
TR 70,245,28672
v_mov_b32 v251, s83
Mvaa 152,80,224
TR 72,246,16384
v_lshlrev_b32 v249, 3, v255
s_mov_b64 exec, 0xffff
buffer_store_dwordx2 v[250:251], v249, s[72:75], s98 offen sc1
s_mov_b64 exec, -1
Mvaa 152,84,228
s_add_u32 s87, s28, 884736
s_add_u32 s88, s28, 958464
s_add_u32 s89, s29, 24576
TR 74,246,20480
Mvaa 156,80,240
s_add_u32 m0, s84, 49152
TR 76,246,24576
Mvaa 156,84,244
WL 7
DM 235,64,87,0
TR 78,246,28672
Mvaa 164,88,240
TR 80,247,16384
Mvaa 164,92,244
DM 236,64,87,1024
TR 82,247,20480
Mvaa 160,88,224
TR 84,247,24576
Mvaa 160,92,228
WL 7
DM 237,64,87,2048
TR 86,247,28672
Mvaa 168,64,224
TR 88,248,16384
Mvaa 168,68,228
DM 238,64,87,3072
TR 90,248,20480
Mvaa 172,64,240
TR 92,248,24576
Mvaa 172,68,244
WL 7
TR 94,248,28672
Mvaa 180,72,240
s_add_u32 m0, s85, 49152
TR 64,240,4096
Mvaa 180,76,244
DM 235,64,88,0
TR 66,240,4608
Mvaa 176,72,224
TR 68,240,5120
Mvaa 176,76,228
WL 7
TR 70,240,5632
Mvaa 184,80,224
DM 236,64,88,1024
TR 72,220,16384
Mvaa 184,84,228
TR 74,220,20480
Mvaa 188,80,240
TR 76,220,24576
Mvaa 188,84,244
WL 7
DM 237,64,88,2048
TR 78,220,28672
Mvaa 212,88,240
TR 80,221,16384
Mvaa 212,92,244
TR 82,221,20480
Mvaa 208,88,224
TR 84,221,24576
Mvaa 208,92,228
WL 7
DM 238,64,88,3072
TR 86,221,28672
Mvaa 144,64,224
TR 88,222,16384
Mvaa 144,68,228
TR 90,222,20480
Mvaa 148,64,240
s_add_u32 m0, s86, 12288
TR 92,222,24576
Mvaa 148,68,244
WL 7
buffer_load_dwordx4 v234, s[72:75], s89 offen offset:0 sc1 lds
TR 94,222,28672
Mvaa 132,72,248
TR 64,223,16384
Mvaa 132,76,252
TR 66,223,20480
Mvaa 128,72,232
s_add_u32 s90, s34, 24576
GL 160,234,72,90,0
TR 68,223,24576
Mvaa 128,76,236
WL 7
TR 70,223,28672
Mvaa 136,80,232
TR 72,224,16384
Mvaa 136,84,236
GL 164,234,72,90,1024
TR 74,224,20480
Mvaa 140,80,248
TR 76,224,24576
Mvaa 140,84,252
WL 7
TR 78,224,28672
Mvaa 156,88,248
TR 80,225,16384
Mvaa 156,92,252
GL 168,234,72,90,2048
TR 82,225,20480
Mvaa 152,88,232
TR 84,225,24576
Mvaa 152,92,236
WL 7
TR 86,225,28672
Mvaa 160,64,232
GL 172,234,72,90,3072
TR 88,226,16384
Mvaa 160,68,236
TR 90,226,20480
Mvaa 164,64,248
TR 92,226,24576
Mvaa 164,68,252
WL 7
TR 94,226,28672
Mvaa 172,72,248
s_add_u32 s90, s34, 155648
GL 176,234,72,90,0
TR 64,227,16384
Mvaa 172,76,252
TR 66,227,20480
Mvaa 168,72,232
TR 68,227,24576
Mvaa 168,76,236
WL 7
GL 180,234,72,90,1024
TR 70,227,28672
Mvaa 176,80,232
TR 72,240,6144
Mvaa 176,84,236
TR 74,240,6656
Mvaa 180,80,248
TR 76,240,7168
Mvaa 180,84,252
WL 7
GL 184,234,72,90,2048
TR 78,240,7680
Mvaa 188,88,248
s_waitcnt vmcnt(25) lgkmcnt(0)
s_barrier
TR 80,241,32768
Mvaa 188,92,252
TR 82,241,36864
Mvaa 184,88,232
GL 188,234,72,90,3072
TR 84,241,40960
Mvaa 184,92,236
TR 86,241,45056
Mvaa 208,64,232
TR 88,242,32768
Mvaa 208,68,236
TR 90,242,36864
Mvaa 212,64,248
s_add_u32 s87, s28, 1032192
s_add_u32 s88, s28, 1105920
s_add_u32 s89, s29, 28672
TR 92,242,40960
Mvaa 212,68,252
s_add_u32 m0, s84, 0
TR 94,242,45056
Mvaa 148,72,248
DM 235,64,87,0
TR 64,243,32768
Mvaa 148,76,252
TR 66,243,36864
Mvaa 144,72,232
DM 236,64,87,1024
TR 68,243,40960
Mvaa 144,76,236
s_waitcnt vmcnt(20) lgkmcnt(7)
TR 70,243,45056
Mvaa 128,80,128
TR 72,244,32768
Mvaa 128,84,132
DM 237,64,87,2048
TR 74,244,36864
Mvaa 132,80,144
TR 76,244,40960
Mvaa 132,84,148
WL 7
TR 78,244,45056
Mvaa 140,88,144
DM 238,64,87,3072
TR 80,245,32768
Mvaa 140,92,148
TR 82,245,36864
Mvaa 136,88,128
TR 84,245,40960
Mvaa 136,92,132
WL 7
TR 86,245,45056
Mvaa 152,64,128
s_add_u32 m0, s85, 0
TR 88,246,32768
Mvaa 152,68,132
DM 235,64,88,0
TR 90,246,36864
Mvaa 156,64,144
TR 92,246,40960
Mvaa 156,68,148
WL 7
DM 236,64,88,1024
TR 94,246,45056
Mvaa 164,72,144
TR 64,247,32768
Mvaa 164,76,148
TR 66,247,36864
Mvaa 160,72,128
TR 68,247,40960
Mvaa 160,76,132
WL 7
DM 237,64,88,2048
TR 70,247,45056
Mvaa 168,80,128
TR 72,248,32768
Mvaa 168,84,132
TR 74,248,36864
Mvaa 172,80,144
DM 238,64,88,3072
TR 76,248,40960
Mvaa 172,84,148
WL 7
TR 78,248,45056
Mvaa 180,88,144
TR 80,240,8192
Mvaa 180,92,148
TR 82,240,8704
Mvaa 176,88,128
s_add_u32 m0, s86, 0
TR 84,240,9216
Mvaa 176,92,132
WL 7
buffer_load_dwordx4 v234, s[72:75], s89 offen offset:0 sc1 lds
TR 86,240,9728
Mvaa 184,64,128
TR 88,220,32768
Mvaa 184,68,132
s_add_u32 s90, s34, 28672
GL 96,234,72,90,0
TR 90,220,36864
Mvaa 188,64,144
TR 92,220,40960
Mvaa 188,68,148
WL 7
TR 94,220,45056
Mvaa 212,72,144
TR 64,221,32768
Mvaa 212,76,148
GL 100,234,72,90,1024
TR 66,221,36864
Mvaa 208,72,128
TR 68,221,40960
Mvaa 208,76,132
WL 7
TR 70,221,45056
Mvaa 144,80,128
GL 104,234,72,90,2048
TR 72,222,32768
Mvaa 144,84,132
TR 74,222,36864
Mvaa 148,80,144
TR 76,222,40960
Mvaa 148,84,148
WL 7
TR 78,222,45056
Mvaa 132,88,152
GL 108,234,72,90,3072
TR 80,223,32768
Mvaa 132,92,156
TR 82,223,36864
Mvaa 128,88,136
TR 84,223,40960
Mvaa 128,92,140
WL 7
s_add_u32 s90, s34, 159744
GL 112,234,72,90,0
TR 86,223,45056
Mvaa 136,64,136
TR 88,224,32768
Mvaa 136,68,140
TR 90,224,36864
Mvaa 140,64,152
TR 92,224,40960
Mvaa 140,68,156
WL 7
GL 116,234,72,90,1024
TR 94,224,45056
Mvaa 156,72,152
TR 64,225,32768
Mvaa 156,76,156
TR 66,225,36864
Mvaa 152,72,136
GL 120,234,72,90,2048
TR 68,225,40960
Mvaa 152,76,140
WL 7
TR 70,225,45056
Mvaa 160,80,136
TR 72,226,32768
Mvaa 160,84,140
TR 74,226,36864
Mvaa 164,80,152
GL 124,234,72,90,3072
TR 76,226,40960
Mvaa 164,84,156
WL 7
TR 78,226,45056
Mvaa 172,88,152
TR 80,227,32768
Mvaa 172,92,156
s_add_u32 s87, s28, 1179648
s_add_u32 s88, s28, 1253376
s_add_u32 s89, s29, 32768
TR 82,227,36864
Mvaa 168,88,136
s_add_u32 m0, s84, 16384
TR 84,227,40960
Mvaa 168,92,140
Lv0stub2_fused_block:
s_cbranch_execz Lv0stub6_fused_block
WL 7
DM 235,64,87,0
TR 86,227,45056
Mvaa 176,64,136
TR 88,240,10240
Mvaa 176,68,140
DM 236,64,87,1024
TR 90,240,10752
Mvaa 180,64,152
TR 92,240,11264
Mvaa 180,68,156
WL 7
TR 94,240,11776
Mvaa 188,72,152
)"
R"(s_waitcnt vmcnt(27) lgkmcnt(0)
s_barrier
DM 237,64,87,2048
TR 64,241,49152
Mvaa 188,76,156
TR 66,241,53248
Mvaa 184,72,136
TR 68,241,57344
Mvaa 184,76,140
TR 70,241,61440
Mvaa 208,80,136
DM 238,64,87,3072
TR 72,242,49152
Mvaa 208,84,140
TR 74,242,53248
Mvaa 212,80,152
TR 76,242,57344
Mvaa 212,84,156
s_add_u32 m0, s85, 16384
TR 78,242,61440
Mvaa 148,88,152
DM 235,64,88,0
TR 80,243,49152
Mvaa 148,92,156
TR 82,243,53248
Mvaa 144,88,136
TR 84,243,57344
Mvaa 144,92,140
s_waitcnt vmcnt(22) lgkmcnt(7)
DM 236,64,88,1024
TR 86,243,61440
Mvaa 128,64,160
TR 88,244,49152
Mvaa 128,68,164
TR 90,244,53248
Mvaa 132,64,176
DM 237,64,88,2048
TR 92,244,57344
Mvaa 132,68,180
WL 7
TR 94,244,61440
Mvaa 140,72,176
TR 64,245,49152
Mvaa 140,76,180
TR 66,245,53248
Mvaa 136,72,160
DM 238,64,88,3072
TR 68,245,57344
Mvaa 136,76,164
WL 7
TR 70,245,61440
Mvaa 152,80,160
TR 72,246,49152
Mvaa 152,84,164
s_add_u32 m0, s86, 4096
TR 74,246,53248
Mvaa 156,80,176
buffer_load_dwordx4 v234, s[72:75], s89 offen offset:0 sc1 lds
TR 76,246,57344
Mvaa 156,84,180
WL 7
TR 78,246,61440
Mvaa 164,88,176
TR 80,247,49152
Mvaa 164,92,180
s_add_u32 s90, s34, 32768
GL 192,234,72,90,0
TR 82,247,53248
Mvaa 160,88,160
TR 84,247,57344
Mvaa 160,92,164
WL 7
TR 86,247,61440
Mvaa 168,64,160
GL 196,234,72,90,1024
TR 88,248,49152
Mvaa 168,68,164
TR 90,248,53248
Mvaa 172,64,176
TR 92,248,57344
Mvaa 172,68,180
WL 7
TR 94,248,61440
Mvaa 180,72,176
GL 200,234,72,90,2048
TR 64,240,12288
Mvaa 180,76,180
TR 66,240,12800
Mvaa 176,72,160
TR 68,240,13312
Mvaa 176,76,164
WL 7
GL 204,234,72,90,3072
TR 70,240,13824
Mvaa 184,80,160
TR 72,220,49152
Mvaa 184,84,164
TR 74,220,53248
Mvaa 188,80,176
TR 76,220,57344
Mvaa 188,84,180
WL 7
s_add_u32 s90, s34, 163840
GL 208,234,72,90,0
TR 78,220,61440
Mvaa 212,88,176
TR 80,221,49152
Mvaa 212,92,180
TR 82,221,53248
Mvaa 208,88,160
GL 212,234,72,90,1024
TR 84,221,57344
Mvaa 208,92,164
WL 7
TR 86,221,61440
Mvaa 144,64,160
TR 88,222,49152
Mvaa 144,68,164
TR 90,222,53248
Mvaa 148,64,176
GL 216,234,72,90,2048
TR 92,222,57344
Mvaa 148,68,180
WL 7
TR 94,222,61440
Mvaa 132,72,184
TR 64,223,49152
Mvaa 132,76,188
GL 220,234,72,90,3072
TR 66,223,53248
Mvaa 128,72,168
TR 68,223,57344
Mvaa 128,76,172
WL 7
TR 70,223,61440
Mvaa 136,80,168
TR 72,224,49152
Mvaa 136,84,172
s_add_u32 s87, s28, 1327104
s_add_u32 s88, s28, 1400832
s_add_u32 s89, s29, 36864
TR 74,224,53248
Mvaa 140,80,184
s_add_u32 m0, s84, 32768
TR 76,224,57344
Mvaa 140,84,188
WL 7
DM 235,64,87,0
TR 78,224,61440
Mvaa 156,88,184
TR 80,225,49152
Mvaa 156,92,188
DM 236,64,87,1024
TR 82,225,53248
Mvaa 152,88,168
TR 84,225,57344
Mvaa 152,92,172
WL 7
TR 86,225,61440
Mvaa 160,64,168
DM 237,64,87,2048
TR 88,226,49152
Mvaa 160,68,172
TR 90,226,53248
Mvaa 164,64,184
TR 92,226,57344
Mvaa 164,68,188
WL 7
DM 238,64,87,3072
TR 94,226,61440
Mvaa 172,72,184
TR 64,227,49152
Mvaa 172,76,188
TR 66,227,53248
Mvaa 168,72,168
TR 68,227,57344
Mvaa 168,76,172
WL 7
s_add_u32 m0, s85, 32768
TR 70,227,61440
Mvaa 176,80,168
DM 235,64,88,0
TR 72,240,14336
Mvaa 176,84,172
TR 74,240,14848
Mvaa 180,80,184
DM 236,64,88,1024
TR 76,240,15360
Mvaa 180,84,188
WL 7
TR 78,240,15872
Mvaa 188,88,184
s_waitcnt vmcnt(31) lgkmcnt(0)
s_barrier
TR 80,241,0
Mvaa 188,92,188
TR 82,241,4096
Mvaa 184,88,168
DM 237,64,88,2048
TR 84,241,8192
Mvaa 184,92,172
TR 86,241,12288
Mvaa 208,64,168
TR 88,242,0
Mvaa 208,68,172
DM 238,64,88,3072
TR 90,242,4096
Mvaa 212,64,184
TR 92,242,8192
Mvaa 212,68,188
TR 94,242,12288
Mvaa 148,72,184
TR 64,243,0
Mvaa 148,76,188
s_add_u32 m0, s86, 8192
TR 66,243,4096
Mvaa 144,72,168
buffer_load_dwordx4 v234, s[72:75], s89 offen offset:0 sc1 lds
TR 68,243,8192
Mvaa 144,76,172
s_waitcnt vmcnt(26) lgkmcnt(7)
TR 70,243,12288
Mvaa 128,80,96
s_add_u32 s90, s34, 36864
GL 224,234,72,90,0
TR 72,244,0
Mvaa 128,84,100
TR 74,244,4096
Mvaa 132,80,112
TR 76,244,8192
Mvaa 132,84,116
WL 7
TR 78,244,12288
Mvaa 140,88,112
GL 228,234,72,90,1024
TR 80,245,0
Mvaa 140,92,116
TR 82,245,4096
Mvaa 136,88,96
TR 84,245,8192
Mvaa 136,92,100
WL 7
GL 232,234,72,90,2048
TR 86,245,12288
Mvaa 152,64,96
TR 88,246,0
Mvaa 152,68,100
TR 90,246,4096
Mvaa 156,64,112
TR 92,246,8192
Mvaa 156,68,116
WL 7
GL 236,234,72,90,3072
TR 94,246,12288
Mvaa 164,72,112
TR 64,247,0
Mvaa 164,76,116
TR 66,247,4096
Mvaa 160,72,96
s_add_u32 s90, s34, 167936
GL 240,234,72,90,0
TR 68,247,8192
Mvaa 160,76,100
WL 7
TR 70,247,12288
Mvaa 168,80,96
TR 72,248,0
Mvaa 168,84,100
TR 74,248,4096
Mvaa 172,80,112
GL 244,234,72,90,1024
TR 76,248,8192
Mvaa 172,84,116
WL 7
TR 78,248,12288
Mvaa 180,88,112
TR 80,240,0
Mvaa 180,92,116
GL 248,234,72,90,2048
TR 82,240,512
Mvaa 176,88,96
TR 84,240,1024
Mvaa 176,92,100
WL 7
TR 86,240,1536
Mvaa 184,64,96
TR 88,220,0
Mvaa 184,68,100
GL 252,234,72,90,3072
TR 90,220,4096
Mvaa 188,64,112
TR 92,220,8192
Mvaa 188,68,116
WL 7
TR 94,220,12288
Mvaa 212,72,112
s_add_u32 s98, s36, 262144
TR 64,221,0
v_and_b32 v249, 3, v255
Mvaa 212,76,116
TR 66,221,4096
v_lshlrev_b32 v249, 7, v249
Mvaa 208,72,96
buffer_load_dwordx2 v[250:251], v249, s[72:75], s98 offen sc1
TR 68,221,8192
Mvaa 208,76,100
WL 7
s_add_u32 s87, s28, 4718592
s_add_u32 s88, s28, 4792320
TR 70,221,12288
Mvaa 144,80,96
s_add_u32 s89, s29, 131072
s_add_u32 m0, s84, 49152
TR 72,222,0
Mvaa 144,84,100
DM 235,64,87,0
TR 74,222,4096
Mvaa 148,80,112
TR 76,222,8192
Mvaa 148,84,116
WL 7
DM 236,64,87,1024
TR 78,222,12288
Mvaa 132,88,120
TR 80,223,0
Mvaa 132,92,124
DM 237,64,87,2048
TR 82,223,4096
Mvaa 128,88,104
TR 84,223,8192
Mvaa 128,92,108
WL 7
TR 86,223,12288
Mvaa 136,64,104
DM 238,64,87,3072
TR 88,224,0
Mvaa 136,68,108
TR 90,224,4096
Mvaa 140,64,120
TR 92,224,8192
Mvaa 140,68,124
WL 7
TR 94,224,12288
Mvaa 156,72,120
s_add_u32 m0, s85, 49152
TR 64,225,0
Mvaa 156,76,124
DM 235,64,88,0
TR 66,225,4096
Mvaa 152,72,104
TR 68,225,8192
Mvaa 152,76,108
WL 7
DM 236,64,88,1024
TR 70,225,12288
Mvaa 160,80,104
TR 72,226,0
Mvaa 160,84,108
TR 74,226,4096
Mvaa 164,80,120
TR 76,226,8192
Mvaa 164,84,124
WL 7
DM 237,64,88,2048
TR 78,226,12288
Mvaa 172,88,120
TR 80,227,0
Mvaa 172,92,124
TR 82,227,4096
Mvaa 168,88,104
DM 238,64,88,3072
TR 84,227,8192
Mvaa 168,92,108
WL 7
TR 86,227,12288
Mvaa 176,64,104
TR 88,240,2048
Mvaa 176,68,108
TR 90,240,2560
Mvaa 180,64,120
s_add_u32 m0, s86, 12288
TR 92,240,3072
Mvaa 180,68,124
WL 7
buffer_load_dwordx4 v234, s[72:75], s89 offen offset:0 sc1 lds
TR 94,240,3584
Mvaa 188,72,120
s_waitcnt vmcnt(35) lgkmcnt(0)
s_barrier
TR 64,241,16384
Mvaa 188,76,124
s_add_u32 s90, s33, 0
GL 128,234,72,90,0
TR 66,241,20480
Mvaa 184,72,104
TR 68,241,24576
Mvaa 184,76,108
TR 70,241,28672
Mvaa 208,80,104
TR 72,242,16384
)"
R"(Mvaa 208,84,108
GL 132,234,72,90,1024
TR 74,242,20480
Mvaa 212,80,120
TR 76,242,24576
Mvaa 212,84,124
TR 78,242,28672
Mvaa 148,88,120
GL 136,234,72,90,2048
TR 80,243,16384
Mvaa 148,92,124
TR 82,243,20480
Mvaa 144,88,104
TR 84,243,24576
Mvaa 144,92,108
s_waitcnt vmcnt(30) lgkmcnt(7)
TR 86,243,28672
Mvaa 128,64,192
GL 140,234,72,90,3072
TR 88,244,16384
Mvaa 128,68,196
TR 90,244,20480
Mvaa 132,64,208
TR 92,244,24576
Mvaa 132,68,212
WL 7
s_add_u32 s90, s33, 16384
GL 144,234,72,90,0
TR 94,244,28672
Mvaa 140,72,208
TR 64,245,16384
Mvaa 140,76,212
TR 66,245,20480
Mvaa 136,72,192
TR 68,245,24576
Mvaa 136,76,196
WL 7
GL 148,234,72,90,1024
TR 70,245,28672
Mvaa 152,80,192
TR 72,246,16384
Mvaa 152,84,196
TR 74,246,20480
Mvaa 156,80,208
GL 152,234,72,90,2048
TR 76,246,24576
Mvaa 156,84,212
WL 7
TR 78,246,28672
Mvaa 164,88,208
TR 80,247,16384
Mvaa 164,92,212
TR 82,247,20480
Mvaa 160,88,192
GL 156,234,72,90,3072
TR 84,247,24576
Mvaa 160,92,196
WL 7
TR 86,247,28672
Mvaa 168,64,192
TR 88,248,16384
Mvaa 168,68,196
s_add_u32 s87, s28, 0
s_add_u32 s88, s28, 73728
s_add_u32 s89, s29, 147456
TR 90,248,20480
Mvaa 172,64,208
s_add_u32 m0, s84, 0
TR 92,248,24576
Mvaa 172,68,212
WL 7
DM 235,56,87,0
TR 94,248,28672
Mvaa 180,72,208
TR 64,240,4096
Mvaa 180,76,212
DM 236,56,87,1024
TR 66,240,4608
Mvaa 176,72,192
TR 68,240,5120
Mvaa 176,76,196
WL 7
TR 70,240,5632
Mvaa 184,80,192
DM 237,56,87,2048
TR 72,220,16384
Mvaa 184,84,196
TR 74,220,20480
Mvaa 188,80,208
TR 76,220,24576
Mvaa 188,84,212
WL 7
TR 78,220,28672
Mvaa 212,88,208
DM 238,56,87,3072
TR 80,221,16384
Mvaa 212,92,212
TR 82,221,20480
Mvaa 208,88,192
TR 84,221,24576
Mvaa 208,92,196
WL 7
s_add_u32 m0, s85, 0
TR 86,221,28672
Mvaa 144,64,192
DM 235,56,88,0
TR 88,222,16384
Mvaa 144,68,196
TR 90,222,20480
Mvaa 148,64,208
TR 92,222,24576
Mvaa 148,68,212
WL 7
DM 236,56,88,1024
TR 94,222,28672
Mvaa 132,72,216
TR 64,223,16384
Mvaa 132,76,220
TR 66,223,20480
Mvaa 128,72,200
DM 237,56,88,2048
TR 68,223,24576
Mvaa 128,76,204
WL 7
TR 70,223,28672
Mvaa 136,80,200
Lv0stub3_fused_block:
s_cbranch_execz Lt2x_fused_block
TR 72,224,16384
Mvaa 136,84,204
TR 74,224,20480
Mvaa 140,80,216
DM 238,56,88,3072
TR 76,224,24576
Mvaa 140,84,220
WL 7
TR 78,224,28672
Mvaa 156,88,216
TR 80,225,16384
Mvaa 156,92,220
s_add_u32 m0, s86, 0
TR 82,225,20480
Mvaa 152,88,200
buffer_load_dwordx4 v234, s[72:75], s89 offen offset:0 sc1 lds
TR 84,225,24576
Mvaa 152,92,204
WL 7
TR 86,225,28672
Mvaa 160,64,200
TR 88,226,16384
Mvaa 160,68,204
s_waitcnt vmcnt(26)
TR 90,226,20480
v_cmp_ne_u64 vcc, s[82:83], v[250:251]
s_nop 3
s_cbranch_vccnz Lslow0_fused_block
Lslowb0_fused_block:
Mvaa 164,64,216
s_add_u32 s90, s34, 16777216
GL 160,234,72,90,0
TR 92,226,24576
Mvaa 164,68,220
WL 7
TR 94,226,28672
Mvaa 172,72,216
GL 164,234,72,90,1024
TR 64,227,16384
Mvaa 172,76,220
TR 66,227,20480
Mvaa 168,72,200
TR 68,227,24576
Mvaa 168,76,204
WL 7
TR 70,227,28672
Mvaa 176,80,200
GL 168,234,72,90,2048
TR 72,240,6144
Mvaa 176,84,204
TR 74,240,6656
Mvaa 180,80,216
TR 76,240,7168
Mvaa 180,84,220
WL 7
GL 172,234,72,90,3072
TR 78,240,7680
Mvaa 188,88,216
WL 0
s_barrier
TR 80,241,32768
Mvaa 188,92,220
TR 82,241,36864
Mvaa 184,88,200
TR 84,241,40960
Mvaa 184,92,204
s_add_u32 s90, s34, 16908288
GL 176,234,72,90,0
TR 86,241,45056
Mvaa 208,64,200
TR 88,242,32768
Mvaa 208,68,204
TR 90,242,36864
Mvaa 212,64,216
GL 180,234,72,90,1024
TR 92,242,40960
Mvaa 212,68,220
TR 94,242,45056
Mvaa 148,72,216
TR 64,243,32768
Mvaa 148,76,220
TR 66,243,36864
Mvaa 144,72,200
GL 184,234,72,90,2048
TR 68,243,40960
Mvaa 144,76,204
WL 7
TR 70,243,45056
Mvaa 128,80,224
TR 72,244,32768
Mvaa 128,84,228
GL 188,234,72,90,3072
TR 74,244,36864
Mvaa 132,80,240
TR 76,244,40960
Mvaa 132,84,244
WL 7
TR 78,244,45056
Mvaa 140,88,240
TR 80,245,32768
Mvaa 140,92,244
s_add_u32 s87, s28, 147456
s_add_u32 s88, s28, 221184
s_add_u32 s89, s29, 151552
TR 82,245,36864
Mvaa 136,88,224
s_add_u32 m0, s84, 16384
TR 84,245,40960
Mvaa 136,92,228
WL 7
DM 235,56,87,0
TR 86,245,45056
Mvaa 152,64,224
TR 88,246,32768
Mvaa 152,68,228
DM 236,56,87,1024
TR 90,246,36864
Mvaa 156,64,240
TR 92,246,40960
Mvaa 156,68,244
WL 7
TR 94,246,45056
Mvaa 164,72,240
DM 237,56,87,2048
TR 64,247,32768
Mvaa 164,76,244
TR 66,247,36864
Mvaa 160,72,224
TR 68,247,40960
Mvaa 160,76,228
WL 7
DM 238,56,87,3072
TR 70,247,45056
Mvaa 168,80,224
TR 72,248,32768
Mvaa 168,84,228
TR 74,248,36864
Mvaa 172,80,240
TR 76,248,40960
Mvaa 172,84,244
WL 7
s_add_u32 m0, s85, 16384
TR 78,248,45056
Mvaa 180,88,240
DM 235,56,88,0
TR 80,240,8192
Mvaa 180,92,244
TR 82,240,8704
Mvaa 176,88,224
DM 236,56,88,1024
TR 84,240,9216
Mvaa 176,92,228
WL 7
TR 86,240,9728
Mvaa 184,64,224
TR 88,220,32768
Mvaa 184,68,228
TR 90,220,36864
Mvaa 188,64,240
DM 237,56,88,2048
TR 92,220,40960
Mvaa 188,68,244
WL 7
TR 94,220,45056
Mvaa 212,72,240
TR 64,221,32768
Mvaa 212,76,244
DM 238,56,88,3072
TR 66,221,36864
Mvaa 208,72,224
TR 68,221,40960
Mvaa 208,76,228
WL 7
TR 70,221,45056
Mvaa 144,80,224
TR 72,222,32768
Mvaa 144,84,228
s_add_u32 m0, s86, 4096
TR 74,222,36864
Mvaa 148,80,240
buffer_load_dwordx4 v234, s[72:75], s89 offen offset:0 sc1 lds
TR 76,222,40960
Mvaa 148,84,244
WL 7
TR 78,222,45056
Mvaa 132,88,248
s_add_u32 s90, s34, 16781312
GL 96,234,72,90,0
TR 80,223,32768
Mvaa 132,92,252
TR 82,223,36864
Mvaa 128,88,232
TR 84,223,40960
Mvaa 128,92,236
WL 7
TR 86,223,45056
Mvaa 136,64,232
GL 100,234,72,90,1024
TR 88,224,32768
Mvaa 136,68,236
TR 90,224,36864
Mvaa 140,64,248
TR 92,224,40960
Mvaa 140,68,252
WL 7
GL 104,234,72,90,2048
TR 94,224,45056
Mvaa 156,72,248
TR 64,225,32768
Mvaa 156,76,252
TR 66,225,36864
Mvaa 152,72,232
TR 68,225,40960
Mvaa 152,76,236
WL 7
GL 108,234,72,90,3072
TR 70,225,45056
Mvaa 160,80,232
TR 72,226,32768
Mvaa 160,84,236
TR 74,226,36864
Mvaa 164,80,248
s_add_u32 s90, s34, 16912384
GL 112,234,72,90,0
TR 76,226,40960
Mvaa 164,84,252
WL 7
TR 78,226,45056
Mvaa 172,88,248
TR 80,227,32768
Mvaa 172,92,252
TR 82,227,36864
Mvaa 168,88,232
GL 116,234,72,90,1024
TR 84,227,40960
Mvaa 168,92,236
WL 7
TR 86,227,45056
Mvaa 176,64,232
TR 88,240,10240
Mvaa 176,68,236
GL 120,234,72,90,2048
TR 90,240,10752
Mvaa 180,64,248
TR 92,240,11264
Mvaa 180,68,252
WL 7
TR 94,240,11776
Mvaa 188,72,248
s_waitcnt vmcnt(41) lgkmcnt(0)
s_barrier
TR 64,241,49152
Mvaa 188,76,252
GL 124,234,72,90,3072
TR 66,241,53248
Mvaa 184,72,232
TR 68,241,57344
Mvaa 184,76,236
TR 70,241,61440
Mvaa 208,80,232
s_add_u32 s87, s28, 294912
s_add_u32 s88, s28, 368640
s_add_u32 s89, s29, 155648
TR 72,242,49152
Mvaa 208,84,236
s_add_u32 m0, s84, 32768
TR 74,242,53248
Mvaa 212,80,248
DM 235,56,87,0
TR 76,242,57344
Mvaa 212,84,252
TR 78,242,61440
)"
R"(Mvaa 148,88,248
DM 236,56,87,1024
TR 80,243,49152
Mvaa 148,92,252
TR 82,243,53248
Mvaa 144,88,232
TR 84,243,57344
Mvaa 144,92,236
s_waitcnt vmcnt(36) lgkmcnt(7)
DM 237,56,87,2048
TR 86,243,61440
Mvaa 128,64,128
TR 88,244,49152
v_lshrrev_b32 v79, 4, v255
v_and_b32 v78, 7, v255
Mvaa 128,68,132
TR 90,244,53248
v_lshl_add_u32 v78, v79, 3, v78
v_lshlrev_b32 v72, 3, v78
Mvaa 132,64,144
TR 92,244,57344
v_and_b32 v78, 15, v255
v_mul_u32_u24 v77, 1152, v78
Mvaa 132,68,148
WL 7
DM 238,56,87,3072
TR 94,244,61440
v_lshl_add_u32 v75, v79, 3, v77
Mvaa 140,72,144
s_lshl_b32 s95, s80, 5
s_add_u32 s95, s95, 1024
TR 64,245,49152
v_add_u32 v76, s95, v75
Mvaa 140,76,148
TR 66,245,53248
v_cmp_gt_u32 vcc, 8, v78
v_mov_b32 v78, 0x7ffffff0
s_nop 0
v_cndmask_b32 v73, v72, v78, vcc
v_cndmask_b32 v72, v78, v72, vcc
v_cndmask_b32 v76, v78, v76, vcc
Mvaa 136,72,128
s_add_u32 m0, s85, 32768
TR 68,245,57344
v_mov_b32 v74, v72
v_and_b32 v78, 1, v79
Mvaa 136,76,132
WL 7
DM 235,56,88,0
TR 70,245,61440
v_lshl_add_u32 v77, v78, 5, v77
Mvaa 152,80,128
TR 72,246,49152
v_lshrrev_b32 v78, 1, v79
v_lshl_add_u32 v77, v78, 4, v77
Mvaa 152,84,132
s_lshl_b32 s95, s79, 2
s_add_u32 s95, s95, s19
s_mul_i32 s95, s95, 36864
TR 74,246,53248
Mvaa 156,80,144
s_add_u32 s31, s95, 0
s_lshl_b32 s96, s80, 8
s_add_u32 s95, s95, s96
DM 236,56,88,1024
TR 76,246,57344
Mvaa 156,84,148
WL 7
s_add_u32 s25, s95, 0
s_add_u32 s32, s95, 18432
TR 78,246,61440
Mvaa 164,88,144
s_lshl_b32 s95, s35, 13
s_add_u32 s30, s95, 40173568
s_add_u32 s20, s95, 40198144
TR 80,247,49152
Mvaa 164,92,148
s_add_u32 s21, s95, 40192256
s_add_u32 s22, s95, 40186368
s_lshl_b32 s91, s35, 7
DM 237,56,88,2048
TR 82,247,53248
Mvaa 160,88,128
s_add_u32 s92, s91, 0
TR 84,247,57344
Mvaa 160,92,132
WL 7
TR 86,247,61440
Mvaa 168,64,128
TR 88,248,49152
Mvaa 168,68,132
DM 238,56,88,3072
TR 90,248,53248
Mvaa 172,64,144
TR 92,248,57344
Mvaa 172,68,148
WL 7
TR 94,248,61440
Mvaa 180,72,144
s_add_u32 m0, s86, 8192
TR 64,240,12288
Mvaa 180,76,148
buffer_load_dwordx4 v234, s[72:75], s89 offen offset:0 sc1 lds
TR 66,240,12800
Mvaa 176,72,128
TR 68,240,13312
Mvaa 176,76,132
WL 7
TR 70,240,13824
Mvaa 184,80,128
s_add_u32 s90, s34, 16785408
GL 192,234,72,90,0
TR 72,220,49152
Mvaa 184,84,132
TR 74,220,53248
Mvaa 188,80,144
TR 76,220,57344
Mvaa 188,84,148
WL 7
GL 196,234,72,90,1024
TR 78,220,61440
Mvaa 212,88,144
TR 80,221,49152
Mvaa 212,92,148
TR 82,221,53248
Mvaa 208,88,128
TR 84,221,57344
Mvaa 208,92,132
WL 7
GL 200,234,72,90,2048
TR 86,221,61440
Mvaa 144,64,128
TR 88,222,49152
Mvaa 144,68,132
TR 90,222,53248
Mvaa 148,64,144
GL 204,234,72,90,3072
TR 92,222,57344
Mvaa 148,68,148
WL 7
TR 94,222,61440
Mvaa 132,72,152
TR 64,223,49152
Mvaa 132,76,156
TR 66,223,53248
Mvaa 128,72,136
s_add_u32 s90, s34, 16916480
GL 208,234,72,90,0
TR 68,223,57344
Mvaa 128,76,140
WL 7
TR 70,223,61440
Mvaa 136,80,136
TR 72,224,49152
Mvaa 136,84,140
GL 212,234,72,90,1024
TR 74,224,53248
Mvaa 140,80,152
TR 76,224,57344
Mvaa 140,84,156
WL 7
TR 78,224,61440
Mvaa 156,88,152
TR 80,225,49152
Mvaa 156,92,156
GL 216,234,72,90,2048
TR 82,225,53248
Mvaa 152,88,136
TR 84,225,57344
Mvaa 152,92,140
WL 7
TR 86,225,61440
Mvaa 160,64,136
GL 220,234,72,90,3072
TR 88,226,49152
Mvaa 160,68,140
TR 90,226,53248
Mvaa 164,64,152
TR 92,226,57344
Mvaa 164,68,156
WL 7
TR 94,226,61440
Mvaa 172,72,152
TR 64,227,49152
Mvaa 172,76,156
TR 66,227,53248
Mvaa 168,72,136
TR 68,227,57344
Mvaa 168,76,140
WL 7
TR 70,227,61440
Mvaa 176,80,136
TR 72,240,14336
Mvaa 176,84,140
TR 74,240,14848
Mvaa 180,80,152
TR 76,240,15360
Mvaa 180,84,156
WL 7
TR 78,240,15872
Mvaa 188,88,152
s_waitcnt vmcnt(42) lgkmcnt(0)
s_barrier
TR 80,241,0
Mvaa 188,92,156
TR 82,241,4096
Mvaa 184,88,136
TR 84,241,8192
Mvaa 184,92,140
TR 86,241,12288
Mvaa 208,64,136
TR 88,242,0
Mvaa 208,68,140
TR 90,242,4096
Mvaa 212,64,152
TR 92,242,8192
Mvaa 212,68,156
TR 94,242,12288
Mvaa 148,72,152
TR 64,243,0
Mvaa 148,76,156
TR 66,243,4096
Mvaa 144,72,136
TR 68,243,8192
Mvaa 144,76,140
s_waitcnt vmcnt(34) lgkmcnt(7)
TR 70,243,12288
v_mfma_f32_16x16x32_bf16 v[0:3], a[80:83], a[160:163], 0
TR 72,244,0
Mvaa 0,84,164
TR 74,244,4096
v_mfma_f32_16x16x32_bf16 v[4:7], a[80:83], a[176:179], 0
TR 76,244,8192
Mvaa 4,84,180
WL 7
TR 78,244,12288
v_mfma_f32_16x16x32_bf16 v[12:15], a[88:91], a[176:179], 0
TR 80,245,0
CV 148,148,149
Mvaa 12,92,180
TR 82,245,4096
CV 149,150,151
v_mfma_f32_16x16x32_bf16 v[8:11], a[88:91], a[160:163], 0
TR 84,245,8192
CV 150,144,145
Mvaa 8,92,164
WL 7
TR 86,245,12288
v_mfma_f32_16x16x32_bf16 v[24:27], a[64:67], a[160:163], 0
TR 88,246,0
CV 151,146,147
Mvaa 24,68,164
TR 90,246,4096
CV 152,152,153
v_mfma_f32_16x16x32_bf16 v[28:31], a[64:67], a[176:179], 0
TR 92,246,8192
CV 153,154,155
Mvaa 28,68,180
WL 7
TR 94,246,12288
v_mfma_f32_16x16x32_bf16 v[36:39], a[72:75], a[176:179], 0
TR 64,247,0
CV 154,156,157
Mvaa 36,76,180
TR 66,247,4096
CV 155,158,159
v_mfma_f32_16x16x32_bf16 v[32:35], a[72:75], a[160:163], 0
TR 68,247,8192
CV 160,160,161
Mvaa 32,76,164
WL 7
TR 70,247,12288
v_mfma_f32_16x16x32_bf16 v[40:43], a[80:83], a[160:163], 0
TR 72,248,0
CV 161,162,163
Mvaa 40,84,164
TR 74,248,4096
CV 162,164,165
v_mfma_f32_16x16x32_bf16 v[44:47], a[80:83], a[176:179], 0
Lv0stub4_fused_block:
s_cbranch_execz Lt2x_fused_block
TR 76,248,8192
CV 163,166,167
Mvaa 44,84,180
WL 7
TR 78,248,12288
v_mfma_f32_16x16x32_bf16 v[52:55], a[88:91], a[176:179], 0
TR 80,240,0
CV 168,168,169
Mvaa 52,92,180
TR 82,240,512
CV 169,170,171
v_mfma_f32_16x16x32_bf16 v[48:51], a[88:91], a[160:163], 0
TR 84,240,1024
CV 170,172,173
Mvaa 48,92,164
WL 7
TR 86,240,1536
v_mfma_f32_16x16x32_bf16 v[56:59], a[64:67], a[160:163], 0
TR 88,220,0
CV 171,174,175
Mvaa 56,68,164
TR 90,220,4096
CV 176,176,177
v_mfma_f32_16x16x32_bf16 v[60:63], a[64:67], a[176:179], 0
TR 92,220,8192
CV 177,178,179
Mvaa 60,68,180
WL 7
TR 94,220,12288
v_mfma_f32_16x16x32_bf16 v[68:71], a[72:75], a[176:179], 0
TR 64,221,0
CV 178,180,181
Mvaa 68,76,180
TR 66,221,4096
CV 179,182,183
v_mfma_f32_16x16x32_bf16 v[64:67], a[72:75], a[160:163], 0
TR 68,221,8192
CV 184,184,185
Mvaa 64,76,164
WL 7
TR 70,221,12288
v_mfma_f32_16x16x32_bf16 v[16:19], a[80:83], a[160:163], 0
TR 72,222,0
CV 185,186,187
Mvaa 16,84,164
TR 74,222,4096
CV 186,188,189
v_mfma_f32_16x16x32_bf16 v[20:23], a[80:83], a[176:179], 0
TR 76,222,8192
CV 187,190,191
Mvaa 20,84,180
WL 7
TR 78,222,12288
Mvaa 4,88,184
TR 80,223,0
CV 208,208,209
Mvaa 4,92,188
TR 82,223,4096
CV 209,210,211
Mvaa 0,88,168
TR 84,223,8192
CV 210,212,213
Mvaa 0,92,172
WL 7
TR 86,223,12288
Mvaa 8,64,168
TR 88,224,0
CV 211,214,215
Mvaa 8,68,172
s_add_u32 s16, s30, 2048
buffer_store_dwordx2 v[150:151], v73, s[72:75], s16 offen sc1
TR 90,224,4096
Mvaa 12,64,184
s_add_u32 s16, s30, 4352
buffer_store_dwordx2 v[148:149], v72, s[72:75], s16 offen sc1
TR 92,224,8192
Mvaa 12,68,188
WL 7
s_add_u32 s16, s30, 6656
buffer_store_dwordx2 v[148:149], v73, s[72:75], s16 offen sc1
TR 94,224,12288
Mvaa 28,72,184
s_add_u32 s16, s30, 0
buffer_store_dwordx4 v[152:155], v234, s[72:75], s16 offen sc1
TR 64,225,0
Mvaa 28,76,188
s_add_u32 s16, s30, 1024
buffer_store_dwordx4 v[160:163], v234, s[72:75], s16 offen sc1
TR 66,225,4096
Mvaa 24,72,168
s_add_u32 s98, s36, 262656
TR 68,225,8192
v_and_b32 v249, 3, v255
Mvaa 24,76,172
WL 7
TR 70,225,12288
v_lshlrev_b32 v249, 7, v249
Mvaa 32,80,168
buffer_load_dwordx2 v[250:251], v249, s[72:75], s98 offen sc1
TR 72,226,0
Mvaa 32,84,172
s_add_u32 s87, s28, 442368
s_add_u32 s88, s28, 516096
s_add_u32 s89, s29, 159744
TR 74,226,4096
Mvaa 36,80,184
s_add_u32 m0, s84, 49152
TR 76,226,8192
Mvaa 36,84,188
WL 7
DM 235,56,87,0
TR 78,226,12288
Mvaa 44,88,184
TR 80,227,0
Mvaa 44,92,188
DM 236,56,87,1024
TR 82,227,4096
Mvaa 40,88,168
TR 84,227,8192
)"
R"(Mvaa 40,92,172
WL 7
DM 237,56,87,2048
TR 86,227,12288
Mvaa 48,64,168
TR 88,240,2048
Mvaa 48,68,172
DM 238,56,87,3072
TR 90,240,2560
Mvaa 52,64,184
s_add_u32 m0, s85, 49152
TR 92,240,3072
Mvaa 52,68,188
WL 7
DM 235,56,88,0
TR 94,240,3584
Mvaa 60,72,184
s_waitcnt vmcnt(36) lgkmcnt(0)
s_barrier
TR 64,241,16384
Mvaa 60,76,188
DM 236,56,88,1024
TR 66,241,20480
Mvaa 56,72,168
TR 68,241,24576
Mvaa 56,76,172
DM 237,56,88,2048
TR 70,241,28672
Mvaa 64,80,168
TR 72,242,16384
Mvaa 64,84,172
DM 238,56,88,3072
TR 74,242,20480
Mvaa 68,80,184
s_add_u32 m0, s86, 12288
TR 76,242,24576
Mvaa 68,84,188
buffer_load_dwordx4 v234, s[72:75], s89 offen offset:0 sc1 lds
TR 78,242,28672
Mvaa 20,88,184
s_add_u32 s90, s34, 16789504
GL 224,234,72,90,0
TR 80,243,16384
Mvaa 20,92,188
TR 82,243,20480
Mvaa 16,88,168
GL 228,234,72,90,1024
TR 84,243,24576
Mvaa 16,92,172
s_waitcnt vmcnt(34) lgkmcnt(7)
TR 86,243,28672
Mvaa 0,64,96
GL 232,234,72,90,2048
TR 88,244,16384
Mvaa 0,68,100
TR 90,244,20480
Mvaa 4,64,112
GL 236,234,72,90,3072
TR 92,244,24576
Mvaa 4,68,116
WL 7
s_add_u32 s90, s34, 16920576
GL 240,234,72,90,0
TR 94,244,28672
Mvaa 12,72,112
TR 64,245,16384
Mvaa 12,76,116
TR 66,245,20480
Mvaa 8,72,96
GL 244,234,72,90,1024
TR 68,245,24576
Mvaa 8,76,100
WL 7
TR 70,245,28672
Mvaa 24,80,96
TR 72,246,16384
Mvaa 24,84,100
GL 248,234,72,90,2048
TR 74,246,20480
Mvaa 28,80,112
TR 76,246,24576
Mvaa 28,84,116
WL 7
TR 78,246,28672
Mvaa 36,88,112
TR 80,247,16384
Mvaa 36,92,116
GL 252,234,72,90,3072
TR 82,247,20480
Mvaa 32,88,96
TR 84,247,24576
Mvaa 32,92,100
WL 7
TR 86,247,28672
Mvaa 40,64,96
s_add_u32 s16, s30, 2304
buffer_store_dwordx4 v[168:171], v234, s[72:75], s16 offen sc1
TR 88,248,16384
Mvaa 40,68,100
TR 90,248,20480
Mvaa 44,64,112
TR 92,248,24576
Mvaa 44,68,116
WL 7
TR 94,248,28672
Mvaa 52,72,112
TR 64,240,4096
Mvaa 52,76,116
TR 66,240,4608
Mvaa 48,72,96
TR 68,240,5120
Mvaa 48,76,100
WL 7
TR 70,240,5632
Mvaa 56,80,96
TR 72,220,16384
Mvaa 56,84,100
TR 74,220,20480
Mvaa 60,80,112
TR 76,220,24576
Mvaa 60,84,116
WL 7
s_add_u32 s16, s30, 3328
buffer_store_dwordx4 v[176:179], v234, s[72:75], s16 offen sc1
TR 78,220,28672
Mvaa 68,88,112
TR 80,221,16384
Mvaa 68,92,116
TR 82,221,20480
Mvaa 64,88,96
TR 84,221,24576
Mvaa 64,92,100
WL 7
TR 86,221,28672
Mvaa 16,64,96
TR 88,222,16384
Mvaa 16,68,100
TR 90,222,20480
Mvaa 20,64,112
TR 92,222,24576
Mvaa 20,68,116
WL 7
TR 94,222,28672
Mvaa 4,72,120
TR 64,223,16384
Mvaa 4,76,124
TR 66,223,20480
Mvaa 0,72,104
s_add_u32 s16, s30, 4608
buffer_store_dwordx4 v[184:187], v234, s[72:75], s16 offen sc1
TR 68,223,24576
Mvaa 0,76,108
WL 7
TR 70,223,28672
Mvaa 8,80,104
TR 72,224,16384
Mvaa 8,84,108
TR 74,224,20480
Mvaa 12,80,120
TR 76,224,24576
Mvaa 12,84,124
WL 7
TR 78,224,28672
Mvaa 28,88,120
TR 80,225,16384
Mvaa 28,92,124
TR 82,225,20480
Mvaa 24,88,104
TR 84,225,24576
Mvaa 24,92,108
WL 7
TR 86,225,28672
Mvaa 32,64,104
TR 88,226,16384
Mvaa 32,68,108
s_add_u32 s16, s30, 5632
buffer_store_dwordx4 v[208:211], v234, s[72:75], s16 offen sc1
TR 90,226,20480
Mvaa 36,64,120
TR 92,226,24576
Mvaa 36,68,124
WL 7
TR 94,226,28672
Mvaa 44,72,120
TR 64,227,16384
Mvaa 44,76,124
TR 66,227,20480
Mvaa 40,72,104
TR 68,227,24576
Mvaa 40,76,108
WL 7
TR 70,227,28672
Mvaa 48,80,104
TR 72,240,6144
Mvaa 48,84,108
TR 74,240,6656
Mvaa 52,80,120
TR 76,240,7168
Mvaa 52,84,124
WL 7
TR 78,240,7680
Mvaa 60,88,120
s_waitcnt vmcnt(35) lgkmcnt(0)
s_barrier
s_add_u32 s87, s28, 589824
TR 80,241,32768
Mvaa 60,92,124
s_add_u32 s88, s28, 663552
s_add_u32 s89, s29, 163840
s_add_u32 m0, s84, 0
TR 82,241,36864
Mvaa 56,88,104
DM 235,56,87,0
TR 84,241,40960
Mvaa 56,92,108
TR 86,241,45056
Mvaa 64,64,104
DM 236,56,87,1024
TR 88,242,32768
Mvaa 64,68,108
TR 90,242,36864
Mvaa 68,64,120
TR 92,242,40960
Mvaa 68,68,124
DM 237,56,87,2048
TR 94,242,45056
Mvaa 20,72,120
TR 64,243,32768
Mvaa 20,76,124
TR 66,243,36864
Mvaa 16,72,104
TR 68,243,40960
Mvaa 16,76,108
s_waitcnt vmcnt(30) lgkmcnt(7)
DM 238,56,87,3072
TR 70,243,45056
Mvaa 0,80,192
TR 72,244,32768
Mvaa 0,84,196
TR 74,244,36864
Mvaa 4,80,208
s_add_u32 m0, s85, 0
TR 76,244,40960
Mvaa 4,84,212
WL 7
DM 235,56,88,0
TR 78,244,45056
Mvaa 12,88,208
TR 80,245,32768
Mvaa 12,92,212
TR 82,245,36864
Mvaa 8,88,192
DM 236,56,88,1024
TR 84,245,40960
Mvaa 8,92,196
WL 7
TR 86,245,45056
Mvaa 24,64,192
TR 88,246,32768
Mvaa 24,68,196
DM 237,56,88,2048
TR 90,246,36864
Mvaa 28,64,208
TR 92,246,40960
Mvaa 28,68,212
WL 7
TR 94,246,45056
Mvaa 36,72,208
TR 64,247,32768
Mvaa 36,76,212
DM 238,56,88,3072
TR 66,247,36864
Mvaa 32,72,192
TR 68,247,40960
Mvaa 32,76,196
WL 7
TR 70,247,45056
Mvaa 40,80,192
s_add_u32 m0, s86, 0
TR 72,248,32768
Mvaa 40,84,196
buffer_load_dwordx4 v234, s[72:75], s89 offen offset:0 sc1 lds
TR 74,248,36864
Mvaa 44,80,208
TR 76,248,40960
Mvaa 44,84,212
WL 7
TR 78,248,45056
Mvaa 52,88,208
s_waitcnt vmcnt(30)
TR 80,240,8192
v_cmp_ne_u64 vcc, s[82:83], v[250:251]
s_nop 3
s_cbranch_vccnz Lslow1_fused_block
Lslowb1_fused_block:
Mvaa 52,92,212
s_add_u32 s90, s34, 16793600
GL 128,234,72,90,0
TR 82,240,8704
Mvaa 48,88,192
TR 84,240,9216
Mvaa 48,92,196
WL 7
GL 132,234,72,90,1024
TR 86,240,9728
Mvaa 56,64,192
TR 88,220,32768
Mvaa 56,68,196
TR 90,220,36864
Mvaa 60,64,208
TR 92,220,40960
Mvaa 60,68,212
WL 7
GL 136,234,72,90,2048
TR 94,220,45056
Mvaa 68,72,208
TR 64,221,32768
Mvaa 68,76,212
TR 66,221,36864
Mvaa 64,72,192
GL 140,234,72,90,3072
TR 68,221,40960
Mvaa 64,76,196
WL 7
TR 70,221,45056
Mvaa 16,80,192
TR 72,222,32768
Mvaa 16,84,196
TR 74,222,36864
Mvaa 20,80,208
s_add_u32 s90, s34, 16924672
GL 144,234,72,90,0
TR 76,222,40960
Mvaa 20,84,212
WL 7
TR 78,222,45056
Mvaa 4,88,216
TR 80,223,32768
Mvaa 4,92,220
GL 148,234,72,90,1024
TR 82,223,36864
Mvaa 0,88,200
TR 84,223,40960
Mvaa 0,92,204
WL 7
TR 86,223,45056
Mvaa 8,64,200
TR 88,224,32768
Mvaa 8,68,204
GL 152,234,72,90,2048
TR 90,224,36864
Mvaa 12,64,216
TR 92,224,40960
Mvaa 12,68,220
WL 7
TR 94,224,45056
Mvaa 28,72,216
GL 156,234,72,90,3072
TR 64,225,32768
Mvaa 28,76,220
TR 66,225,36864
Mvaa 24,72,200
TR 68,225,40960
Mvaa 24,76,204
WL 7
TR 70,225,45056
Mvaa 32,80,200
s_waitcnt vmcnt(17)
s_add_u32 s98, s92, 393216
TR 72,226,32768
v_mov_b32 v250, s82
Mvaa 32,84,204
TR 74,226,36864
v_mov_b32 v251, s83
v_lshlrev_b32 v249, 3, v255
Mvaa 36,80,216
TR 76,226,40960
s_mov_b64 exec, 0xffff
buffer_store_dwordx2 v[250:251], v249, s[72:75], s98 offen sc1
s_mov_b64 exec, -1
Mvaa 36,84,220
WL 7
s_add_u32 s87, s28, 737280
s_add_u32 s88, s28, 811008
TR 78,226,45056
Mvaa 44,88,216
s_add_u32 s89, s29, 167936
s_add_u32 m0, s84, 16384
TR 80,227,32768
Mvaa 44,92,220
DM 235,56,87,0
TR 82,227,36864
Mvaa 40,88,200
TR 84,227,40960
Mvaa 40,92,204
WL 7
DM 236,56,87,1024
TR 86,227,45056
Mvaa 48,64,200
TR 88,240,10240
)"
R"(Mvaa 48,68,204
DM 237,56,87,2048
TR 90,240,10752
Mvaa 52,64,216
TR 92,240,11264
Mvaa 52,68,220
WL 7
DM 238,56,87,3072
TR 94,240,11776
Mvaa 60,72,216
WL 0
s_barrier
TR 64,241,49152
Mvaa 60,76,220
TR 66,241,53248
Mvaa 56,72,200
s_add_u32 m0, s85, 16384
TR 68,241,57344
Mvaa 56,76,204
DM 235,56,88,0
TR 70,241,61440
Mvaa 64,80,200
TR 72,242,49152
Mvaa 64,84,204
Lv0stub5_fused_block:
s_cbranch_execz Lt2x_fused_block
DM 236,56,88,1024
TR 74,242,53248
Mvaa 68,80,216
TR 76,242,57344
Mvaa 68,84,220
TR 78,242,61440
Mvaa 20,88,216
TR 80,243,49152
Mvaa 20,92,220
DM 237,56,88,2048
TR 82,243,53248
Mvaa 16,88,200
TR 84,243,57344
Mvaa 16,92,204
WL 7
TR 86,243,61440
Mvaa 0,64,224
DM 238,56,88,3072
TR 88,244,49152
Mvaa 0,68,228
TR 90,244,53248
Mvaa 4,64,240
TR 92,244,57344
Mvaa 4,68,244
WL 7
TR 94,244,61440
Mvaa 12,72,240
s_add_u32 m0, s86, 4096
TR 64,245,49152
Mvaa 12,76,244
buffer_load_dwordx4 v234, s[72:75], s89 offen offset:0 sc1 lds
TR 66,245,53248
Mvaa 8,72,224
TR 68,245,57344
Mvaa 8,76,228
WL 7
s_add_u32 s90, s34, 16797696
GL 160,234,72,90,0
TR 70,245,61440
Mvaa 24,80,224
TR 72,246,49152
Mvaa 24,84,228
TR 74,246,53248
Mvaa 28,80,240
TR 76,246,57344
Mvaa 28,84,244
WL 7
GL 164,234,72,90,1024
TR 78,246,61440
Mvaa 36,88,240
TR 80,247,49152
Mvaa 36,92,244
TR 82,247,53248
Mvaa 32,88,224
GL 168,234,72,90,2048
TR 84,247,57344
Mvaa 32,92,228
WL 7
TR 86,247,61440
Mvaa 40,64,224
TR 88,248,49152
Mvaa 40,68,228
TR 90,248,53248
Mvaa 44,64,240
GL 172,234,72,90,3072
TR 92,248,57344
Mvaa 44,68,244
WL 7
TR 94,248,61440
Mvaa 52,72,240
TR 64,240,12288
Mvaa 52,76,244
s_add_u32 s90, s34, 16928768
GL 176,234,72,90,0
TR 66,240,12800
Mvaa 48,72,224
TR 68,240,13312
Mvaa 48,76,228
WL 7
TR 70,240,13824
Mvaa 56,80,224
TR 72,220,49152
Mvaa 56,84,228
GL 180,234,72,90,1024
TR 74,220,53248
Mvaa 60,80,240
TR 76,220,57344
Mvaa 60,84,244
WL 7
TR 78,220,61440
Mvaa 68,88,240
GL 184,234,72,90,2048
TR 80,221,49152
Mvaa 68,92,244
TR 82,221,53248
Mvaa 64,88,224
TR 84,221,57344
Mvaa 64,92,228
WL 7
TR 86,221,61440
Mvaa 16,64,224
GL 188,234,72,90,3072
TR 88,222,49152
Mvaa 16,68,228
TR 90,222,53248
Mvaa 20,64,240
TR 92,222,57344
Mvaa 20,68,244
WL 7
s_add_u32 s87, s28, 884736
s_add_u32 s88, s28, 958464
TR 94,222,61440
Mvaa 4,72,248
s_add_u32 s89, s29, 172032
s_add_u32 m0, s84, 32768
TR 64,223,49152
Mvaa 4,76,252
DM 235,56,87,0
TR 66,223,53248
Mvaa 0,72,232
TR 68,223,57344
Mvaa 0,76,236
WL 7
DM 236,56,87,1024
TR 70,223,61440
Mvaa 8,80,232
TR 72,224,49152
Mvaa 8,84,236
TR 74,224,53248
Mvaa 12,80,248
DM 237,56,87,2048
TR 76,224,57344
Mvaa 12,84,252
WL 7
TR 78,224,61440
Mvaa 28,88,248
TR 80,225,49152
Mvaa 28,92,252
TR 82,225,53248
Mvaa 24,88,232
DM 238,56,87,3072
TR 84,225,57344
Mvaa 24,92,236
WL 7
TR 86,225,61440
Mvaa 32,64,232
TR 88,226,49152
Mvaa 32,68,236
s_add_u32 m0, s85, 32768
TR 90,226,53248
Mvaa 36,64,248
DM 235,56,88,0
TR 92,226,57344
Mvaa 36,68,252
WL 7
TR 94,226,61440
Mvaa 44,72,248
TR 64,227,49152
Mvaa 44,76,252
DM 236,56,88,1024
TR 66,227,53248
Mvaa 40,72,232
TR 68,227,57344
Mvaa 40,76,236
WL 7
TR 70,227,61440
Mvaa 48,80,232
DM 237,56,88,2048
TR 72,240,14336
Mvaa 48,84,236
TR 74,240,14848
Mvaa 52,80,248
TR 76,240,15360
Mvaa 52,84,252
WL 7
TR 78,240,15872
Mvaa 60,88,248
s_waitcnt vmcnt(33) lgkmcnt(0)
s_barrier
DM 238,56,88,3072
TR 80,241,0
Mvaa 60,92,252
TR 82,241,4096
Mvaa 56,88,232
TR 84,241,8192
Mvaa 56,92,236
s_add_u32 m0, s86, 8192
TR 86,241,12288
Mvaa 64,64,232
buffer_load_dwordx4 v234, s[72:75], s89 offen offset:0 sc1 lds
TR 88,242,0
Mvaa 64,68,236
TR 90,242,4096
Mvaa 68,64,248
TR 92,242,8192
Mvaa 68,68,252
s_add_u32 s90, s34, 16801792
GL 96,234,72,90,0
TR 94,242,12288
Mvaa 20,72,248
TR 64,243,0
Mvaa 20,76,252
TR 66,243,4096
Mvaa 16,72,232
GL 100,234,72,90,1024
TR 68,243,8192
Mvaa 16,76,236
s_waitcnt vmcnt(29) lgkmcnt(7)
TR 70,243,12288
Mvaa 0,80,128
TR 72,244,0
Mvaa 0,84,132
TR 74,244,4096
Mvaa 4,80,144
GL 104,234,72,90,2048
TR 76,244,8192
Mvaa 4,84,148
WL 7
TR 78,244,12288
Mvaa 12,88,144
TR 80,245,0
Mvaa 12,92,148
GL 108,234,72,90,3072
TR 82,245,4096
Mvaa 8,88,128
TR 84,245,8192
Mvaa 8,92,132
WL 7
TR 86,245,12288
Mvaa 24,64,128
TR 88,246,0
Mvaa 24,68,132
s_add_u32 s90, s34, 16932864
GL 112,234,72,90,0
TR 90,246,4096
Mvaa 28,64,144
TR 92,246,8192
Mvaa 28,68,148
WL 7
TR 94,246,12288
Mvaa 36,72,144
GL 116,234,72,90,1024
TR 64,247,0
Mvaa 36,76,148
TR 66,247,4096
Mvaa 32,72,128
TR 68,247,8192
Mvaa 32,76,132
WL 7
TR 70,247,12288
Mvaa 40,80,128
GL 120,234,72,90,2048
TR 72,248,0
Mvaa 40,84,132
TR 74,248,4096
Mvaa 44,80,144
TR 76,248,8192
Mvaa 44,84,148
WL 7
GL 124,234,72,90,3072
TR 78,248,12288
Mvaa 52,88,144
TR 80,240,0
Mvaa 52,92,148
TR 82,240,512
Mvaa 48,88,128
TR 84,240,1024
Mvaa 48,92,132
WL 7
s_add_u32 s87, s28, 1032192
s_add_u32 s88, s28, 1105920
TR 86,240,1536
Mvaa 56,64,128
s_add_u32 s89, s29, 176128
s_add_u32 m0, s84, 49152
TR 88,220,0
Mvaa 56,68,132
DM 235,56,87,0
TR 90,220,4096
Mvaa 60,64,144
TR 92,220,8192
Mvaa 60,68,148
WL 7
DM 236,56,87,1024
TR 94,220,12288
Mvaa 68,72,144
TR 64,221,0
Mvaa 68,76,148
TR 66,221,4096
Mvaa 64,72,128
DM 237,56,87,2048
TR 68,221,8192
Mvaa 64,76,132
WL 7
TR 70,221,12288
Mvaa 16,80,128
TR 72,222,0
Mvaa 16,84,132
DM 238,56,87,3072
TR 74,222,4096
Mvaa 20,80,144
TR 76,222,8192
Mvaa 20,84,148
WL 7
TR 78,222,12288
Mvaa 4,88,152
TR 80,223,0
Mvaa 4,92,156
s_add_u32 m0, s85, 49152
TR 82,223,4096
Mvaa 0,88,136
DM 235,56,88,0
TR 84,223,8192
Mvaa 0,92,140
WL 7
TR 86,223,12288
Mvaa 8,64,136
DM 236,56,88,1024
TR 88,224,0
Mvaa 8,68,140
TR 90,224,4096
Mvaa 12,64,152
TR 92,224,8192
Mvaa 12,68,156
WL 7
TR 94,224,12288
Mvaa 28,72,152
DM 237,56,88,2048
TR 64,225,0
Mvaa 28,76,156
TR 66,225,4096
Mvaa 24,72,136
TR 68,225,8192
Mvaa 24,76,140
WL 7
DM 238,56,88,3072
TR 70,225,12288
Mvaa 32,80,136
TR 72,226,0
Mvaa 32,84,140
TR 74,226,4096
Mvaa 36,80,152
TR 76,226,8192
Mvaa 36,84,156
WL 7
s_add_u32 m0, s86, 12288
TR 78,226,12288
Mvaa 44,88,152
buffer_load_dwordx4 v234, s[72:75], s89 offen offset:0 sc1 lds
TR 80,227,0
Mvaa 44,92,156
TR 82,227,4096
Mvaa 40,88,136
s_add_u32 s90, s34, 16805888
GL 192,234,72,90,0
TR 84,227,8192
Mvaa 40,92,140
WL 7
TR 86,227,12288
Mvaa 48,64,136
TR 88,240,2048
Mvaa 48,68,140
TR 90,240,2560
Mvaa 52,64,152
GL 196,234,72,90,1024
TR 92,240,3072
Mvaa 52,68,156
WL 7
TR 94,240,3584
Mvaa 60,72,152
s_waitcnt vmcnt(36) lgkmcnt(0)
s_barrier
TR 64,241,16384
Mvaa 60,76,156
)"
R"(GL 200,234,72,90,2048
TR 66,241,20480
Mvaa 56,72,136
TR 68,241,24576
Mvaa 56,76,140
TR 70,241,28672
Mvaa 64,80,136
TR 72,242,16384
Mvaa 64,84,140
GL 204,234,72,90,3072
TR 74,242,20480
Mvaa 68,80,152
TR 76,242,24576
Mvaa 68,84,156
TR 78,242,28672
Mvaa 20,88,152
s_add_u32 s90, s34, 16936960
GL 208,234,72,90,0
TR 80,243,16384
Mvaa 20,92,156
TR 82,243,20480
Mvaa 16,88,136
TR 84,243,24576
Mvaa 16,92,140
s_waitcnt vmcnt(31) lgkmcnt(7)
TR 86,243,28672
Mvaa 0,64,160
GL 212,234,72,90,1024
TR 88,244,16384
Mvaa 0,68,164
TR 90,244,20480
Mvaa 4,64,176
TR 92,244,24576
Mvaa 4,68,180
WL 7
GL 216,234,72,90,2048
TR 94,244,28672
Mvaa 12,72,176
TR 64,245,16384
Mvaa 12,76,180
TR 66,245,20480
Mvaa 8,72,160
TR 68,245,24576
Mvaa 8,76,164
WL 7
GL 220,234,72,90,3072
TR 70,245,28672
Mvaa 24,80,160
TR 72,246,16384
Mvaa 24,84,164
TR 74,246,20480
Mvaa 28,80,176
s_add_u32 s98, s91, 393216
TR 76,246,24576
v_and_b32 v249, 3, v255
Mvaa 28,84,180
WL 7
TR 78,246,28672
v_lshlrev_b32 v249, 7, v249
Mvaa 36,88,176
buffer_load_dwordx2 v[250:251], v249, s[72:75], s98 offen sc1
TR 80,247,16384
Mvaa 36,92,180
TR 82,247,20480
Mvaa 32,88,160
buffer_load_dwordx2 v[80:81], v75, s[40:43], s25 offen offset:0
TR 84,247,24576
Mvaa 32,92,164
WL 7
TR 86,247,28672
Mvaa 40,64,160
buffer_load_dwordx2 v[82:83], v75, s[40:43], s32 offen offset:0
TR 88,248,16384
Mvaa 40,68,164
TR 90,248,20480
Mvaa 44,64,176
buffer_load_dwordx2 v[84:85], v75, s[40:43], s25 offen offset:32
TR 92,248,24576
Mvaa 44,68,180
WL 7
TR 94,248,28672
Mvaa 52,72,176
buffer_load_dwordx2 v[86:87], v75, s[40:43], s32 offen offset:32
TR 64,240,4096
Mvaa 52,76,180
TR 66,240,4608
Mvaa 48,72,160
buffer_load_dwordx2 v[88:89], v76, s[40:43], s31 offen
TR 68,240,5120
Mvaa 48,76,164
WL 7
TR 70,240,5632
Mvaa 56,80,160
TR 72,220,16384
Mvaa 56,84,164
TR 74,220,20480
Mvaa 60,80,176
TR 76,220,24576
Mvaa 60,84,180
WL 7
TR 78,220,28672
Mvaa 68,88,176
TR 80,221,16384
Mvaa 68,92,180
TR 82,221,20480
Mvaa 64,88,160
TR 84,221,24576
Mvaa 64,92,164
WL 7
TR 86,221,28672
Mvaa 16,64,160
TR 88,222,16384
Mvaa 16,68,164
TR 90,222,20480
Mvaa 20,64,176
TR 92,222,24576
Mvaa 20,68,180
WL 7
TR 94,222,28672
Mvaa 4,72,184
TR 64,223,16384
Mvaa 4,76,188
TR 66,223,20480
Mvaa 0,72,168
TR 68,223,24576
Mvaa 0,76,172
WL 7
TR 70,223,28672
Mvaa 8,80,168
TR 72,224,16384
Mvaa 8,84,172
TR 74,224,20480
Mvaa 12,80,184
TR 76,224,24576
Mvaa 12,84,188
WL 7
TR 78,224,28672
Mvaa 28,88,184
TR 80,225,16384
Mvaa 28,92,188
TR 82,225,20480
Mvaa 24,88,168
TR 84,225,24576
Mvaa 24,92,172
WL 7
TR 86,225,28672
Mvaa 32,64,168
TR 88,226,16384
Mvaa 32,68,172
TR 90,226,20480
Mvaa 36,64,184
TR 92,226,24576
Mvaa 36,68,188
WL 7
TR 94,226,28672
Mvaa 44,72,184
TR 64,227,16384
Mvaa 44,76,188
TR 66,227,20480
Mvaa 40,72,168
TR 68,227,24576
Mvaa 40,76,172
WL 7
TR 70,227,28672
Mvaa 48,80,168
Lv0stub6_fused_block:
s_cbranch_execz Lt2x_fused_block
TR 72,240,6144
Mvaa 48,84,172
TR 74,240,6656
Mvaa 52,80,184
TR 76,240,7168
Mvaa 52,84,188
WL 7
TR 78,240,7680
Mvaa 60,88,184
s_waitcnt vmcnt(14) lgkmcnt(0)
s_barrier
TR 80,240,8192
Mvaa 60,92,188
TR 82,240,8704
Mvaa 56,88,168
TR 84,240,9216
Mvaa 56,92,172
TR 86,240,9728
Mvaa 64,64,168
TR 88,240,10240
Mvaa 64,68,172
TR 90,240,10752
Mvaa 68,64,184
TR 92,240,11264
Mvaa 68,68,188
TR 94,240,11776
Mvaa 20,72,184
TR 64,240,12288
Mvaa 20,76,188
TR 66,240,12800
Mvaa 16,72,168
TR 68,240,13312
Mvaa 16,76,172
WL 7
TR 70,240,13824
Mvaa 16,80,96
TR 72,240,14336
Mvaa 16,84,100
TR 74,240,14848
Mvaa 20,80,112
TR 76,240,15360
Mvaa 20,84,116
WL 7
TR 78,240,15872
Mvaa 20,88,120
TR 80,243,32768
Mvaa 20,92,124
TR 82,243,36864
Mvaa 16,88,104
TR 84,243,40960
Mvaa 16,92,108
s_waitcnt vmcnt(6) lgkmcnt(7)
TR 86,243,45056
Mvaa 16,64,192
TR 88,222,32768
Mvaa 16,68,196
TR 90,222,36864
Mvaa 20,64,208
s_waitcnt vmcnt(5)
TR 92,222,40960
v_cmp_ne_u64 vcc, s[82:83], v[250:251]
s_nop 3
s_cbranch_vccnz Lslow2_fused_block
Lslowb2_fused_block:
Mvaa 20,68,212
WL 7
buffer_load_dwordx4 v[152:155], v234, s[72:75], s20 offen offset:0 sc1
TR 94,222,45056
Mvaa 20,72,216
TR 64,243,49152
Mvaa 20,76,220
buffer_load_dwordx4 v[156:159], v234, s[72:75], s20 offen offset:1024 sc1
TR 66,243,53248
Mvaa 16,72,200
TR 68,243,57344
Mvaa 16,76,204
WL 7
buffer_load_dwordx2 v[160:161], v74, s[72:75], s20 offen offset:2048 sc1
TR 70,243,61440
Mvaa 24,80,96
TR 72,222,49152
Mvaa 24,84,100
buffer_load_dwordx4 v[162:165], v234, s[72:75], s21 offen offset:0 sc1
TR 74,222,53248
Mvaa 28,80,112
TR 76,222,57344
Mvaa 28,84,116
WL 7
buffer_load_dwordx4 v[166:169], v234, s[72:75], s21 offen offset:1024 sc1
TR 78,222,61440
CV 20,20,21
Mvaa 28,88,120
TR 80,244,32768
CV 21,22,23
CV 22,16,17
Mvaa 28,92,124
buffer_load_dwordx2 v[170:171], v74, s[72:75], s21 offen offset:2048 sc1
TR 82,244,36864
CV 23,18,19
Mvaa 24,88,104
TR 84,244,40960
Mvaa 24,92,108
WL 7
buffer_load_dwordx4 v[172:175], v234, s[72:75], s22 offen offset:0 sc1
TR 86,244,45056
Mvaa 24,64,192
TR 88,223,32768
Mvaa 24,68,196
buffer_load_dwordx4 v[176:179], v234, s[72:75], s22 offen offset:1024 sc1
TR 90,223,36864
Mvaa 28,64,208
TR 92,223,40960
Mvaa 28,68,212
WL 7
buffer_load_dwordx2 v[180:181], v74, s[72:75], s22 offen offset:2048 sc1
TR 94,223,45056
Mvaa 28,72,216
s_add_u32 s20, s20, 8388608
s_add_u32 s21, s21, 8388608
s_add_u32 s22, s22, 8388608
TR 64,244,49152
Mvaa 28,76,220
s_add_u32 s16, s30, 8390656
buffer_store_dwordx2 v[22:23], v73, s[72:75], s16 offen sc1
TR 66,244,53248
Mvaa 24,72,200
s_add_u32 s16, s30, 8392960
buffer_store_dwordx2 v[20:21], v72, s[72:75], s16 offen sc1
TR 68,244,57344
Mvaa 24,76,204
WL 7
s_add_u32 s16, s30, 8395264
buffer_store_dwordx2 v[20:21], v73, s[72:75], s16 offen sc1
TR 70,244,61440
Mvaa 32,80,96
TR 72,223,49152
Mvaa 32,84,100
TR 74,223,53248
Mvaa 36,80,112
TR 76,223,57344
Mvaa 36,84,116
WL 7
TR 78,223,61440
CV 24,24,25
Mvaa 36,88,120
TR 80,245,32768
CV 25,26,27
CV 26,28,29
Mvaa 36,92,124
TR 82,245,36864
CV 27,30,31
Mvaa 32,88,104
s_add_u32 s16, s30, 8388608
buffer_store_dwordx4 v[24:27], v234, s[72:75], s16 offen sc1
TR 84,245,40960
Mvaa 32,92,108
WL 7
TR 86,245,45056
Mvaa 32,64,192
TR 88,224,32768
Mvaa 32,68,196
TR 90,224,36864
Mvaa 36,64,208
TR 92,224,40960
Mvaa 36,68,212
WL 7
TR 94,224,45056
Mvaa 36,72,216
TR 64,245,49152
Mvaa 36,76,220
TR 66,245,53248
Mvaa 32,72,200
TR 68,245,57344
Mvaa 32,76,204
WL 7
TR 70,245,61440
Mvaa 40,80,96
TR 72,224,49152
Mvaa 40,84,100
TR 74,224,53248
Mvaa 44,80,112
TR 76,224,57344
Mvaa 44,84,116
WL 7
TR 78,224,61440
CV 32,32,33
Mvaa 44,88,120
TR 80,246,32768
CV 33,34,35
CV 34,36,37
Mvaa 44,92,124
TR 82,246,36864
CV 35,38,39
Mvaa 40,88,104
s_add_u32 s16, s30, 8389632
buffer_store_dwordx4 v[32:35], v234, s[72:75], s16 offen sc1
TR 84,246,40960
Mvaa 40,92,108
WL 7
TR 86,246,45056
Mvaa 40,64,192
TR 88,225,32768
Mvaa 40,68,196
TR 90,225,36864
Mvaa 44,64,208
TR 92,225,40960
Mvaa 44,68,212
WL 7
TR 94,225,45056
Mvaa 44,72,216
TR 64,246,49152
Mvaa 44,76,220
TR 66,246,53248
Mvaa 40,72,200
TR 68,246,57344
Mvaa 40,76,204
WL 7
TR 70,246,61440
Mvaa 48,80,96
TR 72,225,49152
Mvaa 48,84,100
TR 74,225,53248
Mvaa 52,80,112
TR 76,225,57344
Mvaa 52,84,116
WL 7
TR 78,225,61440
CV 40,40,41
Mvaa 52,88,120
TR 80,247,32768
CV 41,42,43
CV 42,44,45
Mvaa 52,92,124
)"
R"(TR 82,247,36864
CV 43,46,47
Mvaa 48,88,104
s_add_u32 s16, s30, 8390912
buffer_store_dwordx4 v[40:43], v234, s[72:75], s16 offen sc1
TR 84,247,40960
Mvaa 48,92,108
WL 7
TR 86,247,45056
Mvaa 48,64,192
TR 88,226,32768
Mvaa 48,68,196
TR 90,226,36864
Mvaa 52,64,208
TR 92,226,40960
Mvaa 52,68,212
WL 7
TR 94,226,45056
Mvaa 52,72,216
TR 64,247,49152
Mvaa 52,76,220
TR 66,247,53248
Mvaa 48,72,200
TR 68,247,57344
Mvaa 48,76,204
WL 7
TR 70,247,61440
Mvaa 56,80,96
TR 72,226,49152
Mvaa 56,84,100
TR 74,226,53248
Mvaa 60,80,112
TR 76,226,57344
Mvaa 60,84,116
WL 7
TR 78,226,61440
CV 48,48,49
Mvaa 60,88,120
TR 80,248,32768
CV 49,50,51
CV 50,52,53
Mvaa 60,92,124
TR 82,248,36864
CV 51,54,55
Mvaa 56,88,104
s_add_u32 s16, s30, 8391936
buffer_store_dwordx4 v[48:51], v234, s[72:75], s16 offen sc1
TR 84,248,40960
Mvaa 56,92,108
WL 7
TR 86,248,45056
Mvaa 56,64,192
TR 88,227,32768
Mvaa 56,68,196
TR 90,227,36864
Mvaa 60,64,208
TR 92,227,40960
Mvaa 60,68,212
WL 7
TR 94,227,45056
Mvaa 60,72,216
TR 64,248,49152
Mvaa 60,76,220
TR 66,248,53248
Mvaa 56,72,200
TR 68,248,57344
Mvaa 56,76,204
WL 7
TR 70,248,61440
Mvaa 64,80,96
TR 72,227,49152
Mvaa 64,84,100
TR 74,227,53248
Mvaa 68,80,112
TR 76,227,57344
Mvaa 68,84,116
WL 7
TR 78,227,61440
CV 56,56,57
Mvaa 68,88,120
TR 80,241,32768
CV 57,58,59
CV 58,60,61
Mvaa 68,92,124
TR 82,241,36864
CV 59,62,63
Mvaa 64,88,104
s_add_u32 s16, s30, 8393216
buffer_store_dwordx4 v[56:59], v234, s[72:75], s16 offen sc1
TR 84,241,40960
Mvaa 64,92,108
WL 7
TR 86,241,45056
Mvaa 64,64,192
TR 88,220,32768
Mvaa 64,68,196
TR 90,220,36864
Mvaa 68,64,208
TR 92,220,40960
Mvaa 68,68,212
WL 7
TR 94,220,45056
Mvaa 68,72,216
TR 64,241,49152
Mvaa 68,76,220
TR 66,241,53248
Mvaa 64,72,200
TR 68,241,57344
Mvaa 64,76,204
WL 7
TR 70,241,61440
Mvaa 0,80,96
TR 72,220,49152
Mvaa 0,84,100
TR 74,220,53248
Mvaa 4,80,112
TR 76,220,57344
Mvaa 4,84,116
WL 7
TR 78,220,61440
CV 64,64,65
Mvaa 4,88,120
TR 80,242,32768
CV 65,66,67
CV 66,68,69
Mvaa 4,92,124
TR 82,242,36864
CV 67,70,71
Mvaa 0,88,104
s_add_u32 s16, s30, 8394240
buffer_store_dwordx4 v[64:67], v234, s[72:75], s16 offen sc1
TR 84,242,40960
Mvaa 0,92,108
WL 7
TR 86,242,45056
Mvaa 0,64,192
TR 88,221,32768
Mvaa 0,68,196
TR 90,221,36864
Mvaa 4,64,208
TR 92,221,40960
Mvaa 4,68,212
WL 7
TR 94,221,45056
Mvaa 4,72,216
TR 64,242,49152
Mvaa 4,76,220
TR 66,242,53248
Mvaa 0,72,200
TR 68,242,57344
Mvaa 0,76,204
WL 7
TR 70,242,61440
Mvaa 8,80,96
TR 72,221,49152
Mvaa 8,84,100
TR 74,221,53248
Mvaa 12,80,112
TR 76,221,57344
Mvaa 12,84,116
WL 7
TR 78,221,61440
Mvaa 12,88,120
Mvaa 12,92,124
Mvaa 8,88,104
Mvaa 8,92,108
WL 4
Mvaa 8,64,192
Mvaa 8,68,196
Mvaa 12,64,208
Mvaa 12,68,212
WL 0
Mvaa 12,72,216
Mvaa 12,76,220
Mvaa 8,72,200
Mvaa 8,76,204
buffer_load_dwordx4 v[96:99], v234, s[72:75], s20 offen offset:0 sc1
buffer_load_dwordx4 v[100:103], v234, s[72:75], s20 offen offset:1024 sc1
buffer_load_dwordx2 v[104:105], v74, s[72:75], s20 offen offset:2048 sc1
buffer_load_dwordx4 v[106:109], v234, s[72:75], s21 offen offset:0 sc1
buffer_load_dwordx4 v[110:113], v234, s[72:75], s21 offen offset:1024 sc1
buffer_load_dwordx2 v[114:115], v74, s[72:75], s21 offen offset:2048 sc1
buffer_load_dwordx4 v[116:119], v234, s[72:75], s22 offen offset:0 sc1
buffer_load_dwordx4 v[120:123], v234, s[72:75], s22 offen offset:1024 sc1
buffer_load_dwordx2 v[124:125], v74, s[72:75], s22 offen offset:2048 sc1
s_waitcnt vmcnt(18)
s_branch Ltail_fused_block
Lslow0_fused_block:
s_add_u32 s98, s36, 262144
v_and_b32 v249, 3, v255
v_lshlrev_b32 v249, 7, v249
s_mov_b32 s99, 0x20000
Lpoll1_fused_block:
buffer_load_dwordx2 v[250:251], v249, s[72:75], s98 offen sc1
s_waitcnt vmcnt(0)
v_cmp_ne_u64 vcc, s[82:83], v[250:251]
s_sub_u32 s99, s99, 1
s_cbranch_scc1 Lpolld1_fused_block
s_nop 2
s_cbranch_vccnz Lpoll1_fused_block
Lpolld1_fused_block:
s_branch Lslowb0_fused_block
Lslow1_fused_block:
s_add_u32 s98, s36, 262656
v_and_b32 v249, 3, v255
v_lshlrev_b32 v249, 7, v249
s_mov_b32 s99, 0x20000
Lpoll2_fused_block:
buffer_load_dwordx2 v[250:251], v249, s[72:75], s98 offen sc1
s_waitcnt vmcnt(0)
v_cmp_ne_u64 vcc, s[82:83], v[250:251]
s_sub_u32 s99, s99, 1
s_cbranch_scc1 Lpolld2_fused_block
s_nop 2
s_cbranch_vccnz Lpoll2_fused_block
Lpolld2_fused_block:
s_branch Lslowb1_fused_block
Lslow2_fused_block:
s_add_u32 s98, s91, 393216
v_and_b32 v249, 3, v255
v_lshlrev_b32 v249, 7, v249
s_mov_b32 s99, 0x20000
Lpoll3_fused_block:
buffer_load_dwordx2 v[250:251], v249, s[72:75], s98 offen sc1
s_waitcnt vmcnt(0)
v_cmp_ne_u64 vcc, s[82:83], v[250:251]
s_sub_u32 s99, s99, 1
s_cbranch_scc1 Lpolld3_fused_block
s_nop 2
s_cbranch_vccnz Lpoll3_fused_block
Lpolld3_fused_block:
s_branch Lslowb2_fused_block
.p2align 6
s_branch Lv1stub0_fused_block
s_branch Lv1stub1_fused_block
s_branch Lv1stub2_fused_block
s_branch Lv1stub3_fused_block
Lvar1_fused_block:
v_mfma_f32_16x16x32_bf16 v[128:131], a[64:67], a[32:35], v[184:187]
v_exp_f32 v212, v144
Mvaa 128,68,36
s_add_u32 s90, s34, 40960
GL 128,234,72,90,0
Mvaa 128,72,40
s_add_u32 s20, s25, 16777216
v_exp_f32 v213, v145
Mvaa 128,76,44
GL 132,234,72,90,1024
CV 168,212,213
v_mfma_f32_16x16x32_bf16 v[132:135], a[92:95], a[44:47], v[184:187]
v_exp_f32 v214, v146
Mvaa 132,88,40
GL 136,234,72,90,2048
Mvaa 132,84,36
s_add_u32 s21, s25, 16908288
v_exp_f32 v215, v147
Mvaa 132,80,32
GL 140,234,72,90,3072
CV 169,214,215
v_mfma_f32_16x16x32_bf16 v[136:139], a[96:99], a[32:35], v[184:187]
s_add_u32 s90, s34, 172032
GL 144,234,72,90,0
v_exp_f32 v212, v148
Mvaa 136,100,36
Mvaa 136,104,40
s_add_u32 s22, s26, 16777216
GL 148,234,72,90,1024
v_exp_f32 v213, v149
Mvaa 136,108,44
CV 170,212,213
v_mfma_f32_16x16x32_bf16 v[140:143], a[124:127], a[44:47], v[184:187]
GL 152,234,72,90,2048
v_exp_f32 v214, v150
Mvaa 140,120,40
Mvaa 140,116,36
s_add_u32 s23, s26, 16908288
GL 156,234,72,90,3072
v_exp_f32 v215, v151
Mvaa 140,112,32
s_add_u32 s90, s34, 45056
GL 160,234,72,90,0
CV 171,214,215
Mvav 0,192,160
v_exp_f32 v212, v152
Mvav 0,196,164
GL 164,234,72,90,1024
Mvav 4,200,160
v_exp_f32 v213, v153
Mvav 4,204,164
GL 168,234,72,90,2048
CV 172,212,213
Mvav 8,208,160
v_exp_f32 v214, v154
Mvav 8,212,164
GL 172,234,72,90,3072
Mvav 12,216,160
s_add_u32 s90, s34, 176128
GL 176,234,72,90,0
v_exp_f32 v215, v155
Mvav 12,220,164
CV 173,214,215
Mvav 16,224,160
GL 180,234,72,90,1024
v_exp_f32 v212, v156
Mvav 16,228,164
Mvav 20,232,160
GL 184,234,72,90,2048
v_exp_f32 v213, v157
Mvav 20,236,164
CV 174,212,213
Mvav 24,240,160
GL 188,234,72,90,3072
v_exp_f32 v214, v158
Mvav 24,244,164
Mvav 28,248,160
v_exp_f32 v215, v159
Mvav 28,252,164
CV 175,214,215
v_smfmac_f32_16x16x64_bf16 v[192:195], v[208:211], v[160:167], v239
v_mfma_f32_16x16x32_bf16 v[144:147], a[64:67], a[48:51], v[188:191]
v_exp_f32 v212, v128
Mvaa 144,68,52
Mvaa 144,72,56
v_exp_f32 v213, v129
Mvaa 144,76,60
CV 160,212,213
v_mfma_f32_16x16x32_bf16 v[148:151], a[92:95], a[60:63], v[188:191]
v_exp_f32 v214, v130
Mvaa 148,88,56
Mvaa 148,84,52
v_exp_f32 v215, v131
v_add_f32 v192, v192, v193
Mvaa 148,80,48
CV 161,214,215
v_rcp_f32 v193, v192
v_mfma_f32_16x16x32_bf16 v[152:155], a[96:99], a[48:51], v[188:191]
Lv1stub0_fused_block:
s_cbranch_execz Lv1stub4_fused_block
v_exp_f32 v212, v132
v_mov_b32 v194, v194
Mvaa 152,100,52
v_mul_f32 v0, v0, v193
v_mul_f32 v1, v1, v193
v_mul_f32 v2, v2, v193
Mvaa 152,104,56
v_exp_f32 v213, v133
v_mul_f32 v3, v3, v193
Mvaa 152,108,60
CV 162,212,213
v_mul_f32 v4, v4, v193
v_mul_f32 v5, v5, v193
v_mfma_f32_16x16x32_bf16 v[156:159], a[124:127], a[60:63], v[188:191]
v_exp_f32 v214, v134
v_mul_f32 v6, v6, v193
Mvaa 156,120,56
v_mul_f32 v7, v7, v193
CV 0,0,1
CV 1,2,3
Mvaa 156,116,52
v_exp_f32 v215, v135
CV 2,4,5
Mvaa 156,112,48
CV 163,214,215
CV 3,6,7
v_mul_f32 v8, v8, v193
Mvav 32,192,168
buffer_store_dwordx4 v[0:3], v234, s[72:75], s20 offen offset:0 sc1
v_exp_f32 v212, v136
v_mul_f32 v9, v9, v193
Mvav 32,196,172
v_mul_f32 v10, v10, v193
v_mul_f32 v11, v11, v193
v_mul_f32 v12, v12, v193
Mvav 36,200,168
v_exp_f32 v213, v137
v_mul_f32 v13, v13, v193
Mvav 36,204,172
CV 164,212,213
v_mul_f32 v14, v14, v193
v_mul_f32 v15, v15, v193
Mvav 40,208,168
v_exp_f32 v214, v138
CV 8,8,9
Mvav 40,212,172
CV 9,10,11
CV 10,12,13
CV 11,14,15
Mvav 44,216,168
buffer_store_dwordx4 v[8:11], v234, s[72:75], s20 offen offset:1024 sc1
v_exp_f32 v215, v139
v_mul_f32 v16, v16, v193
Mvav 44,220,172
CV 165,214,215
v_mul_f32 v17, v17, v193
v_mul_f32 v18, v18, v193
Mvav 48,224,168
v_exp_f32 v212, v140
v_mul_f32 v19, v19, v193
Mvav 48,228,172
v_mul_f32 v20, v20, v193
)"
R"(v_mul_f32 v21, v21, v193
v_mul_f32 v22, v22, v193
Mvav 52,232,168
v_exp_f32 v213, v141
v_mul_f32 v23, v23, v193
Mvav 52,236,172
CV 166,212,213
CV 16,16,17
CV 17,18,19
Mvav 56,240,168
v_exp_f32 v214, v142
CV 18,20,21
Mvav 56,244,172
CV 19,22,23
v_mul_f32 v24, v24, v193
v_mul_f32 v25, v25, v193
Mvav 60,248,168
buffer_store_dwordx4 v[16:19], v234, s[72:75], s20 offen offset:2048 sc1
v_exp_f32 v215, v143
v_mul_f32 v26, v26, v193
Mvav 60,252,172
CV 167,214,215
v_mul_f32 v27, v27, v193
v_mul_f32 v28, v28, v193
v_smfmac_f32_16x16x64_bf16 v[196:199], v[208:211], v[168:175], v239
v_mul_f32 v29, v29, v193
v_mul_f32 v30, v30, v193
v_mul_f32 v31, v31, v193
Mvav 64,192,160
v_exp_f32 v212, v144
CV 24,24,25
Mvav 64,196,164
v_exp_f32 v213, v145
CV 25,26,27
Mvav 68,200,160
v_exp_f32 v214, v146
CV 168,212,213
Mvav 68,204,164
v_exp_f32 v215, v147
CV 26,28,29
Mvav 72,208,160
v_exp_f32 v212, v148
CV 169,214,215
Mvav 72,212,164
v_exp_f32 v213, v149
CV 27,30,31
Mvav 76,216,160
buffer_store_dwordx4 v[24:27], v234, s[72:75], s20 offen offset:3072 sc1
v_exp_f32 v214, v150
CV 170,212,213
Mvav 76,220,164
s_add_u32 s90, s34, 49152
GL 96,234,72,90,0
v_exp_f32 v215, v151
v_add_f32 v196, v196, v197
Mvav 80,224,160
v_exp_f32 v212, v152
CV 171,214,215
Mvav 80,228,164
GL 100,234,72,90,1024
v_exp_f32 v213, v153
Mvav 84,232,160
v_exp_f32 v214, v154
CV 172,212,213
Mvav 84,236,164
GL 104,234,72,90,2048
v_exp_f32 v215, v155
Mvav 88,240,160
v_exp_f32 v212, v156
CV 173,214,215
Mvav 88,244,164
GL 108,234,72,90,3072
v_exp_f32 v213, v157
Mvav 92,248,160
s_add_u32 s90, s34, 180224
GL 112,234,72,90,0
v_exp_f32 v214, v158
CV 174,212,213
Mvav 92,252,164
v_exp_f32 v215, v159
v_smfmac_f32_16x16x64_bf16 v[200:203], v[208:211], v[160:167], v239
GL 116,234,72,90,1024
CV 175,214,215
s_nop 0
Mvav 96,192,168
v_rcp_f32 v197, v196
v_add_f32 v192, v192, v196
Mvav 96,196,172
GL 120,234,72,90,2048
v_mul_f32 v32, v32, v197
v_mul_f32 v33, v33, v197
v_mul_f32 v34, v34, v197
Mvav 100,200,168
v_mul_f32 v35, v35, v197
v_mul_f32 v36, v36, v197
v_mul_f32 v37, v37, v197
Mvav 100,204,172
GL 124,234,72,90,3072
v_mul_f32 v38, v38, v197
v_mul_f32 v39, v39, v197
CV 32,32,33
Mvav 104,208,168
CV 33,34,35
CV 34,36,37
CV 35,38,39
Mvav 104,212,172
buffer_store_dwordx4 v[32:35], v234, s[72:75], s21 offen offset:0 sc1
TR 64,241,16384
v_mul_f32 v40, v40, v197
v_mul_f32 v41, v41, v197
Mvav 108,216,168
TR 66,241,20480
v_mul_f32 v42, v42, v197
v_mul_f32 v43, v43, v197
Mvav 108,220,172
TR 68,241,24576
v_mul_f32 v44, v44, v197
v_mul_f32 v45, v45, v197
Mvav 112,224,168
TR 70,241,28672
v_mul_f32 v46, v46, v197
v_mul_f32 v47, v47, v197
Mvav 112,228,172
TR 72,242,16384
CV 40,40,41
CV 41,42,43
Mvav 116,232,168
TR 74,242,20480
CV 42,44,45
CV 43,46,47
Mvav 116,236,172
TR 76,242,24576
v_mul_f32 v48, v48, v197
v_mul_f32 v49, v49, v197
Mvav 120,240,168
buffer_store_dwordx4 v[40:43], v234, s[72:75], s21 offen offset:1024 sc1
TR 78,242,28672
v_mul_f32 v50, v50, v197
v_mul_f32 v51, v51, v197
Mvav 120,244,172
TR 80,243,16384
v_mul_f32 v52, v52, v197
v_mul_f32 v53, v53, v197
Mvav 124,248,168
TR 82,243,20480
v_mul_f32 v54, v54, v197
v_mul_f32 v55, v55, v197
Mvav 124,252,172
TR 84,243,24576
CV 48,48,49
CV 49,50,51
v_smfmac_f32_16x16x64_bf16 v[204:207], v[208:211], v[168:175], v239
s_waitcnt vmcnt(22) lgkmcnt(7)
TR 86,243,28672
CV 50,52,53
v_mfma_f32_16x16x32_bf16 v[184:187], a[64:67], a[128:131], 0
TR 88,244,16384
CV 51,54,55
v_mul_f32 v56, v56, v197
Mvaa 184,68,132
TR 90,244,20480
v_mul_f32 v57, v57, v197
v_mul_f32 v58, v58, v197
v_mfma_f32_16x16x32_bf16 v[188:191], a[64:67], a[144:147], 0
TR 92,244,24576
v_mul_f32 v59, v59, v197
v_mul_f32 v60, v60, v197
Mvaa 188,68,148
WL 7
TR 94,244,28672
v_mul_f32 v61, v61, v197
v_mfma_f32_16x16x32_bf16 v[212:215], a[72:75], a[144:147], 0
TR 64,245,16384
v_mul_f32 v62, v62, v197
v_mul_f32 v63, v63, v197
Mvaa 212,76,148
TR 66,245,20480
CV 56,56,57
CV 57,58,59
v_mfma_f32_16x16x32_bf16 v[208:211], a[72:75], a[128:131], 0
buffer_store_dwordx4 v[48:51], v234, s[72:75], s21 offen offset:2048 sc1
TR 68,245,24576
CV 58,60,61
CV 59,62,63
Mvaa 208,76,132
WL 7
TR 70,245,28672
v_add_f32 v200, v200, v201
v_mfma_f32_16x16x32_bf16 v[128:131], a[80:83], a[128:131], 0
TR 72,246,16384
v_rcp_f32 v201, v200
Mvaa 128,84,132
TR 74,246,20480
v_add_f32 v192, v192, v200
v_mul_f32 v64, v64, v201
v_mfma_f32_16x16x32_bf16 v[132:135], a[80:83], a[144:147], 0
TR 76,246,24576
v_mul_f32 v65, v65, v201
v_mul_f32 v66, v66, v201
Mvaa 132,84,148
WL 7
TR 78,246,28672
v_mul_f32 v67, v67, v201
v_mfma_f32_16x16x32_bf16 v[140:143], a[88:91], a[144:147], 0
TR 80,247,16384
v_mul_f32 v68, v68, v201
v_mul_f32 v69, v69, v201
Mvaa 140,92,148
TR 82,247,20480
v_mul_f32 v70, v70, v201
v_mul_f32 v71, v71, v201
v_mfma_f32_16x16x32_bf16 v[136:139], a[88:91], a[128:131], 0
TR 84,247,24576
CV 64,64,65
CV 65,66,67
Mvaa 136,92,132
WL 7
TR 86,247,28672
CV 66,68,69
v_mfma_f32_16x16x32_bf16 v[152:155], a[64:67], a[128:131], 0
TR 88,248,16384
CV 67,70,71
v_mul_f32 v72, v72, v201
Mvaa 152,68,132
buffer_store_dwordx4 v[56:59], v234, s[72:75], s21 offen offset:3072 sc1
TR 90,248,20480
v_mul_f32 v73, v73, v201
v_mul_f32 v74, v74, v201
v_mfma_f32_16x16x32_bf16 v[156:159], a[64:67], a[144:147], 0
TR 92,248,24576
v_mul_f32 v75, v75, v201
v_mul_f32 v76, v76, v201
Mvaa 156,68,148
WL 7
TR 94,248,28672
v_mul_f32 v77, v77, v201
v_mfma_f32_16x16x32_bf16 v[164:167], a[72:75], a[144:147], 0
TR 64,240,4096
v_mul_f32 v78, v78, v201
v_mul_f32 v79, v79, v201
Mvaa 164,76,148
TR 66,240,4608
CV 72,72,73
CV 73,74,75
v_mfma_f32_16x16x32_bf16 v[160:163], a[72:75], a[128:131], 0
TR 68,240,5120
CV 74,76,77
CV 75,78,79
Mvaa 160,76,132
WL 7
TR 70,240,5632
v_mul_f32 v80, v80, v201
v_mfma_f32_16x16x32_bf16 v[168:171], a[80:83], a[128:131], 0
TR 72,220,16384
v_mul_f32 v81, v81, v201
v_mul_f32 v82, v82, v201
Mvaa 168,84,132
TR 74,220,20480
v_mul_f32 v83, v83, v201
v_mul_f32 v84, v84, v201
v_mfma_f32_16x16x32_bf16 v[172:175], a[80:83], a[144:147], 0
TR 76,220,24576
v_mul_f32 v85, v85, v201
v_mul_f32 v86, v86, v201
Mvaa 172,84,148
WL 7
TR 78,220,28672
v_mul_f32 v87, v87, v201
v_mfma_f32_16x16x32_bf16 v[180:183], a[88:91], a[144:147], 0
s_add_u32 m0, s84, 0
TR 80,221,16384
CV 80,80,81
CV 81,82,83
Mvaa 180,92,148
DM 235,64,87,0
TR 82,221,20480
CV 82,84,85
CV 83,86,87
v_mfma_f32_16x16x32_bf16 v[176:179], a[88:91], a[128:131], 0
TR 84,221,24576
v_mul_f32 v88, v88, v201
v_mul_f32 v89, v89, v201
Mvaa 176,92,132
WL 7
DM 236,64,87,1024
TR 86,221,28672
v_mul_f32 v90, v90, v201
v_mfma_f32_16x16x32_bf16 v[144:147], a[64:67], a[128:131], 0
TR 88,222,16384
v_mul_f32 v91, v91, v201
v_mul_f32 v92, v92, v201
Mvaa 144,68,132
TR 90,222,20480
v_mul_f32 v93, v93, v201
v_mul_f32 v94, v94, v201
v_mfma_f32_16x16x32_bf16 v[148:151], a[64:67], a[144:147], 0
TR 92,222,24576
v_mul_f32 v95, v95, v201
CV 88,88,89
Mvaa 148,68,148
WL 7
DM 237,64,87,2048
TR 94,222,28672
CV 89,90,91
Mvaa 188,72,152
TR 64,223,16384
CV 90,92,93
CV 91,94,95
Mvaa 188,76,156
TR 66,223,20480
v_add_f32 v204, v204, v205
Mvaa 184,72,136
DM 238,64,87,3072
TR 68,223,24576
v_rcp_f32 v205, v204
Mvaa 184,76,140
WL 7
TR 70,223,28672
v_add_f32 v192, v192, v204
Mvaa 208,80,136
TR 72,224,16384
v_cmp_nge_f32 vcc, 0x53800000, v192
s_lshl_b32 s96, s19, 2
s_add_u32 s96, s96, 147456
s_cmp_lg_u64 vcc, 0
s_cselect_b32 s95, 1, 0
v_mov_b32 v249, s96
v_mov_b32 v250, s95
ds_write_b32 v249, v250
v_mov_b32 v249, 147456
Mvaa 208,84,140
TR 74,224,20480
v_mul_f32 v96, v96, v205
v_mul_f32 v97, v97, v205
Mvaa 212,80,152
s_add_u32 m0, s85, 0
TR 76,224,24576
v_mul_f32 v98, v98, v205
v_mul_f32 v99, v99, v205
Mvaa 212,84,156
WL 8
DM 235,64,88,0
TR 78,224,28672
v_mul_f32 v100, v100, v205
Mvaa 132,88,152
TR 80,225,16384
v_mul_f32 v101, v101, v205
v_mul_f32 v102, v102, v205
Mvaa 132,92,156
DM 236,64,88,1024
TR 82,225,20480
v_mul_f32 v103, v103, v205
CV 96,96,97
Mvaa 128,88,136
TR 84,225,24576
CV 97,98,99
CV 98,100,101
Mvaa 128,92,140
WL 8
TR 86,225,28672
CV 99,102,103
Mvaa 136,64,136
TR 88,226,16384
v_mul_f32 v104, v104, v205
v_mul_f32 v105, v105, v205
Mvaa 136,68,140
DM 237,64,88,2048
TR 90,226,20480
v_mul_f32 v106, v106, v205
v_mul_f32 v107, v107, v205
Mvaa 140,64,152
TR 92,226,24576
v_mul_f32 v108, v108, v205
v_mul_f32 v109, v109, v205
Mvaa 140,68,156
WL 7
TR 94,226,28672
v_mul_f32 v110, v110, v205
Mvaa 156,72,152
DM 238,64,88,3072
TR 64,227,16384
v_mul_f32 v111, v111, v205
CV 104,104,105
Mvaa 156,76,156
TR 66,227,20480
CV 105,106,107
CV 106,108,109
Mvaa 152,72,136
TR 68,227,24576
CV 107,110,111
v_mul_f32 v112, v112, v205
Mvaa 152,76,140
WL 7
TR 70,227,28672
v_mul_f32 v113, v113, v205
Mvaa 160,80,136
s_add_u32 m0, s86, 0
TR 72,240,6144
v_mul_f32 v114, v114, v205
v_mul_f32 v115, v115, v205
)"
R"(Mvaa 160,84,140
buffer_load_dwordx4 v234, s[72:75], s89 offen offset:0 sc1 lds
TR 74,240,6656
v_mul_f32 v116, v116, v205
v_mul_f32 v117, v117, v205
Mvaa 164,80,152
TR 76,240,7168
v_mul_f32 v118, v118, v205
v_mul_f32 v119, v119, v205
Mvaa 164,84,156
WL 7
s_add_u32 s90, s34, 53248
GL 192,234,72,90,0
TR 78,240,7680
CV 112,112,113
Mvaa 172,88,152
WL 0
s_barrier
TR 80,241,32768
CV 113,114,115
Mvaa 172,92,156
TR 82,241,36864
CV 114,116,117
CV 115,118,119
Mvaa 168,88,136
TR 84,241,40960
ds_read_b128 v[4:7], v249
v_mul_f32 v120, v120, v205
v_mul_f32 v121, v121, v205
Mvaa 168,92,140
GL 196,234,72,90,1024
TR 86,241,45056
v_mul_f32 v122, v122, v205
Mvaa 176,64,136
TR 88,242,32768
v_mul_f32 v123, v123, v205
v_mul_f32 v124, v124, v205
Mvaa 176,68,140
TR 90,242,36864
v_mul_f32 v125, v125, v205
v_mul_f32 v126, v126, v205
Mvaa 180,64,152
GL 200,234,72,90,2048
TR 92,242,40960
v_mul_f32 v127, v127, v205
CV 120,120,121
Mvaa 180,68,156
TR 94,242,45056
CV 121,122,123
Mvaa 148,72,152
TR 64,243,32768
CV 122,124,125
CV 123,126,127
Mvaa 148,76,156
TR 66,243,36864
Mvaa 144,72,136
GL 204,234,72,90,3072
TR 68,243,40960
Mvaa 144,76,140
s_waitcnt vmcnt(29) lgkmcnt(7)
TR 70,243,45056
Mvaa 184,80,160
TR 72,244,32768
Mvaa 184,84,164
s_add_u32 s90, s34, 184320
GL 208,234,72,90,0
TR 74,244,36864
Mvaa 188,80,176
TR 76,244,40960
v_or3_b32 v249, v4, v5, v6
v_or_b32 v249, v249, v7
s_nop 0
v_readfirstlane_b32 s97, v249
s_nop 3
s_andn2_b32 s97, s97, s94
s_cbranch_scc1 Lretry_fused_block
Mvaa 188,84,180
WL 7
TR 78,244,45056
Mvaa 212,88,176
TR 80,245,32768
Mvaa 212,92,180
GL 212,234,72,90,1024
TR 82,245,36864
Mvaa 208,88,160
TR 84,245,40960
Mvaa 208,92,164
WL 7
TR 86,245,45056
Mvaa 128,64,160
GL 216,234,72,90,2048
TR 88,246,32768
Mvaa 128,68,164
TR 90,246,36864
Mvaa 132,64,176
TR 92,246,40960
Mvaa 132,68,180
WL 7
TR 94,246,45056
Mvaa 140,72,176
GL 220,234,72,90,3072
TR 64,247,32768
Mvaa 140,76,180
TR 66,247,36864
Mvaa 136,72,160
TR 68,247,40960
Mvaa 136,76,164
WL 7
buffer_store_dwordx4 v[64:67], v234, s[72:75], s22 offen offset:0 sc1
TR 70,247,45056
Mvaa 152,80,160
TR 72,248,32768
Mvaa 152,84,164
TR 74,248,36864
Mvaa 156,80,176
TR 76,248,40960
Mvaa 156,84,180
WL 7
TR 78,248,45056
Mvaa 164,88,176
TR 80,240,8192
Mvaa 164,92,180
TR 82,240,8704
Mvaa 160,88,160
TR 84,240,9216
Mvaa 160,92,164
WL 7
TR 86,240,9728
Mvaa 168,64,160
TR 88,220,32768
Mvaa 168,68,164
TR 90,220,36864
Mvaa 172,64,176
buffer_store_dwordx4 v[72:75], v234, s[72:75], s22 offen offset:1024 sc1
TR 92,220,40960
Mvaa 172,68,180
WL 7
TR 94,220,45056
Mvaa 180,72,176
TR 64,221,32768
Mvaa 180,76,180
TR 66,221,36864
Mvaa 176,72,160
TR 68,221,40960
Mvaa 176,76,164
WL 7
TR 70,221,45056
Mvaa 144,80,160
TR 72,222,32768
Mvaa 144,84,164
TR 74,222,36864
Mvaa 148,80,176
TR 76,222,40960
Mvaa 148,84,180
Lv1stub1_fused_block:
s_cbranch_execz Lv1stub5_fused_block
WL 7
TR 78,222,45056
Mvaa 188,88,184
TR 80,223,32768
Mvaa 188,92,188
buffer_store_dwordx4 v[80:83], v234, s[72:75], s22 offen offset:2048 sc1
TR 82,223,36864
Mvaa 184,88,168
TR 84,223,40960
Mvaa 184,92,172
WL 7
TR 86,223,45056
Mvaa 208,64,168
TR 88,224,32768
Mvaa 208,68,172
TR 90,224,36864
Mvaa 212,64,184
TR 92,224,40960
Mvaa 212,68,188
WL 7
TR 94,224,45056
Mvaa 132,72,184
TR 64,225,32768
Mvaa 132,76,188
TR 66,225,36864
Mvaa 128,72,168
TR 68,225,40960
Mvaa 128,76,172
WL 7
TR 70,225,45056
Mvaa 136,80,168
buffer_store_dwordx4 v[88:91], v234, s[72:75], s22 offen offset:3072 sc1
TR 72,226,32768
Mvaa 136,84,172
TR 74,226,36864
Mvaa 140,80,184
TR 76,226,40960
Mvaa 140,84,188
WL 7
TR 78,226,45056
Mvaa 156,88,184
TR 80,227,32768
Mvaa 156,92,188
TR 82,227,36864
Mvaa 152,88,168
TR 84,227,40960
Mvaa 152,92,172
WL 7
TR 86,227,45056
Mvaa 160,64,168
TR 88,240,10240
Mvaa 160,68,172
TR 90,240,10752
Mvaa 164,64,184
TR 92,240,11264
Mvaa 164,68,188
WL 7
s_add_u32 s87, s28, 2064384
s_add_u32 s88, s28, 2138112
TR 94,240,11776
Mvaa 172,72,184
WL 0
s_barrier
s_add_u32 s89, s29, 57344
TR 64,241,49152
Mvaa 172,76,188
s_add_u32 m0, s84, 16384
TR 66,241,53248
Mvaa 168,72,168
DM 235,64,87,0
TR 68,241,57344
Mvaa 168,76,172
TR 70,241,61440
Mvaa 176,80,168
DM 236,64,87,1024
TR 72,242,49152
Mvaa 176,84,172
TR 74,242,53248
Mvaa 180,80,184
DM 237,64,87,2048
TR 76,242,57344
Mvaa 180,84,188
TR 78,242,61440
Mvaa 148,88,184
TR 80,243,49152
Mvaa 148,92,188
TR 82,243,53248
Mvaa 144,88,168
DM 238,64,87,3072
TR 84,243,57344
Mvaa 144,92,172
s_waitcnt vmcnt(29) lgkmcnt(7)
TR 86,243,61440
Mvaa 184,64,96
TR 88,244,49152
Mvaa 184,68,100
s_add_u32 m0, s85, 16384
TR 90,244,53248
Mvaa 188,64,112
DM 235,64,88,0
TR 92,244,57344
Mvaa 188,68,116
WL 7
TR 94,244,61440
Mvaa 212,72,112
TR 64,245,49152
Mvaa 212,76,116
DM 236,64,88,1024
TR 66,245,53248
Mvaa 208,72,96
TR 68,245,57344
Mvaa 208,76,100
WL 7
TR 70,245,61440
Mvaa 128,80,96
DM 237,64,88,2048
TR 72,246,49152
Mvaa 128,84,100
TR 74,246,53248
Mvaa 132,80,112
TR 76,246,57344
Mvaa 132,84,116
WL 7
TR 78,246,61440
Mvaa 140,88,112
DM 238,64,88,3072
TR 80,247,49152
Mvaa 140,92,116
TR 82,247,53248
Mvaa 136,88,96
TR 84,247,57344
Mvaa 136,92,100
WL 7
s_add_u32 m0, s86, 4096
TR 86,247,61440
Mvaa 152,64,96
buffer_load_dwordx4 v234, s[72:75], s89 offen offset:0 sc1 lds
TR 88,248,49152
Mvaa 152,68,100
TR 90,248,53248
Mvaa 156,64,112
TR 92,248,57344
Mvaa 156,68,116
WL 7
s_add_u32 s90, s34, 57344
GL 224,234,72,90,0
TR 94,248,61440
Mvaa 164,72,112
TR 64,240,12288
Mvaa 164,76,116
TR 66,240,12800
Mvaa 160,72,96
GL 228,234,72,90,1024
TR 68,240,13312
Mvaa 160,76,100
WL 7
TR 70,240,13824
Mvaa 168,80,96
TR 72,220,49152
Mvaa 168,84,100
TR 74,220,53248
Mvaa 172,80,112
GL 232,234,72,90,2048
TR 76,220,57344
Mvaa 172,84,116
WL 7
TR 78,220,61440
Mvaa 180,88,112
TR 80,221,49152
Mvaa 180,92,116
GL 236,234,72,90,3072
TR 82,221,53248
Mvaa 176,88,96
TR 84,221,57344
Mvaa 176,92,100
WL 7
TR 86,221,61440
Mvaa 144,64,96
TR 88,222,49152
Mvaa 144,68,100
s_add_u32 s90, s34, 188416
GL 240,234,72,90,0
TR 90,222,53248
Mvaa 148,64,112
TR 92,222,57344
Mvaa 148,68,116
WL 7
TR 94,222,61440
Mvaa 188,72,120
GL 244,234,72,90,1024
TR 64,223,49152
Mvaa 188,76,124
TR 66,223,53248
Mvaa 184,72,104
TR 68,223,57344
Mvaa 184,76,108
WL 7
TR 70,223,61440
Mvaa 208,80,104
GL 248,234,72,90,2048
TR 72,224,49152
Mvaa 208,84,108
TR 74,224,53248
Mvaa 212,80,120
TR 76,224,57344
Mvaa 212,84,124
WL 7
GL 252,234,72,90,3072
TR 78,224,61440
Mvaa 132,88,120
TR 80,225,49152
Mvaa 132,92,124
TR 82,225,53248
Mvaa 128,88,104
TR 84,225,57344
Mvaa 128,92,108
WL 7
buffer_store_dwordx4 v[96:99], v234, s[72:75], s23 offen offset:0 sc1
TR 86,225,61440
Mvaa 136,64,104
TR 88,226,49152
Mvaa 136,68,108
TR 90,226,53248
Mvaa 140,64,120
TR 92,226,57344
Mvaa 140,68,124
WL 7
TR 94,226,61440
Mvaa 156,72,120
TR 64,227,49152
Mvaa 156,76,124
TR 66,227,53248
Mvaa 152,72,104
TR 68,227,57344
Mvaa 152,76,108
WL 7
TR 70,227,61440
Mvaa 160,80,104
TR 72,240,14336
Mvaa 160,84,108
TR 74,240,14848
Mvaa 164,80,120
buffer_store_dwordx4 v[104:107], v234, s[72:75], s23 offen offset:1024 sc1
TR 76,240,15360
)"
R"(Mvaa 164,84,124
WL 7
TR 78,240,15872
Mvaa 172,88,120
s_waitcnt vmcnt(31) lgkmcnt(0)
s_barrier
TR 80,241,0
Mvaa 172,92,124
TR 82,241,4096
Mvaa 168,88,104
TR 84,241,8192
Mvaa 168,92,108
TR 86,241,12288
Mvaa 176,64,104
TR 88,242,0
Mvaa 176,68,108
TR 90,242,4096
Mvaa 180,64,120
TR 92,242,8192
Mvaa 180,68,124
TR 94,242,12288
Mvaa 148,72,120
TR 64,243,0
Mvaa 148,76,124
buffer_store_dwordx4 v[112:115], v234, s[72:75], s23 offen offset:2048 sc1
TR 66,243,4096
Mvaa 144,72,104
TR 68,243,8192
Mvaa 144,76,108
s_waitcnt vmcnt(24) lgkmcnt(7)
TR 70,243,12288
Mvaa 184,80,192
TR 72,244,0
Mvaa 184,84,196
TR 74,244,4096
Mvaa 188,80,208
TR 76,244,8192
Mvaa 188,84,212
WL 7
TR 78,244,12288
Mvaa 212,88,208
TR 80,245,0
Mvaa 212,92,212
TR 82,245,4096
Mvaa 208,88,192
TR 84,245,8192
Mvaa 208,92,196
WL 7
TR 86,245,12288
Mvaa 128,64,192
buffer_store_dwordx4 v[120:123], v234, s[72:75], s23 offen offset:3072 sc1
TR 88,246,0
Mvaa 128,68,196
TR 90,246,4096
Mvaa 132,64,208
TR 92,246,8192
Mvaa 132,68,212
WL 7
TR 94,246,12288
Mvaa 140,72,208
TR 64,247,0
Mvaa 140,76,212
TR 66,247,4096
Mvaa 136,72,192
TR 68,247,8192
Mvaa 136,76,196
WL 7
TR 70,247,12288
Mvaa 152,80,192
TR 72,248,0
Mvaa 152,84,196
TR 74,248,4096
Mvaa 156,80,208
TR 76,248,8192
Mvaa 156,84,212
WL 7
s_add_u32 s87, s28, 2211840
s_add_u32 s88, s28, 2285568
TR 78,248,12288
Mvaa 164,88,208
s_add_u32 s89, s29, 61440
s_add_u32 m0, s84, 32768
TR 80,240,0
Mvaa 164,92,212
DM 235,64,87,0
TR 82,240,512
Mvaa 160,88,192
TR 84,240,1024
Mvaa 160,92,196
WL 7
DM 236,64,87,1024
TR 86,240,1536
Mvaa 168,64,192
TR 88,220,0
Mvaa 168,68,196
TR 90,220,4096
Mvaa 172,64,208
DM 237,64,87,2048
TR 92,220,8192
Mvaa 172,68,212
WL 7
TR 94,220,12288
Mvaa 180,72,208
TR 64,221,0
Mvaa 180,76,212
DM 238,64,87,3072
TR 66,221,4096
Mvaa 176,72,192
TR 68,221,8192
Mvaa 176,76,196
WL 7
TR 70,221,12288
Mvaa 144,80,192
TR 72,222,0
Mvaa 144,84,196
s_add_u32 m0, s85, 32768
TR 74,222,4096
Mvaa 148,80,208
DM 235,64,88,0
TR 76,222,8192
Mvaa 148,84,212
WL 7
TR 78,222,12288
Mvaa 188,88,216
DM 236,64,88,1024
TR 80,223,0
Mvaa 188,92,220
TR 82,223,4096
Mvaa 184,88,200
TR 84,223,8192
Mvaa 184,92,204
WL 7
TR 86,223,12288
Mvaa 208,64,200
DM 237,64,88,2048
TR 88,224,0
Mvaa 208,68,204
TR 90,224,4096
Mvaa 212,64,216
TR 92,224,8192
Mvaa 212,68,220
WL 7
DM 238,64,88,3072
TR 94,224,12288
Mvaa 132,72,216
TR 64,225,0
Mvaa 132,76,220
TR 66,225,4096
Mvaa 128,72,200
TR 68,225,8192
Mvaa 128,76,204
WL 7
s_add_u32 m0, s86, 8192
TR 70,225,12288
Mvaa 136,80,200
buffer_load_dwordx4 v234, s[72:75], s89 offen offset:0 sc1 lds
TR 72,226,0
Mvaa 136,84,204
TR 74,226,4096
Mvaa 140,80,216
s_add_u32 s90, s34, 61440
GL 128,234,72,90,0
TR 76,226,8192
Mvaa 140,84,220
WL 7
TR 78,226,12288
Mvaa 156,88,216
TR 80,227,0
Mvaa 156,92,220
TR 82,227,4096
Mvaa 152,88,200
GL 132,234,72,90,1024
TR 84,227,8192
Mvaa 152,92,204
WL 7
TR 86,227,12288
Mvaa 160,64,200
TR 88,240,2048
Mvaa 160,68,204
GL 136,234,72,90,2048
TR 90,240,2560
Mvaa 164,64,216
TR 92,240,3072
Mvaa 164,68,220
WL 7
TR 94,240,3584
Mvaa 172,72,216
s_waitcnt vmcnt(24) lgkmcnt(0)
s_barrier
TR 64,241,16384
Mvaa 172,76,220
GL 140,234,72,90,3072
TR 66,241,20480
Mvaa 168,72,200
TR 68,241,24576
Mvaa 168,76,204
TR 70,241,28672
Mvaa 176,80,200
s_add_u32 s90, s34, 192512
GL 144,234,72,90,0
TR 72,242,16384
Mvaa 176,84,204
TR 74,242,20480
Mvaa 180,80,216
TR 76,242,24576
Mvaa 180,84,220
TR 78,242,28672
Mvaa 148,88,216
GL 148,234,72,90,1024
TR 80,243,16384
Mvaa 148,92,220
TR 82,243,20480
Mvaa 144,88,200
TR 84,243,24576
Mvaa 144,92,204
s_waitcnt vmcnt(19) lgkmcnt(7)
GL 152,234,72,90,2048
TR 86,243,28672
Mvaa 184,64,224
TR 88,244,16384
Mvaa 184,68,228
TR 90,244,20480
Mvaa 188,64,240
TR 92,244,24576
Mvaa 188,68,244
WL 7
GL 156,234,72,90,3072
TR 94,244,28672
Mvaa 212,72,240
TR 64,245,16384
Mvaa 212,76,244
TR 66,245,20480
Mvaa 208,72,224
s_waitcnt vmcnt(17)
s_add_u32 s98, s37, 262144
TR 68,245,24576
v_mov_b32 v250, s82
Mvaa 208,76,228
WL 7
TR 70,245,28672
v_mov_b32 v251, s83
Mvaa 128,80,224
TR 72,246,16384
v_lshlrev_b32 v249, 3, v255
s_mov_b64 exec, 0xffff
buffer_store_dwordx2 v[250:251], v249, s[72:75], s98 offen sc1
s_mov_b64 exec, -1
Mvaa 128,84,228
s_add_u32 s87, s28, 2359296
s_add_u32 s88, s28, 2433024
s_add_u32 s89, s29, 65536
TR 74,246,20480
Mvaa 132,80,240
s_add_u32 m0, s84, 49152
TR 76,246,24576
Mvaa 132,84,244
WL 7
DM 235,64,87,0
TR 78,246,28672
Mvaa 140,88,240
TR 80,247,16384
Mvaa 140,92,244
DM 236,64,87,1024
TR 82,247,20480
Mvaa 136,88,224
TR 84,247,24576
Mvaa 136,92,228
WL 7
DM 237,64,87,2048
TR 86,247,28672
Mvaa 152,64,224
TR 88,248,16384
Mvaa 152,68,228
DM 238,64,87,3072
TR 90,248,20480
Mvaa 156,64,240
TR 92,248,24576
Mvaa 156,68,244
WL 7
TR 94,248,28672
Mvaa 164,72,240
s_add_u32 m0, s85, 49152
TR 64,240,4096
Mvaa 164,76,244
DM 235,64,88,0
TR 66,240,4608
Mvaa 160,72,224
TR 68,240,5120
Mvaa 160,76,228
WL 7
TR 70,240,5632
Mvaa 168,80,224
DM 236,64,88,1024
TR 72,220,16384
Mvaa 168,84,228
Lv1stub2_fused_block:
s_cbranch_execz Lv1stub6_fused_block
TR 74,220,20480
Mvaa 172,80,240
TR 76,220,24576
Mvaa 172,84,244
WL 7
DM 237,64,88,2048
TR 78,220,28672
Mvaa 180,88,240
TR 80,221,16384
Mvaa 180,92,244
TR 82,221,20480
Mvaa 176,88,224
TR 84,221,24576
Mvaa 176,92,228
WL 7
DM 238,64,88,3072
TR 86,221,28672
Mvaa 144,64,224
TR 88,222,16384
Mvaa 144,68,228
TR 90,222,20480
Mvaa 148,64,240
s_add_u32 m0, s86, 12288
TR 92,222,24576
Mvaa 148,68,244
WL 7
buffer_load_dwordx4 v234, s[72:75], s89 offen offset:0 sc1 lds
TR 94,222,28672
Mvaa 188,72,248
TR 64,223,16384
Mvaa 188,76,252
TR 66,223,20480
Mvaa 184,72,232
s_add_u32 s90, s34, 65536
GL 160,234,72,90,0
TR 68,223,24576
Mvaa 184,76,236
WL 7
TR 70,223,28672
Mvaa 208,80,232
TR 72,224,16384
Mvaa 208,84,236
GL 164,234,72,90,1024
TR 74,224,20480
Mvaa 212,80,248
TR 76,224,24576
Mvaa 212,84,252
WL 7
TR 78,224,28672
Mvaa 132,88,248
TR 80,225,16384
Mvaa 132,92,252
GL 168,234,72,90,2048
TR 82,225,20480
Mvaa 128,88,232
TR 84,225,24576
Mvaa 128,92,236
WL 7
TR 86,225,28672
Mvaa 136,64,232
GL 172,234,72,90,3072
TR 88,226,16384
Mvaa 136,68,236
TR 90,226,20480
Mvaa 140,64,248
TR 92,226,24576
Mvaa 140,68,252
WL 7
TR 94,226,28672
Mvaa 156,72,248
s_add_u32 s90, s34, 196608
GL 176,234,72,90,0
TR 64,227,16384
Mvaa 156,76,252
TR 66,227,20480
Mvaa 152,72,232
TR 68,227,24576
Mvaa 152,76,236
WL 7
GL 180,234,72,90,1024
TR 70,227,28672
Mvaa 160,80,232
TR 72,240,6144
Mvaa 160,84,236
TR 74,240,6656
Mvaa 164,80,248
TR 76,240,7168
Mvaa 164,84,252
WL 7
GL 184,234,72,90,2048
TR 78,240,7680
Mvaa 172,88,248
s_waitcnt vmcnt(25) lgkmcnt(0)
s_barrier
TR 80,241,32768
Mvaa 172,92,252
TR 82,241,36864
Mvaa 168,88,232
GL 188,234,72,90,3072
TR 84,241,40960
Mvaa 168,92,236
TR 86,241,45056
Mvaa 176,64,232
)"
R"(TR 88,242,32768
Mvaa 176,68,236
TR 90,242,36864
Mvaa 180,64,248
s_add_u32 s87, s28, 2506752
s_add_u32 s88, s28, 2580480
s_add_u32 s89, s29, 69632
TR 92,242,40960
Mvaa 180,68,252
s_add_u32 m0, s84, 0
TR 94,242,45056
Mvaa 148,72,248
DM 235,64,87,0
TR 64,243,32768
Mvaa 148,76,252
TR 66,243,36864
Mvaa 144,72,232
DM 236,64,87,1024
TR 68,243,40960
Mvaa 144,76,236
s_waitcnt vmcnt(20) lgkmcnt(7)
TR 70,243,45056
Mvaa 184,80,128
TR 72,244,32768
Mvaa 184,84,132
DM 237,64,87,2048
TR 74,244,36864
Mvaa 188,80,144
TR 76,244,40960
Mvaa 188,84,148
WL 7
TR 78,244,45056
Mvaa 212,88,144
DM 238,64,87,3072
TR 80,245,32768
Mvaa 212,92,148
TR 82,245,36864
Mvaa 208,88,128
TR 84,245,40960
Mvaa 208,92,132
WL 7
TR 86,245,45056
Mvaa 128,64,128
s_add_u32 m0, s85, 0
TR 88,246,32768
Mvaa 128,68,132
DM 235,64,88,0
TR 90,246,36864
Mvaa 132,64,144
TR 92,246,40960
Mvaa 132,68,148
WL 7
DM 236,64,88,1024
TR 94,246,45056
Mvaa 140,72,144
TR 64,247,32768
Mvaa 140,76,148
TR 66,247,36864
Mvaa 136,72,128
TR 68,247,40960
Mvaa 136,76,132
WL 7
DM 237,64,88,2048
TR 70,247,45056
Mvaa 152,80,128
TR 72,248,32768
Mvaa 152,84,132
TR 74,248,36864
Mvaa 156,80,144
DM 238,64,88,3072
TR 76,248,40960
Mvaa 156,84,148
WL 7
TR 78,248,45056
Mvaa 164,88,144
TR 80,240,8192
Mvaa 164,92,148
TR 82,240,8704
Mvaa 160,88,128
s_add_u32 m0, s86, 0
TR 84,240,9216
Mvaa 160,92,132
WL 7
buffer_load_dwordx4 v234, s[72:75], s89 offen offset:0 sc1 lds
TR 86,240,9728
Mvaa 168,64,128
TR 88,220,32768
Mvaa 168,68,132
s_add_u32 s90, s34, 69632
GL 96,234,72,90,0
TR 90,220,36864
Mvaa 172,64,144
TR 92,220,40960
Mvaa 172,68,148
WL 7
TR 94,220,45056
Mvaa 180,72,144
TR 64,221,32768
Mvaa 180,76,148
GL 100,234,72,90,1024
TR 66,221,36864
Mvaa 176,72,128
TR 68,221,40960
Mvaa 176,76,132
WL 7
TR 70,221,45056
Mvaa 144,80,128
GL 104,234,72,90,2048
TR 72,222,32768
Mvaa 144,84,132
TR 74,222,36864
Mvaa 148,80,144
TR 76,222,40960
Mvaa 148,84,148
WL 7
TR 78,222,45056
Mvaa 188,88,152
GL 108,234,72,90,3072
TR 80,223,32768
Mvaa 188,92,156
TR 82,223,36864
Mvaa 184,88,136
TR 84,223,40960
Mvaa 184,92,140
WL 7
s_add_u32 s90, s34, 200704
GL 112,234,72,90,0
TR 86,223,45056
Mvaa 208,64,136
TR 88,224,32768
Mvaa 208,68,140
TR 90,224,36864
Mvaa 212,64,152
TR 92,224,40960
Mvaa 212,68,156
WL 7
GL 116,234,72,90,1024
TR 94,224,45056
Mvaa 132,72,152
TR 64,225,32768
Mvaa 132,76,156
TR 66,225,36864
Mvaa 128,72,136
GL 120,234,72,90,2048
TR 68,225,40960
Mvaa 128,76,140
WL 7
TR 70,225,45056
Mvaa 136,80,136
TR 72,226,32768
Mvaa 136,84,140
TR 74,226,36864
Mvaa 140,80,152
GL 124,234,72,90,3072
TR 76,226,40960
Mvaa 140,84,156
WL 7
TR 78,226,45056
Mvaa 156,88,152
TR 80,227,32768
Mvaa 156,92,156
s_add_u32 s87, s28, 2654208
s_add_u32 s88, s28, 2727936
s_add_u32 s89, s29, 73728
TR 82,227,36864
Mvaa 152,88,136
s_add_u32 m0, s84, 16384
TR 84,227,40960
Mvaa 152,92,140
WL 7
DM 235,64,87,0
TR 86,227,45056
Mvaa 160,64,136
TR 88,240,10240
Mvaa 160,68,140
DM 236,64,87,1024
TR 90,240,10752
Mvaa 164,64,152
TR 92,240,11264
Mvaa 164,68,156
WL 7
TR 94,240,11776
Mvaa 172,72,152
s_waitcnt vmcnt(27) lgkmcnt(0)
s_barrier
DM 237,64,87,2048
TR 64,241,49152
Mvaa 172,76,156
TR 66,241,53248
Mvaa 168,72,136
TR 68,241,57344
Mvaa 168,76,140
TR 70,241,61440
Mvaa 176,80,136
DM 238,64,87,3072
TR 72,242,49152
Mvaa 176,84,140
TR 74,242,53248
Mvaa 180,80,152
TR 76,242,57344
Mvaa 180,84,156
s_add_u32 m0, s85, 16384
TR 78,242,61440
Mvaa 148,88,152
DM 235,64,88,0
TR 80,243,49152
Mvaa 148,92,156
TR 82,243,53248
Mvaa 144,88,136
TR 84,243,57344
Mvaa 144,92,140
s_waitcnt vmcnt(22) lgkmcnt(7)
DM 236,64,88,1024
TR 86,243,61440
Mvaa 184,64,160
TR 88,244,49152
Mvaa 184,68,164
TR 90,244,53248
Mvaa 188,64,176
DM 237,64,88,2048
TR 92,244,57344
Mvaa 188,68,180
WL 7
TR 94,244,61440
Mvaa 212,72,176
TR 64,245,49152
Mvaa 212,76,180
TR 66,245,53248
Mvaa 208,72,160
DM 238,64,88,3072
TR 68,245,57344
Mvaa 208,76,164
WL 7
TR 70,245,61440
Mvaa 128,80,160
TR 72,246,49152
Mvaa 128,84,164
s_add_u32 m0, s86, 4096
TR 74,246,53248
Mvaa 132,80,176
buffer_load_dwordx4 v234, s[72:75], s89 offen offset:0 sc1 lds
TR 76,246,57344
Mvaa 132,84,180
WL 7
TR 78,246,61440
Mvaa 140,88,176
TR 80,247,49152
Mvaa 140,92,180
s_add_u32 s90, s34, 73728
GL 192,234,72,90,0
TR 82,247,53248
Mvaa 136,88,160
TR 84,247,57344
Mvaa 136,92,164
WL 7
TR 86,247,61440
Mvaa 152,64,160
GL 196,234,72,90,1024
TR 88,248,49152
Mvaa 152,68,164
TR 90,248,53248
Mvaa 156,64,176
TR 92,248,57344
Mvaa 156,68,180
WL 7
TR 94,248,61440
Mvaa 164,72,176
GL 200,234,72,90,2048
TR 64,240,12288
Mvaa 164,76,180
TR 66,240,12800
Mvaa 160,72,160
TR 68,240,13312
Mvaa 160,76,164
WL 7
GL 204,234,72,90,3072
TR 70,240,13824
Mvaa 168,80,160
TR 72,220,49152
Mvaa 168,84,164
TR 74,220,53248
Mvaa 172,80,176
TR 76,220,57344
Mvaa 172,84,180
WL 7
s_add_u32 s90, s34, 204800
GL 208,234,72,90,0
TR 78,220,61440
Mvaa 180,88,176
TR 80,221,49152
Mvaa 180,92,180
TR 82,221,53248
Mvaa 176,88,160
GL 212,234,72,90,1024
TR 84,221,57344
Mvaa 176,92,164
WL 7
TR 86,221,61440
Mvaa 144,64,160
TR 88,222,49152
Mvaa 144,68,164
TR 90,222,53248
Mvaa 148,64,176
GL 216,234,72,90,2048
TR 92,222,57344
Mvaa 148,68,180
WL 7
TR 94,222,61440
Mvaa 188,72,184
TR 64,223,49152
Mvaa 188,76,188
GL 220,234,72,90,3072
TR 66,223,53248
Mvaa 184,72,168
TR 68,223,57344
Mvaa 184,76,172
WL 7
TR 70,223,61440
Mvaa 208,80,168
TR 72,224,49152
Mvaa 208,84,172
s_add_u32 s98, s36, 263168
TR 74,224,53248
v_and_b32 v249, 3, v255
Mvaa 212,80,184
TR 76,224,57344
v_lshlrev_b32 v249, 7, v249
Mvaa 212,84,188
WL 7
buffer_load_dwordx2 v[250:251], v249, s[72:75], s98 offen sc1
TR 78,224,61440
Mvaa 132,88,184
s_add_u32 s87, s28, 4866048
s_add_u32 s88, s28, 4939776
s_add_u32 s89, s29, 135168
TR 80,225,49152
Mvaa 132,92,188
s_add_u32 m0, s84, 32768
TR 82,225,53248
Mvaa 128,88,168
DM 235,64,87,0
TR 84,225,57344
Mvaa 128,92,172
WL 7
TR 86,225,61440
Mvaa 136,64,168
DM 236,64,87,1024
TR 88,226,49152
Mvaa 136,68,172
TR 90,226,53248
Mvaa 140,64,184
DM 237,64,87,2048
TR 92,226,57344
Mvaa 140,68,188
WL 7
TR 94,226,61440
Mvaa 156,72,184
TR 64,227,49152
Mvaa 156,76,188
DM 238,64,87,3072
TR 66,227,53248
Mvaa 152,72,168
TR 68,227,57344
Mvaa 152,76,172
WL 7
TR 70,227,61440
Mvaa 160,80,168
s_add_u32 m0, s85, 32768
TR 72,240,14336
Mvaa 160,84,172
DM 235,64,88,0
TR 74,240,14848
Mvaa 164,80,184
TR 76,240,15360
Mvaa 164,84,188
WL 7
TR 78,240,15872
Mvaa 172,88,184
s_waitcnt vmcnt(31) lgkmcnt(0)
s_barrier
DM 236,64,88,1024
TR 80,241,0
Mvaa 172,92,188
TR 82,241,4096
Mvaa 168,88,168
TR 84,241,8192
Mvaa 168,92,172
DM 237,64,88,2048
TR 86,241,12288
Mvaa 176,64,168
TR 88,242,0
Mvaa 176,68,172
TR 90,242,4096
Mvaa 180,64,184
TR 92,242,8192
Mvaa 180,68,188
DM 238,64,88,3072
TR 94,242,12288
Mvaa 148,72,184
TR 64,243,0
)"
R"(Mvaa 148,76,188
TR 66,243,4096
Mvaa 144,72,168
s_add_u32 m0, s86, 8192
TR 68,243,8192
Mvaa 144,76,172
s_waitcnt vmcnt(26) lgkmcnt(7)
buffer_load_dwordx4 v234, s[72:75], s89 offen offset:0 sc1 lds
TR 70,243,12288
Mvaa 184,80,96
TR 72,244,0
Mvaa 184,84,100
TR 74,244,4096
Mvaa 188,80,112
s_add_u32 s90, s33, 4096
GL 224,234,72,90,0
TR 76,244,8192
Mvaa 188,84,116
WL 7
TR 78,244,12288
Mvaa 212,88,112
TR 80,245,0
Mvaa 212,92,116
GL 228,234,72,90,1024
TR 82,245,4096
Mvaa 208,88,96
TR 84,245,8192
Mvaa 208,92,100
WL 7
TR 86,245,12288
Mvaa 128,64,96
TR 88,246,0
Mvaa 128,68,100
GL 232,234,72,90,2048
TR 90,246,4096
Mvaa 132,64,112
Lv1stub3_fused_block:
s_cbranch_execz Lt2x_fused_block
TR 92,246,8192
Mvaa 132,68,116
WL 7
TR 94,246,12288
Mvaa 140,72,112
GL 236,234,72,90,3072
TR 64,247,0
Mvaa 140,76,116
TR 66,247,4096
Mvaa 136,72,96
TR 68,247,8192
Mvaa 136,76,100
WL 7
TR 70,247,12288
Mvaa 152,80,96
s_add_u32 s90, s33, 20480
GL 240,234,72,90,0
TR 72,248,0
Mvaa 152,84,100
TR 74,248,4096
Mvaa 156,80,112
TR 76,248,8192
Mvaa 156,84,116
WL 7
GL 244,234,72,90,1024
TR 78,248,12288
Mvaa 164,88,112
TR 80,240,0
Mvaa 164,92,116
TR 82,240,512
Mvaa 160,88,96
TR 84,240,1024
Mvaa 160,92,100
WL 7
GL 248,234,72,90,2048
TR 86,240,1536
Mvaa 168,64,96
TR 88,220,0
Mvaa 168,68,100
TR 90,220,4096
Mvaa 172,64,112
GL 252,234,72,90,3072
TR 92,220,8192
Mvaa 172,68,116
WL 7
TR 94,220,12288
Mvaa 180,72,112
TR 64,221,0
Mvaa 180,76,116
TR 66,221,4096
Mvaa 176,72,96
s_add_u32 s87, s28, 1179648
s_add_u32 s88, s28, 1253376
s_add_u32 s89, s29, 180224
TR 68,221,8192
Mvaa 176,76,100
WL 7
s_add_u32 m0, s84, 49152
TR 70,221,12288
Mvaa 144,80,96
DM 235,56,87,0
TR 72,222,0
Mvaa 144,84,100
TR 74,222,4096
Mvaa 148,80,112
DM 236,56,87,1024
TR 76,222,8192
Mvaa 148,84,116
WL 7
TR 78,222,12288
Mvaa 188,88,120
TR 80,223,0
Mvaa 188,92,124
DM 237,56,87,2048
TR 82,223,4096
Mvaa 184,88,104
TR 84,223,8192
Mvaa 184,92,108
WL 7
TR 86,223,12288
Mvaa 208,64,104
DM 238,56,87,3072
TR 88,224,0
Mvaa 208,68,108
TR 90,224,4096
Mvaa 212,64,120
TR 92,224,8192
Mvaa 212,68,124
WL 7
TR 94,224,12288
Mvaa 132,72,120
s_add_u32 m0, s85, 49152
TR 64,225,0
Mvaa 132,76,124
DM 235,56,88,0
TR 66,225,4096
Mvaa 128,72,104
TR 68,225,8192
Mvaa 128,76,108
WL 7
DM 236,56,88,1024
TR 70,225,12288
Mvaa 136,80,104
TR 72,226,0
Mvaa 136,84,108
TR 74,226,4096
Mvaa 140,80,120
TR 76,226,8192
Mvaa 140,84,124
WL 7
DM 237,56,88,2048
TR 78,226,12288
Mvaa 156,88,120
TR 80,227,0
Mvaa 156,92,124
TR 82,227,4096
Mvaa 152,88,104
DM 238,56,88,3072
TR 84,227,8192
Mvaa 152,92,108
WL 7
TR 86,227,12288
Mvaa 160,64,104
TR 88,240,2048
Mvaa 160,68,108
TR 90,240,2560
Mvaa 164,64,120
s_add_u32 m0, s86, 12288
TR 92,240,3072
Mvaa 164,68,124
WL 7
buffer_load_dwordx4 v234, s[72:75], s89 offen offset:0 sc1 lds
TR 94,240,3584
Mvaa 172,72,120
s_waitcnt vmcnt(35) lgkmcnt(0)
s_barrier
TR 64,241,16384
Mvaa 172,76,124
s_waitcnt vmcnt(26)
TR 66,241,20480
v_cmp_ne_u64 vcc, s[82:83], v[250:251]
s_nop 3
s_cbranch_vccnz Lslow3_fused_block
Lslowb3_fused_block:
Mvaa 168,72,104
s_add_u32 s90, s34, 16809984
GL 128,234,72,90,0
TR 68,241,24576
Mvaa 168,76,108
TR 70,241,28672
Mvaa 176,80,104
TR 72,242,16384
Mvaa 176,84,108
GL 132,234,72,90,1024
TR 74,242,20480
Mvaa 180,80,120
TR 76,242,24576
Mvaa 180,84,124
TR 78,242,28672
Mvaa 148,88,120
GL 136,234,72,90,2048
TR 80,243,16384
Mvaa 148,92,124
TR 82,243,20480
Mvaa 144,88,104
TR 84,243,24576
Mvaa 144,92,108
WL 7
TR 86,243,28672
Mvaa 184,64,192
GL 140,234,72,90,3072
TR 88,244,16384
Mvaa 184,68,196
TR 90,244,20480
Mvaa 188,64,208
TR 92,244,24576
Mvaa 188,68,212
WL 7
s_add_u32 s90, s34, 16941056
GL 144,234,72,90,0
TR 94,244,28672
Mvaa 212,72,208
TR 64,245,16384
Mvaa 212,76,212
TR 66,245,20480
Mvaa 208,72,192
TR 68,245,24576
Mvaa 208,76,196
WL 7
GL 148,234,72,90,1024
TR 70,245,28672
Mvaa 128,80,192
TR 72,246,16384
Mvaa 128,84,196
TR 74,246,20480
Mvaa 132,80,208
GL 152,234,72,90,2048
TR 76,246,24576
Mvaa 132,84,212
WL 7
TR 78,246,28672
Mvaa 140,88,208
TR 80,247,16384
Mvaa 140,92,212
TR 82,247,20480
Mvaa 136,88,192
GL 156,234,72,90,3072
TR 84,247,24576
Mvaa 136,92,196
WL 7
TR 86,247,28672
Mvaa 152,64,192
TR 88,248,16384
Mvaa 152,68,196
s_add_u32 s87, s28, 1327104
s_add_u32 s88, s28, 1400832
s_add_u32 s89, s29, 184320
TR 90,248,20480
Mvaa 156,64,208
s_add_u32 m0, s84, 0
TR 92,248,24576
Mvaa 156,68,212
WL 7
DM 235,56,87,0
TR 94,248,28672
Mvaa 164,72,208
TR 64,240,4096
Mvaa 164,76,212
DM 236,56,87,1024
TR 66,240,4608
Mvaa 160,72,192
TR 68,240,5120
Mvaa 160,76,196
WL 7
TR 70,240,5632
Mvaa 168,80,192
DM 237,56,87,2048
TR 72,220,16384
Mvaa 168,84,196
TR 74,220,20480
Mvaa 172,80,208
TR 76,220,24576
Mvaa 172,84,212
WL 7
TR 78,220,28672
Mvaa 180,88,208
DM 238,56,87,3072
TR 80,221,16384
Mvaa 180,92,212
TR 82,221,20480
Mvaa 176,88,192
TR 84,221,24576
Mvaa 176,92,196
WL 7
s_add_u32 m0, s85, 0
TR 86,221,28672
Mvaa 144,64,192
DM 235,56,88,0
TR 88,222,16384
Mvaa 144,68,196
TR 90,222,20480
Mvaa 148,64,208
TR 92,222,24576
Mvaa 148,68,212
WL 7
DM 236,56,88,1024
TR 94,222,28672
Mvaa 188,72,216
TR 64,223,16384
Mvaa 188,76,220
TR 66,223,20480
Mvaa 184,72,200
DM 237,56,88,2048
TR 68,223,24576
Mvaa 184,76,204
WL 7
TR 70,223,28672
Mvaa 208,80,200
TR 72,224,16384
Mvaa 208,84,204
TR 74,224,20480
Mvaa 212,80,216
DM 238,56,88,3072
TR 76,224,24576
Mvaa 212,84,220
WL 7
TR 78,224,28672
Mvaa 132,88,216
TR 80,225,16384
Mvaa 132,92,220
s_add_u32 m0, s86, 0
TR 82,225,20480
Mvaa 128,88,200
buffer_load_dwordx4 v234, s[72:75], s89 offen offset:0 sc1 lds
TR 84,225,24576
Mvaa 128,92,204
WL 7
TR 86,225,28672
Mvaa 136,64,200
TR 88,226,16384
Mvaa 136,68,204
s_add_u32 s90, s34, 16814080
GL 160,234,72,90,0
TR 90,226,20480
Mvaa 140,64,216
TR 92,226,24576
Mvaa 140,68,220
WL 7
TR 94,226,28672
Mvaa 156,72,216
GL 164,234,72,90,1024
TR 64,227,16384
Mvaa 156,76,220
TR 66,227,20480
Mvaa 152,72,200
TR 68,227,24576
Mvaa 152,76,204
WL 7
TR 70,227,28672
Mvaa 160,80,200
GL 168,234,72,90,2048
TR 72,240,6144
Mvaa 160,84,204
TR 74,240,6656
Mvaa 164,80,216
TR 76,240,7168
Mvaa 164,84,220
WL 7
GL 172,234,72,90,3072
TR 78,240,7680
Mvaa 172,88,216
s_waitcnt vmcnt(38) lgkmcnt(0)
s_barrier
TR 80,241,32768
Mvaa 172,92,220
TR 82,241,36864
Mvaa 168,88,200
TR 84,241,40960
Mvaa 168,92,204
s_add_u32 s90, s34, 16945152
GL 176,234,72,90,0
TR 86,241,45056
Mvaa 176,64,200
TR 88,242,32768
Mvaa 176,68,204
TR 90,242,36864
Mvaa 180,64,216
GL 180,234,72,90,1024
TR 92,242,40960
Mvaa 180,68,220
TR 94,242,45056
Mvaa 148,72,216
TR 64,243,32768
Mvaa 148,76,220
TR 66,243,36864
Mvaa 144,72,200
GL 184,234,72,90,2048
TR 68,243,40960
Mvaa 144,76,204
s_waitcnt vmcnt(33) lgkmcnt(7)
TR 70,243,45056
Mvaa 184,80,224
)"
R"(TR 72,244,32768
v_lshrrev_b32 v79, 4, v255
v_and_b32 v78, 7, v255
Mvaa 184,84,228
GL 188,234,72,90,3072
TR 74,244,36864
v_lshl_add_u32 v78, v79, 3, v78
v_lshlrev_b32 v72, 3, v78
Mvaa 188,80,240
TR 76,244,40960
v_and_b32 v78, 15, v255
v_mul_u32_u24 v77, 1152, v78
Mvaa 188,84,244
WL 7
TR 78,244,45056
v_lshl_add_u32 v75, v79, 3, v77
Mvaa 212,88,240
s_lshl_b32 s95, s80, 5
s_add_u32 s95, s95, 1024
TR 80,245,32768
v_add_u32 v76, s95, v75
Mvaa 212,92,244
s_add_u32 s87, s28, 1474560
s_add_u32 s88, s28, 1548288
s_add_u32 s89, s29, 188416
TR 82,245,36864
v_cmp_gt_u32 vcc, 8, v78
v_mov_b32 v78, 0x7ffffff0
s_nop 0
v_cndmask_b32 v73, v72, v78, vcc
v_cndmask_b32 v72, v78, v72, vcc
v_cndmask_b32 v76, v76, v78, vcc
Mvaa 208,88,224
s_add_u32 m0, s84, 16384
TR 84,245,40960
v_mov_b32 v74, v73
v_and_b32 v78, 1, v79
Mvaa 208,92,228
WL 7
DM 235,56,87,0
TR 86,245,45056
v_lshl_add_u32 v77, v78, 5, v77
Mvaa 128,64,224
TR 88,246,32768
v_lshrrev_b32 v78, 1, v79
v_lshl_add_u32 v77, v78, 4, v77
Mvaa 128,68,228
s_lshl_b32 s95, s79, 2
s_add_u32 s95, s95, s19
s_mul_i32 s95, s95, 36864
DM 236,56,87,1024
TR 90,246,36864
Mvaa 132,64,240
s_add_u32 s31, s95, 0
s_lshl_b32 s96, s80, 8
s_add_u32 s95, s95, s96
TR 92,246,40960
Mvaa 132,68,244
WL 7
s_add_u32 s25, s95, 64
s_add_u32 s32, s95, 18496
TR 94,246,45056
Mvaa 140,72,240
s_lshl_b32 s95, s35, 13
s_add_u32 s30, s95, 40181760
s_add_u32 s20, s95, 40173568
DM 237,56,87,2048
TR 64,247,32768
Mvaa 140,76,244
s_add_u32 s21, s95, 40200448
s_add_u32 s22, s95, 40194560
s_lshl_b32 s91, s35, 7
TR 66,247,36864
Mvaa 136,72,224
s_add_u32 s92, s91, 128
TR 68,247,40960
Mvaa 136,76,228
WL 7
DM 238,56,87,3072
TR 70,247,45056
Mvaa 152,80,224
TR 72,248,32768
Mvaa 152,84,228
TR 74,248,36864
Mvaa 156,80,240
TR 76,248,40960
Mvaa 156,84,244
WL 7
s_add_u32 m0, s85, 16384
TR 78,248,45056
Mvaa 164,88,240
DM 235,56,88,0
TR 80,240,8192
Mvaa 164,92,244
TR 82,240,8704
Mvaa 160,88,224
DM 236,56,88,1024
TR 84,240,9216
Mvaa 160,92,228
WL 7
TR 86,240,9728
Mvaa 168,64,224
TR 88,220,32768
Mvaa 168,68,228
TR 90,220,36864
Mvaa 172,64,240
DM 237,56,88,2048
TR 92,220,40960
Mvaa 172,68,244
WL 7
TR 94,220,45056
Mvaa 180,72,240
TR 64,221,32768
Mvaa 180,76,244
DM 238,56,88,3072
TR 66,221,36864
Mvaa 176,72,224
TR 68,221,40960
Mvaa 176,76,228
WL 7
TR 70,221,45056
Mvaa 144,80,224
TR 72,222,32768
Mvaa 144,84,228
s_add_u32 m0, s86, 4096
TR 74,222,36864
Mvaa 148,80,240
buffer_load_dwordx4 v234, s[72:75], s89 offen offset:0 sc1 lds
TR 76,222,40960
Mvaa 148,84,244
WL 7
TR 78,222,45056
Mvaa 188,88,248
s_add_u32 s90, s34, 16818176
GL 96,234,72,90,0
TR 80,223,32768
Mvaa 188,92,252
TR 82,223,36864
Mvaa 184,88,232
TR 84,223,40960
Mvaa 184,92,236
WL 7
TR 86,223,45056
Mvaa 208,64,232
GL 100,234,72,90,1024
TR 88,224,32768
Mvaa 208,68,236
TR 90,224,36864
Mvaa 212,64,248
TR 92,224,40960
Mvaa 212,68,252
WL 7
GL 104,234,72,90,2048
TR 94,224,45056
Mvaa 132,72,248
TR 64,225,32768
Mvaa 132,76,252
TR 66,225,36864
Mvaa 128,72,232
TR 68,225,40960
Mvaa 128,76,236
WL 7
GL 108,234,72,90,3072
TR 70,225,45056
Mvaa 136,80,232
TR 72,226,32768
Mvaa 136,84,236
TR 74,226,36864
Mvaa 140,80,248
s_add_u32 s90, s34, 16949248
GL 112,234,72,90,0
TR 76,226,40960
Mvaa 140,84,252
WL 7
TR 78,226,45056
Mvaa 156,88,248
TR 80,227,32768
Mvaa 156,92,252
TR 82,227,36864
Mvaa 152,88,232
GL 116,234,72,90,1024
TR 84,227,40960
Mvaa 152,92,236
WL 7
TR 86,227,45056
Mvaa 160,64,232
TR 88,240,10240
Mvaa 160,68,236
GL 120,234,72,90,2048
TR 90,240,10752
Mvaa 164,64,248
Lv1stub4_fused_block:
s_cbranch_execz Lt2x_fused_block
TR 92,240,11264
Mvaa 164,68,252
WL 7
TR 94,240,11776
Mvaa 172,72,248
s_waitcnt vmcnt(41) lgkmcnt(0)
s_barrier
TR 64,241,49152
Mvaa 172,76,252
GL 124,234,72,90,3072
TR 66,241,53248
Mvaa 168,72,232
TR 68,241,57344
Mvaa 168,76,236
TR 70,241,61440
Mvaa 176,80,232
TR 72,242,49152
Mvaa 176,84,236
TR 74,242,53248
Mvaa 180,80,248
TR 76,242,57344
Mvaa 180,84,252
TR 78,242,61440
Mvaa 148,88,248
TR 80,243,49152
Mvaa 148,92,252
TR 82,243,53248
Mvaa 144,88,232
TR 84,243,57344
Mvaa 144,92,236
s_waitcnt vmcnt(34) lgkmcnt(7)
TR 86,243,61440
v_mfma_f32_16x16x32_bf16 v[56:59], a[64:67], a[128:131], 0
TR 88,244,49152
Mvaa 56,68,132
TR 90,244,53248
v_mfma_f32_16x16x32_bf16 v[60:63], a[64:67], a[144:147], 0
TR 92,244,57344
Mvaa 60,68,148
WL 7
TR 94,244,61440
v_mfma_f32_16x16x32_bf16 v[68:71], a[72:75], a[144:147], 0
TR 64,245,49152
CV 148,148,149
Mvaa 68,76,148
TR 66,245,53248
CV 149,150,151
v_mfma_f32_16x16x32_bf16 v[64:67], a[72:75], a[128:131], 0
TR 68,245,57344
CV 150,144,145
Mvaa 64,76,132
WL 7
TR 70,245,61440
v_mfma_f32_16x16x32_bf16 v[0:3], a[80:83], a[128:131], 0
TR 72,246,49152
CV 151,146,147
Mvaa 0,84,132
TR 74,246,53248
CV 152,152,153
v_mfma_f32_16x16x32_bf16 v[4:7], a[80:83], a[144:147], 0
TR 76,246,57344
CV 153,154,155
Mvaa 4,84,148
WL 7
TR 78,246,61440
v_mfma_f32_16x16x32_bf16 v[12:15], a[88:91], a[144:147], 0
TR 80,247,49152
CV 154,156,157
Mvaa 12,92,148
TR 82,247,53248
CV 155,158,159
v_mfma_f32_16x16x32_bf16 v[8:11], a[88:91], a[128:131], 0
TR 84,247,57344
CV 160,160,161
Mvaa 8,92,132
WL 7
TR 86,247,61440
v_mfma_f32_16x16x32_bf16 v[24:27], a[64:67], a[128:131], 0
TR 88,248,49152
CV 161,162,163
Mvaa 24,68,132
TR 90,248,53248
CV 162,164,165
v_mfma_f32_16x16x32_bf16 v[28:31], a[64:67], a[144:147], 0
TR 92,248,57344
CV 163,166,167
Mvaa 28,68,148
WL 7
TR 94,248,61440
v_mfma_f32_16x16x32_bf16 v[36:39], a[72:75], a[144:147], 0
TR 64,240,12288
CV 168,168,169
Mvaa 36,76,148
TR 66,240,12800
CV 169,170,171
v_mfma_f32_16x16x32_bf16 v[32:35], a[72:75], a[128:131], 0
TR 68,240,13312
CV 170,172,173
Mvaa 32,76,132
WL 7
TR 70,240,13824
v_mfma_f32_16x16x32_bf16 v[40:43], a[80:83], a[128:131], 0
TR 72,220,49152
CV 171,174,175
Mvaa 40,84,132
TR 74,220,53248
CV 176,176,177
v_mfma_f32_16x16x32_bf16 v[44:47], a[80:83], a[144:147], 0
TR 76,220,57344
CV 177,178,179
Mvaa 44,84,148
WL 7
TR 78,220,61440
v_mfma_f32_16x16x32_bf16 v[52:55], a[88:91], a[144:147], 0
TR 80,221,49152
CV 178,180,181
Mvaa 52,92,148
TR 82,221,53248
CV 179,182,183
v_mfma_f32_16x16x32_bf16 v[48:51], a[88:91], a[128:131], 0
TR 84,221,57344
CV 184,184,185
Mvaa 48,92,132
WL 7
TR 86,221,61440
v_mfma_f32_16x16x32_bf16 v[16:19], a[64:67], a[128:131], 0
TR 88,222,49152
CV 185,186,187
Mvaa 16,68,132
TR 90,222,53248
CV 186,188,189
v_mfma_f32_16x16x32_bf16 v[20:23], a[64:67], a[144:147], 0
TR 92,222,57344
CV 187,190,191
Mvaa 20,68,148
WL 7
TR 94,222,61440
Mvaa 60,72,152
TR 64,223,49152
CV 208,208,209
Mvaa 60,76,156
TR 66,223,53248
CV 209,210,211
Mvaa 56,72,136
TR 68,223,57344
CV 210,212,213
Mvaa 56,76,140
WL 7
TR 70,223,61440
Mvaa 64,80,136
TR 72,224,49152
CV 211,214,215
Mvaa 64,84,140
s_add_u32 s16, s30, 2048
buffer_store_dwordx2 v[148:149], v72, s[72:75], s16 offen sc1
TR 74,224,53248
Mvaa 68,80,152
s_add_u32 s16, s30, 4352
buffer_store_dwordx2 v[148:149], v73, s[72:75], s16 offen sc1
TR 76,224,57344
Mvaa 68,84,156
WL 7
s_add_u32 s16, s30, 6656
buffer_store_dwordx2 v[150:151], v72, s[72:75], s16 offen sc1
TR 78,224,61440
Mvaa 4,88,152
s_add_u32 s16, s30, 0
buffer_store_dwordx4 v[152:155], v234, s[72:75], s16 offen sc1
TR 80,225,49152
Mvaa 4,92,156
s_add_u32 s16, s30, 1024
buffer_store_dwordx4 v[160:163], v234, s[72:75], s16 offen sc1
TR 82,225,53248
Mvaa 0,88,136
s_add_u32 s98, s36, 263680
TR 84,225,57344
v_and_b32 v249, 3, v255
Mvaa 0,92,140
WL 7
TR 86,225,61440
v_lshlrev_b32 v249, 7, v249
Mvaa 8,64,136
buffer_load_dwordx2 v[250:251], v249, s[72:75], s98 offen sc1
TR 88,226,49152
Mvaa 8,68,140
s_add_u32 s87, s28, 1622016
s_add_u32 s88, s28, 1695744
s_add_u32 s89, s29, 192512
TR 90,226,53248
Mvaa 12,64,152
s_add_u32 m0, s84, 32768
TR 92,226,57344
Mvaa 12,68,156
WL 7
DM 235,56,87,0
TR 94,226,61440
Mvaa 28,72,152
TR 64,227,49152
Mvaa 28,76,156
DM 236,56,87,1024
TR 66,227,53248
Mvaa 24,72,136
TR 68,227,57344
Mvaa 24,76,140
WL 7
DM 237,56,87,2048
TR 70,227,61440
)"
R"(Mvaa 32,80,136
TR 72,240,14336
Mvaa 32,84,140
DM 238,56,87,3072
TR 74,240,14848
Mvaa 36,80,152
s_add_u32 m0, s85, 32768
TR 76,240,15360
Mvaa 36,84,156
WL 7
DM 235,56,88,0
TR 78,240,15872
Mvaa 44,88,152
s_waitcnt vmcnt(36) lgkmcnt(0)
s_barrier
TR 80,241,0
Mvaa 44,92,156
DM 236,56,88,1024
TR 82,241,4096
Mvaa 40,88,136
TR 84,241,8192
Mvaa 40,92,140
DM 237,56,88,2048
TR 86,241,12288
Mvaa 48,64,136
TR 88,242,0
Mvaa 48,68,140
DM 238,56,88,3072
TR 90,242,4096
Mvaa 52,64,152
s_add_u32 m0, s86, 8192
TR 92,242,8192
Mvaa 52,68,156
buffer_load_dwordx4 v234, s[72:75], s89 offen offset:0 sc1 lds
TR 94,242,12288
Mvaa 20,72,152
s_add_u32 s90, s34, 16822272
GL 192,234,72,90,0
TR 64,243,0
Mvaa 20,76,156
TR 66,243,4096
Mvaa 16,72,136
GL 196,234,72,90,1024
TR 68,243,8192
Mvaa 16,76,140
s_waitcnt vmcnt(34) lgkmcnt(7)
TR 70,243,12288
Mvaa 56,80,160
GL 200,234,72,90,2048
TR 72,244,0
Mvaa 56,84,164
TR 74,244,4096
Mvaa 60,80,176
GL 204,234,72,90,3072
TR 76,244,8192
Mvaa 60,84,180
WL 7
s_add_u32 s90, s34, 16953344
GL 208,234,72,90,0
TR 78,244,12288
Mvaa 68,88,176
TR 80,245,0
Mvaa 68,92,180
TR 82,245,4096
Mvaa 64,88,160
GL 212,234,72,90,1024
TR 84,245,8192
Mvaa 64,92,164
WL 7
TR 86,245,12288
Mvaa 0,64,160
TR 88,246,0
Mvaa 0,68,164
GL 216,234,72,90,2048
TR 90,246,4096
Mvaa 4,64,176
TR 92,246,8192
Mvaa 4,68,180
WL 7
TR 94,246,12288
Mvaa 12,72,176
TR 64,247,0
Mvaa 12,76,180
GL 220,234,72,90,3072
TR 66,247,4096
Mvaa 8,72,160
TR 68,247,8192
Mvaa 8,76,164
WL 7
TR 70,247,12288
Mvaa 24,80,160
s_add_u32 s16, s30, 2304
buffer_store_dwordx4 v[168:171], v234, s[72:75], s16 offen sc1
TR 72,248,0
Mvaa 24,84,164
TR 74,248,4096
Mvaa 28,80,176
TR 76,248,8192
Mvaa 28,84,180
WL 7
TR 78,248,12288
Mvaa 36,88,176
TR 80,240,0
Mvaa 36,92,180
TR 82,240,512
Mvaa 32,88,160
TR 84,240,1024
Mvaa 32,92,164
WL 7
TR 86,240,1536
Mvaa 40,64,160
TR 88,220,0
Mvaa 40,68,164
TR 90,220,4096
Mvaa 44,64,176
TR 92,220,8192
Mvaa 44,68,180
WL 7
s_add_u32 s16, s30, 3328
buffer_store_dwordx4 v[176:179], v234, s[72:75], s16 offen sc1
TR 94,220,12288
Mvaa 52,72,176
TR 64,221,0
Mvaa 52,76,180
TR 66,221,4096
Mvaa 48,72,160
TR 68,221,8192
Mvaa 48,76,164
WL 7
TR 70,221,12288
Mvaa 16,80,160
TR 72,222,0
Mvaa 16,84,164
TR 74,222,4096
Mvaa 20,80,176
TR 76,222,8192
Mvaa 20,84,180
WL 7
TR 78,222,12288
Mvaa 60,88,184
TR 80,223,0
Mvaa 60,92,188
TR 82,223,4096
Mvaa 56,88,168
s_add_u32 s16, s30, 4608
buffer_store_dwordx4 v[184:187], v234, s[72:75], s16 offen sc1
TR 84,223,8192
Mvaa 56,92,172
WL 7
TR 86,223,12288
Mvaa 64,64,168
TR 88,224,0
Mvaa 64,68,172
TR 90,224,4096
Mvaa 68,64,184
TR 92,224,8192
Mvaa 68,68,188
WL 7
TR 94,224,12288
Mvaa 4,72,184
TR 64,225,0
Mvaa 4,76,188
TR 66,225,4096
Mvaa 0,72,168
TR 68,225,8192
Mvaa 0,76,172
WL 7
TR 70,225,12288
Mvaa 8,80,168
TR 72,226,0
Mvaa 8,84,172
s_add_u32 s16, s30, 5632
buffer_store_dwordx4 v[208:211], v234, s[72:75], s16 offen sc1
TR 74,226,4096
Mvaa 12,80,184
TR 76,226,8192
Mvaa 12,84,188
WL 7
TR 78,226,12288
Mvaa 28,88,184
TR 80,227,0
Mvaa 28,92,188
TR 82,227,4096
Mvaa 24,88,168
TR 84,227,8192
Mvaa 24,92,172
WL 7
TR 86,227,12288
Mvaa 32,64,168
TR 88,240,2048
Mvaa 32,68,172
TR 90,240,2560
Mvaa 36,64,184
TR 92,240,3072
Mvaa 36,68,188
WL 7
TR 94,240,3584
Mvaa 44,72,184
s_waitcnt vmcnt(35) lgkmcnt(0)
s_barrier
s_add_u32 s87, s28, 1769472
TR 64,241,16384
Mvaa 44,76,188
s_add_u32 s88, s28, 1843200
s_add_u32 s89, s29, 196608
s_add_u32 m0, s84, 49152
TR 66,241,20480
Mvaa 40,72,168
DM 235,56,87,0
TR 68,241,24576
Mvaa 40,76,172
TR 70,241,28672
Mvaa 48,80,168
DM 236,56,87,1024
TR 72,242,16384
Mvaa 48,84,172
TR 74,242,20480
Mvaa 52,80,184
TR 76,242,24576
Mvaa 52,84,188
DM 237,56,87,2048
TR 78,242,28672
Mvaa 20,88,184
TR 80,243,16384
Mvaa 20,92,188
TR 82,243,20480
Mvaa 16,88,168
TR 84,243,24576
Mvaa 16,92,172
s_waitcnt vmcnt(30) lgkmcnt(7)
DM 238,56,87,3072
TR 86,243,28672
Mvaa 56,64,96
TR 88,244,16384
Mvaa 56,68,100
TR 90,244,20480
Mvaa 60,64,112
s_add_u32 m0, s85, 49152
TR 92,244,24576
Mvaa 60,68,116
WL 7
DM 235,56,88,0
TR 94,244,28672
Mvaa 68,72,112
TR 64,245,16384
Mvaa 68,76,116
TR 66,245,20480
Mvaa 64,72,96
DM 236,56,88,1024
TR 68,245,24576
Mvaa 64,76,100
WL 7
TR 70,245,28672
Mvaa 0,80,96
TR 72,246,16384
Mvaa 0,84,100
DM 237,56,88,2048
TR 74,246,20480
Mvaa 4,80,112
TR 76,246,24576
Mvaa 4,84,116
WL 7
TR 78,246,28672
Mvaa 12,88,112
TR 80,247,16384
Mvaa 12,92,116
DM 238,56,88,3072
TR 82,247,20480
Mvaa 8,88,96
TR 84,247,24576
Mvaa 8,92,100
WL 7
TR 86,247,28672
Mvaa 24,64,96
s_add_u32 m0, s86, 12288
TR 88,248,16384
Mvaa 24,68,100
buffer_load_dwordx4 v234, s[72:75], s89 offen offset:0 sc1 lds
TR 90,248,20480
Mvaa 28,64,112
TR 92,248,24576
Mvaa 28,68,116
WL 7
TR 94,248,28672
Mvaa 36,72,112
s_waitcnt vmcnt(30)
TR 64,240,4096
v_cmp_ne_u64 vcc, s[82:83], v[250:251]
s_nop 3
s_cbranch_vccnz Lslow4_fused_block
Lslowb4_fused_block:
Mvaa 36,76,116
s_add_u32 s90, s34, 16826368
GL 224,234,72,90,0
TR 66,240,4608
Mvaa 32,72,96
TR 68,240,5120
Mvaa 32,76,100
WL 7
GL 228,234,72,90,1024
TR 70,240,5632
Mvaa 40,80,96
TR 72,220,16384
Mvaa 40,84,100
TR 74,220,20480
Mvaa 44,80,112
TR 76,220,24576
Mvaa 44,84,116
WL 7
GL 232,234,72,90,2048
TR 78,220,28672
Mvaa 52,88,112
TR 80,221,16384
Mvaa 52,92,116
TR 82,221,20480
Mvaa 48,88,96
GL 236,234,72,90,3072
TR 84,221,24576
Mvaa 48,92,100
WL 7
TR 86,221,28672
Mvaa 16,64,96
TR 88,222,16384
Mvaa 16,68,100
TR 90,222,20480
Mvaa 20,64,112
s_add_u32 s90, s34, 16957440
GL 240,234,72,90,0
TR 92,222,24576
Mvaa 20,68,116
WL 7
TR 94,222,28672
Mvaa 60,72,120
Lv1stub5_fused_block:
s_cbranch_execz Lt2x_fused_block
TR 64,223,16384
Mvaa 60,76,124
GL 244,234,72,90,1024
TR 66,223,20480
Mvaa 56,72,104
TR 68,223,24576
Mvaa 56,76,108
WL 7
TR 70,223,28672
Mvaa 64,80,104
TR 72,224,16384
Mvaa 64,84,108
GL 248,234,72,90,2048
TR 74,224,20480
Mvaa 68,80,120
TR 76,224,24576
Mvaa 68,84,124
WL 7
TR 78,224,28672
Mvaa 4,88,120
GL 252,234,72,90,3072
TR 80,225,16384
Mvaa 4,92,124
TR 82,225,20480
Mvaa 0,88,104
TR 84,225,24576
Mvaa 0,92,108
WL 7
TR 86,225,28672
Mvaa 8,64,104
s_waitcnt vmcnt(17)
s_add_u32 s98, s92, 393216
TR 88,226,16384
v_mov_b32 v250, s82
Mvaa 8,68,108
TR 90,226,20480
v_mov_b32 v251, s83
v_lshlrev_b32 v249, 3, v255
Mvaa 12,64,120
TR 92,226,24576
s_mov_b64 exec, 0xffff
buffer_store_dwordx2 v[250:251], v249, s[72:75], s98 offen sc1
s_mov_b64 exec, -1
Mvaa 12,68,124
WL 7
s_add_u32 s87, s28, 1916928
s_add_u32 s88, s28, 1990656
TR 94,226,28672
Mvaa 28,72,120
s_add_u32 s89, s29, 200704
s_add_u32 m0, s84, 0
TR 64,227,16384
Mvaa 28,76,124
DM 235,56,87,0
TR 66,227,20480
Mvaa 24,72,104
TR 68,227,24576
Mvaa 24,76,108
WL 7
DM 236,56,87,1024
TR 70,227,28672
Mvaa 32,80,104
TR 72,240,6144
Mvaa 32,84,108
DM 237,56,87,2048
)"
R"(TR 74,240,6656
Mvaa 36,80,120
TR 76,240,7168
Mvaa 36,84,124
WL 7
DM 238,56,87,3072
TR 78,240,7680
Mvaa 44,88,120
WL 0
s_barrier
TR 80,241,32768
Mvaa 44,92,124
TR 82,241,36864
Mvaa 40,88,104
s_add_u32 m0, s85, 0
TR 84,241,40960
Mvaa 40,92,108
DM 235,56,88,0
TR 86,241,45056
Mvaa 48,64,104
TR 88,242,32768
Mvaa 48,68,108
DM 236,56,88,1024
TR 90,242,36864
Mvaa 52,64,120
TR 92,242,40960
Mvaa 52,68,124
TR 94,242,45056
Mvaa 20,72,120
TR 64,243,32768
Mvaa 20,76,124
DM 237,56,88,2048
TR 66,243,36864
Mvaa 16,72,104
TR 68,243,40960
Mvaa 16,76,108
WL 7
TR 70,243,45056
Mvaa 56,80,192
DM 238,56,88,3072
TR 72,244,32768
Mvaa 56,84,196
TR 74,244,36864
Mvaa 60,80,208
TR 76,244,40960
Mvaa 60,84,212
WL 7
TR 78,244,45056
Mvaa 68,88,208
s_add_u32 m0, s86, 0
TR 80,245,32768
Mvaa 68,92,212
buffer_load_dwordx4 v234, s[72:75], s89 offen offset:0 sc1 lds
TR 82,245,36864
Mvaa 64,88,192
TR 84,245,40960
Mvaa 64,92,196
WL 7
s_add_u32 s90, s34, 16830464
GL 128,234,72,90,0
TR 86,245,45056
Mvaa 0,64,192
TR 88,246,32768
Mvaa 0,68,196
TR 90,246,36864
Mvaa 4,64,208
TR 92,246,40960
Mvaa 4,68,212
WL 7
GL 132,234,72,90,1024
TR 94,246,45056
Mvaa 12,72,208
TR 64,247,32768
Mvaa 12,76,212
TR 66,247,36864
Mvaa 8,72,192
GL 136,234,72,90,2048
TR 68,247,40960
Mvaa 8,76,196
WL 7
TR 70,247,45056
Mvaa 24,80,192
TR 72,248,32768
Mvaa 24,84,196
TR 74,248,36864
Mvaa 28,80,208
GL 140,234,72,90,3072
TR 76,248,40960
Mvaa 28,84,212
WL 7
TR 78,248,45056
Mvaa 36,88,208
TR 80,240,8192
Mvaa 36,92,212
s_add_u32 s90, s34, 16961536
GL 144,234,72,90,0
TR 82,240,8704
Mvaa 32,88,192
TR 84,240,9216
Mvaa 32,92,196
WL 7
TR 86,240,9728
Mvaa 40,64,192
TR 88,220,32768
Mvaa 40,68,196
GL 148,234,72,90,1024
TR 90,220,36864
Mvaa 44,64,208
TR 92,220,40960
Mvaa 44,68,212
WL 7
TR 94,220,45056
Mvaa 52,72,208
GL 152,234,72,90,2048
TR 64,221,32768
Mvaa 52,76,212
TR 66,221,36864
Mvaa 48,72,192
TR 68,221,40960
Mvaa 48,76,196
WL 7
TR 70,221,45056
Mvaa 16,80,192
GL 156,234,72,90,3072
TR 72,222,32768
Mvaa 16,84,196
TR 74,222,36864
Mvaa 20,80,208
TR 76,222,40960
Mvaa 20,84,212
WL 7
s_add_u32 s87, s28, 2064384
s_add_u32 s88, s28, 2138112
TR 78,222,45056
Mvaa 60,88,216
s_add_u32 s89, s29, 204800
s_add_u32 m0, s84, 16384
TR 80,223,32768
Mvaa 60,92,220
DM 235,56,87,0
TR 82,223,36864
Mvaa 56,88,200
TR 84,223,40960
Mvaa 56,92,204
WL 7
DM 236,56,87,1024
TR 86,223,45056
Mvaa 64,64,200
TR 88,224,32768
Mvaa 64,68,204
TR 90,224,36864
Mvaa 68,64,216
DM 237,56,87,2048
TR 92,224,40960
Mvaa 68,68,220
WL 7
TR 94,224,45056
Mvaa 4,72,216
TR 64,225,32768
Mvaa 4,76,220
TR 66,225,36864
Mvaa 0,72,200
DM 238,56,87,3072
TR 68,225,40960
Mvaa 0,76,204
WL 7
TR 70,225,45056
Mvaa 8,80,200
TR 72,226,32768
Mvaa 8,84,204
s_add_u32 m0, s85, 16384
TR 74,226,36864
Mvaa 12,80,216
DM 235,56,88,0
TR 76,226,40960
Mvaa 12,84,220
WL 7
TR 78,226,45056
Mvaa 28,88,216
TR 80,227,32768
Mvaa 28,92,220
DM 236,56,88,1024
TR 82,227,36864
Mvaa 24,88,200
TR 84,227,40960
Mvaa 24,92,204
WL 7
TR 86,227,45056
Mvaa 32,64,200
DM 237,56,88,2048
TR 88,240,10240
Mvaa 32,68,204
TR 90,240,10752
Mvaa 36,64,216
TR 92,240,11264
Mvaa 36,68,220
WL 7
TR 94,240,11776
Mvaa 44,72,216
s_waitcnt vmcnt(33) lgkmcnt(0)
s_barrier
DM 238,56,88,3072
TR 64,241,49152
Mvaa 44,76,220
TR 66,241,53248
Mvaa 40,72,200
TR 68,241,57344
Mvaa 40,76,204
s_add_u32 m0, s86, 4096
TR 70,241,61440
Mvaa 48,80,200
buffer_load_dwordx4 v234, s[72:75], s89 offen offset:0 sc1 lds
TR 72,242,49152
Mvaa 48,84,204
TR 74,242,53248
Mvaa 52,80,216
TR 76,242,57344
Mvaa 52,84,220
s_add_u32 s90, s34, 16834560
GL 160,234,72,90,0
TR 78,242,61440
Mvaa 20,88,216
TR 80,243,49152
Mvaa 20,92,220
TR 82,243,53248
Mvaa 16,88,200
GL 164,234,72,90,1024
TR 84,243,57344
Mvaa 16,92,204
s_waitcnt vmcnt(29) lgkmcnt(7)
TR 86,243,61440
Mvaa 56,64,224
TR 88,244,49152
Mvaa 56,68,228
TR 90,244,53248
Mvaa 60,64,240
GL 168,234,72,90,2048
TR 92,244,57344
Mvaa 60,68,244
WL 7
TR 94,244,61440
Mvaa 68,72,240
TR 64,245,49152
Mvaa 68,76,244
GL 172,234,72,90,3072
TR 66,245,53248
Mvaa 64,72,224
TR 68,245,57344
Mvaa 64,76,228
WL 7
TR 70,245,61440
Mvaa 0,80,224
TR 72,246,49152
Mvaa 0,84,228
s_add_u32 s90, s34, 16965632
GL 176,234,72,90,0
TR 74,246,53248
Mvaa 4,80,240
TR 76,246,57344
Mvaa 4,84,244
WL 7
TR 78,246,61440
Mvaa 12,88,240
GL 180,234,72,90,1024
TR 80,247,49152
Mvaa 12,92,244
TR 82,247,53248
Mvaa 8,88,224
TR 84,247,57344
Mvaa 8,92,228
WL 7
TR 86,247,61440
Mvaa 24,64,224
GL 184,234,72,90,2048
TR 88,248,49152
Mvaa 24,68,228
TR 90,248,53248
Mvaa 28,64,240
TR 92,248,57344
Mvaa 28,68,244
WL 7
GL 188,234,72,90,3072
TR 94,248,61440
Mvaa 36,72,240
TR 64,240,12288
Mvaa 36,76,244
TR 66,240,12800
Mvaa 32,72,224
TR 68,240,13312
Mvaa 32,76,228
WL 7
s_add_u32 s87, s28, 2211840
s_add_u32 s88, s28, 2285568
TR 70,240,13824
Mvaa 40,80,224
s_add_u32 s89, s29, 208896
s_add_u32 m0, s84, 32768
TR 72,220,49152
Mvaa 40,84,228
DM 235,56,87,0
TR 74,220,53248
Mvaa 44,80,240
TR 76,220,57344
Mvaa 44,84,244
WL 7
DM 236,56,87,1024
TR 78,220,61440
Mvaa 52,88,240
TR 80,221,49152
Mvaa 52,92,244
TR 82,221,53248
Mvaa 48,88,224
DM 237,56,87,2048
TR 84,221,57344
Mvaa 48,92,228
WL 7
TR 86,221,61440
Mvaa 16,64,224
TR 88,222,49152
Mvaa 16,68,228
DM 238,56,87,3072
TR 90,222,53248
Mvaa 20,64,240
TR 92,222,57344
Mvaa 20,68,244
WL 7
TR 94,222,61440
Mvaa 60,72,248
TR 64,223,49152
Mvaa 60,76,252
s_add_u32 m0, s85, 32768
TR 66,223,53248
Mvaa 56,72,232
DM 235,56,88,0
TR 68,223,57344
Mvaa 56,76,236
WL 7
TR 70,223,61440
Mvaa 64,80,232
DM 236,56,88,1024
TR 72,224,49152
Mvaa 64,84,236
TR 74,224,53248
Mvaa 68,80,248
TR 76,224,57344
Mvaa 68,84,252
WL 7
TR 78,224,61440
Mvaa 4,88,248
DM 237,56,88,2048
TR 80,225,49152
Mvaa 4,92,252
TR 82,225,53248
Mvaa 0,88,232
TR 84,225,57344
Mvaa 0,92,236
WL 7
DM 238,56,88,3072
TR 86,225,61440
Mvaa 8,64,232
TR 88,226,49152
Mvaa 8,68,236
TR 90,226,53248
Mvaa 12,64,248
TR 92,226,57344
Mvaa 12,68,252
WL 7
s_add_u32 m0, s86, 8192
TR 94,226,61440
Mvaa 28,72,248
buffer_load_dwordx4 v234, s[72:75], s89 offen offset:0 sc1 lds
TR 64,227,49152
Mvaa 28,76,252
TR 66,227,53248
Mvaa 24,72,232
s_add_u32 s90, s34, 16838656
GL 96,234,72,90,0
TR 68,227,57344
Mvaa 24,76,236
WL 7
TR 70,227,61440
Mvaa 32,80,232
TR 72,240,14336
Mvaa 32,84,236
TR 74,240,14848
Mvaa 36,80,248
GL 100,234,72,90,1024
TR 76,240,15360
Mvaa 36,84,252
WL 7
TR 78,240,15872
Mvaa 44,88,248
s_waitcnt vmcnt(36) lgkmcnt(0)
s_barrier
TR 80,241,0
Mvaa 44,92,252
GL 104,234,72,90,2048
TR 82,241,4096
Mvaa 40,88,232
TR 84,241,8192
)"
R"(Mvaa 40,92,236
TR 86,241,12288
Mvaa 48,64,232
TR 88,242,0
Mvaa 48,68,236
GL 108,234,72,90,3072
TR 90,242,4096
Mvaa 52,64,248
TR 92,242,8192
Mvaa 52,68,252
TR 94,242,12288
Mvaa 20,72,248
s_add_u32 s90, s34, 16969728
GL 112,234,72,90,0
TR 64,243,0
Mvaa 20,76,252
TR 66,243,4096
Mvaa 16,72,232
TR 68,243,8192
Mvaa 16,76,236
s_waitcnt vmcnt(31) lgkmcnt(7)
TR 70,243,12288
Mvaa 56,80,128
GL 116,234,72,90,1024
TR 72,244,0
Mvaa 56,84,132
TR 74,244,4096
Mvaa 60,80,144
TR 76,244,8192
Mvaa 60,84,148
WL 7
GL 120,234,72,90,2048
TR 78,244,12288
Mvaa 68,88,144
TR 80,245,0
Mvaa 68,92,148
TR 82,245,4096
Mvaa 64,88,128
TR 84,245,8192
Mvaa 64,92,132
WL 7
GL 124,234,72,90,3072
TR 86,245,12288
Mvaa 0,64,128
TR 88,246,0
Mvaa 0,68,132
TR 90,246,4096
Mvaa 4,64,144
s_add_u32 s98, s91, 393216
TR 92,246,8192
v_and_b32 v249, 3, v255
Mvaa 4,68,148
WL 7
TR 94,246,12288
v_lshlrev_b32 v249, 7, v249
Mvaa 12,72,144
buffer_load_dwordx2 v[250:251], v249, s[72:75], s98 offen sc1
TR 64,247,0
Mvaa 12,76,148
TR 66,247,4096
Mvaa 8,72,128
buffer_load_dwordx2 v[80:81], v75, s[40:43], s25 offen offset:0
TR 68,247,8192
Mvaa 8,76,132
WL 7
TR 70,247,12288
Mvaa 24,80,128
buffer_load_dwordx2 v[82:83], v75, s[40:43], s32 offen offset:0
TR 72,248,0
Mvaa 24,84,132
TR 74,248,4096
Mvaa 28,80,144
buffer_load_dwordx2 v[84:85], v75, s[40:43], s25 offen offset:32
TR 76,248,8192
Mvaa 28,84,148
Lv1stub6_fused_block:
s_cbranch_execz Lt2x_fused_block
WL 7
TR 78,248,12288
Mvaa 36,88,144
buffer_load_dwordx2 v[86:87], v75, s[40:43], s32 offen offset:32
TR 80,240,0
Mvaa 36,92,148
TR 82,240,512
Mvaa 32,88,128
buffer_load_dwordx2 v[88:89], v76, s[40:43], s31 offen
TR 84,240,1024
Mvaa 32,92,132
WL 7
TR 86,240,1536
Mvaa 40,64,128
TR 88,220,0
Mvaa 40,68,132
TR 90,220,4096
Mvaa 44,64,144
TR 92,220,8192
Mvaa 44,68,148
WL 7
TR 94,220,12288
Mvaa 52,72,144
TR 64,221,0
Mvaa 52,76,148
TR 66,221,4096
Mvaa 48,72,128
TR 68,221,8192
Mvaa 48,76,132
WL 7
TR 70,221,12288
Mvaa 16,80,128
TR 72,222,0
Mvaa 16,84,132
TR 74,222,4096
Mvaa 20,80,144
TR 76,222,8192
Mvaa 20,84,148
WL 7
TR 78,222,12288
Mvaa 60,88,152
TR 80,223,0
Mvaa 60,92,156
TR 82,223,4096
Mvaa 56,88,136
TR 84,223,8192
Mvaa 56,92,140
WL 7
TR 86,223,12288
Mvaa 64,64,136
TR 88,224,0
Mvaa 64,68,140
TR 90,224,4096
Mvaa 68,64,152
TR 92,224,8192
Mvaa 68,68,156
WL 7
TR 94,224,12288
Mvaa 4,72,152
TR 64,225,0
Mvaa 4,76,156
TR 66,225,4096
Mvaa 0,72,136
TR 68,225,8192
Mvaa 0,76,140
WL 7
TR 70,225,12288
Mvaa 8,80,136
TR 72,226,0
Mvaa 8,84,140
TR 74,226,4096
Mvaa 12,80,152
TR 76,226,8192
Mvaa 12,84,156
WL 7
TR 78,226,12288
Mvaa 28,88,152
TR 80,227,0
Mvaa 28,92,156
TR 82,227,4096
Mvaa 24,88,136
TR 84,227,8192
Mvaa 24,92,140
WL 7
TR 86,227,12288
Mvaa 32,64,136
TR 88,240,2048
Mvaa 32,68,140
TR 90,240,2560
Mvaa 36,64,152
TR 92,240,3072
Mvaa 36,68,156
WL 7
TR 94,240,3584
Mvaa 44,72,152
s_waitcnt vmcnt(14) lgkmcnt(0)
s_barrier
TR 64,240,4096
Mvaa 44,76,156
TR 66,240,4608
Mvaa 40,72,136
TR 68,240,5120
Mvaa 40,76,140
TR 70,240,5632
Mvaa 48,80,136
TR 72,240,6144
Mvaa 48,84,140
TR 74,240,6656
Mvaa 52,80,152
TR 76,240,7168
Mvaa 52,84,156
TR 78,240,7680
Mvaa 20,88,152
TR 80,240,8192
Mvaa 20,92,156
TR 82,240,8704
Mvaa 16,88,136
TR 84,240,9216
Mvaa 16,92,140
WL 7
TR 86,240,9728
Mvaa 16,64,160
TR 88,240,10240
Mvaa 16,68,164
TR 90,240,10752
Mvaa 20,64,176
TR 92,240,11264
Mvaa 20,68,180
WL 7
TR 94,240,11776
Mvaa 20,72,184
TR 64,245,16384
Mvaa 20,76,188
TR 66,245,20480
Mvaa 16,72,168
TR 68,245,24576
Mvaa 16,76,172
s_waitcnt vmcnt(6) lgkmcnt(7)
TR 70,245,28672
Mvaa 16,80,96
TR 72,224,16384
Mvaa 16,84,100
TR 74,224,20480
Mvaa 20,80,112
s_waitcnt vmcnt(5)
TR 76,224,24576
v_cmp_ne_u64 vcc, s[82:83], v[250:251]
s_nop 3
s_cbranch_vccnz Lslow5_fused_block
Lslowb5_fused_block:
Mvaa 20,84,116
WL 7
buffer_load_dwordx4 v[152:155], v234, s[72:75], s20 offen offset:0 sc1
TR 78,224,28672
Mvaa 20,88,120
TR 80,245,32768
Mvaa 20,92,124
buffer_load_dwordx4 v[156:159], v234, s[72:75], s20 offen offset:1024 sc1
TR 82,245,36864
Mvaa 16,88,104
TR 84,245,40960
Mvaa 16,92,108
WL 7
buffer_load_dwordx2 v[160:161], v74, s[72:75], s20 offen offset:2048 sc1
TR 86,245,45056
Mvaa 24,64,160
TR 88,224,32768
Mvaa 24,68,164
buffer_load_dwordx4 v[162:165], v234, s[72:75], s21 offen offset:0 sc1
TR 90,224,36864
Mvaa 28,64,176
TR 92,224,40960
Mvaa 28,68,180
WL 7
buffer_load_dwordx4 v[166:169], v234, s[72:75], s21 offen offset:1024 sc1
TR 94,224,45056
CV 20,20,21
Mvaa 28,72,184
TR 64,246,16384
CV 21,22,23
CV 22,16,17
Mvaa 28,76,188
buffer_load_dwordx2 v[170:171], v74, s[72:75], s21 offen offset:2048 sc1
TR 66,246,20480
CV 23,18,19
Mvaa 24,72,168
TR 68,246,24576
Mvaa 24,76,172
WL 7
buffer_load_dwordx4 v[172:175], v234, s[72:75], s22 offen offset:0 sc1
TR 70,246,28672
Mvaa 24,80,96
TR 72,225,16384
Mvaa 24,84,100
buffer_load_dwordx4 v[176:179], v234, s[72:75], s22 offen offset:1024 sc1
TR 74,225,20480
Mvaa 28,80,112
TR 76,225,24576
Mvaa 28,84,116
WL 7
buffer_load_dwordx2 v[180:181], v74, s[72:75], s22 offen offset:2048 sc1
TR 78,225,28672
Mvaa 28,88,120
s_add_u32 s20, s20, 8388608
s_add_u32 s21, s21, 8388608
s_add_u32 s22, s22, 8388608
TR 80,246,32768
Mvaa 28,92,124
s_add_u32 s16, s30, 8390656
buffer_store_dwordx2 v[20:21], v72, s[72:75], s16 offen sc1
TR 82,246,36864
Mvaa 24,88,104
s_add_u32 s16, s30, 8392960
buffer_store_dwordx2 v[20:21], v73, s[72:75], s16 offen sc1
TR 84,246,40960
Mvaa 24,92,108
WL 7
s_add_u32 s16, s30, 8395264
buffer_store_dwordx2 v[22:23], v72, s[72:75], s16 offen sc1
TR 86,246,45056
Mvaa 32,64,160
TR 88,225,32768
Mvaa 32,68,164
TR 90,225,36864
Mvaa 36,64,176
TR 92,225,40960
Mvaa 36,68,180
WL 7
TR 94,225,45056
CV 24,24,25
Mvaa 36,72,184
TR 64,247,16384
CV 25,26,27
CV 26,28,29
Mvaa 36,76,188
TR 66,247,20480
CV 27,30,31
Mvaa 32,72,168
s_add_u32 s16, s30, 8388608
buffer_store_dwordx4 v[24:27], v234, s[72:75], s16 offen sc1
TR 68,247,24576
Mvaa 32,76,172
WL 7
TR 70,247,28672
Mvaa 32,80,96
TR 72,226,16384
Mvaa 32,84,100
TR 74,226,20480
Mvaa 36,80,112
TR 76,226,24576
Mvaa 36,84,116
WL 7
TR 78,226,28672
Mvaa 36,88,120
TR 80,247,32768
Mvaa 36,92,124
TR 82,247,36864
Mvaa 32,88,104
TR 84,247,40960
Mvaa 32,92,108
WL 7
TR 86,247,45056
Mvaa 40,64,160
TR 88,226,32768
Mvaa 40,68,164
TR 90,226,36864
Mvaa 44,64,176
TR 92,226,40960
Mvaa 44,68,180
WL 7
TR 94,226,45056
CV 32,32,33
Mvaa 44,72,184
TR 64,248,16384
CV 33,34,35
CV 34,36,37
Mvaa 44,76,188
TR 66,248,20480
CV 35,38,39
Mvaa 40,72,168
s_add_u32 s16, s30, 8389632
buffer_store_dwordx4 v[32:35], v234, s[72:75], s16 offen sc1
TR 68,248,24576
Mvaa 40,76,172
WL 7
TR 70,248,28672
Mvaa 40,80,96
TR 72,227,16384
Mvaa 40,84,100
TR 74,227,20480
Mvaa 44,80,112
TR 76,227,24576
Mvaa 44,84,116
WL 7
TR 78,227,28672
Mvaa 44,88,120
TR 80,248,32768
Mvaa 44,92,124
TR 82,248,36864
Mvaa 40,88,104
TR 84,248,40960
Mvaa 40,92,108
WL 7
TR 86,248,45056
Mvaa 48,64,160
TR 88,227,32768
Mvaa 48,68,164
TR 90,227,36864
Mvaa 52,64,176
TR 92,227,40960
Mvaa 52,68,180
WL 7
TR 94,227,45056
CV 40,40,41
Mvaa 52,72,184
TR 64,241,16384
CV 41,42,43
CV 42,44,45
Mvaa 52,76,188
TR 66,241,20480
CV 43,46,47
Mvaa 48,72,168
s_add_u32 s16, s30, 8390912
)"
R"(buffer_store_dwordx4 v[40:43], v234, s[72:75], s16 offen sc1
TR 68,241,24576
Mvaa 48,76,172
WL 7
TR 70,241,28672
Mvaa 48,80,96
TR 72,220,16384
Mvaa 48,84,100
TR 74,220,20480
Mvaa 52,80,112
TR 76,220,24576
Mvaa 52,84,116
WL 7
TR 78,220,28672
Mvaa 52,88,120
TR 80,241,32768
Mvaa 52,92,124
TR 82,241,36864
Mvaa 48,88,104
TR 84,241,40960
Mvaa 48,92,108
WL 7
TR 86,241,45056
Mvaa 56,64,160
TR 88,220,32768
Mvaa 56,68,164
TR 90,220,36864
Mvaa 60,64,176
TR 92,220,40960
Mvaa 60,68,180
WL 7
TR 94,220,45056
CV 48,48,49
Mvaa 60,72,184
TR 64,242,16384
CV 49,50,51
CV 50,52,53
Mvaa 60,76,188
TR 66,242,20480
CV 51,54,55
Mvaa 56,72,168
s_add_u32 s16, s30, 8391936
buffer_store_dwordx4 v[48:51], v234, s[72:75], s16 offen sc1
TR 68,242,24576
Mvaa 56,76,172
WL 7
TR 70,242,28672
Mvaa 56,80,96
TR 72,221,16384
Mvaa 56,84,100
TR 74,221,20480
Mvaa 60,80,112
TR 76,221,24576
Mvaa 60,84,116
WL 7
TR 78,221,28672
Mvaa 60,88,120
TR 80,242,32768
Mvaa 60,92,124
TR 82,242,36864
Mvaa 56,88,104
TR 84,242,40960
Mvaa 56,92,108
WL 7
TR 86,242,45056
Mvaa 64,64,160
TR 88,221,32768
Mvaa 64,68,164
TR 90,221,36864
Mvaa 68,64,176
TR 92,221,40960
Mvaa 68,68,180
WL 7
TR 94,221,45056
CV 56,56,57
Mvaa 68,72,184
TR 64,243,16384
CV 57,58,59
CV 58,60,61
Mvaa 68,76,188
TR 66,243,20480
CV 59,62,63
Mvaa 64,72,168
s_add_u32 s16, s30, 8393216
buffer_store_dwordx4 v[56:59], v234, s[72:75], s16 offen sc1
TR 68,243,24576
Mvaa 64,76,172
WL 7
TR 70,243,28672
Mvaa 64,80,96
TR 72,222,16384
Mvaa 64,84,100
TR 74,222,20480
Mvaa 68,80,112
TR 76,222,24576
Mvaa 68,84,116
WL 7
TR 78,222,28672
Mvaa 68,88,120
TR 80,243,32768
Mvaa 68,92,124
TR 82,243,36864
Mvaa 64,88,104
TR 84,243,40960
Mvaa 64,92,108
WL 7
TR 86,243,45056
Mvaa 0,64,160
TR 88,222,32768
Mvaa 0,68,164
TR 90,222,36864
Mvaa 4,64,176
TR 92,222,40960
Mvaa 4,68,180
WL 7
TR 94,222,45056
CV 64,64,65
Mvaa 4,72,184
TR 64,244,16384
CV 65,66,67
CV 66,68,69
Mvaa 4,76,188
TR 66,244,20480
CV 67,70,71
Mvaa 0,72,168
s_add_u32 s16, s30, 8394240
buffer_store_dwordx4 v[64:67], v234, s[72:75], s16 offen sc1
TR 68,244,24576
Mvaa 0,76,172
WL 7
TR 70,244,28672
Mvaa 0,80,96
TR 72,223,16384
Mvaa 0,84,100
TR 74,223,20480
Mvaa 4,80,112
TR 76,223,24576
Mvaa 4,84,116
WL 7
TR 78,223,28672
Mvaa 4,88,120
TR 80,244,32768
Mvaa 4,92,124
TR 82,244,36864
Mvaa 0,88,104
TR 84,244,40960
Mvaa 0,92,108
WL 7
TR 86,244,45056
Mvaa 8,64,160
TR 88,223,32768
Mvaa 8,68,164
TR 90,223,36864
Mvaa 12,64,176
TR 92,223,40960
Mvaa 12,68,180
WL 7
TR 94,223,45056
Mvaa 12,72,184
Mvaa 12,76,188
Mvaa 8,72,168
Mvaa 8,76,172
WL 4
Mvaa 8,80,96
Mvaa 8,84,100
Mvaa 12,80,112
Mvaa 12,84,116
WL 0
Mvaa 12,88,120
Mvaa 12,92,124
Mvaa 8,88,104
Mvaa 8,92,108
buffer_load_dwordx4 v[96:99], v234, s[72:75], s20 offen offset:0 sc1
buffer_load_dwordx4 v[100:103], v234, s[72:75], s20 offen offset:1024 sc1
buffer_load_dwordx2 v[104:105], v74, s[72:75], s20 offen offset:2048 sc1
buffer_load_dwordx4 v[106:109], v234, s[72:75], s21 offen offset:0 sc1
buffer_load_dwordx4 v[110:113], v234, s[72:75], s21 offen offset:1024 sc1
buffer_load_dwordx2 v[114:115], v74, s[72:75], s21 offen offset:2048 sc1
buffer_load_dwordx4 v[116:119], v234, s[72:75], s22 offen offset:0 sc1
buffer_load_dwordx4 v[120:123], v234, s[72:75], s22 offen offset:1024 sc1
buffer_load_dwordx2 v[124:125], v74, s[72:75], s22 offen offset:2048 sc1
s_waitcnt vmcnt(18)
s_branch Ltail_fused_block
Lslow3_fused_block:
s_add_u32 s98, s36, 263168
v_and_b32 v249, 3, v255
v_lshlrev_b32 v249, 7, v249
s_mov_b32 s99, 0x20000
Lpoll4_fused_block:
buffer_load_dwordx2 v[250:251], v249, s[72:75], s98 offen sc1
s_waitcnt vmcnt(0)
v_cmp_ne_u64 vcc, s[82:83], v[250:251]
s_sub_u32 s99, s99, 1
s_cbranch_scc1 Lpolld4_fused_block
s_nop 2
s_cbranch_vccnz Lpoll4_fused_block
Lpolld4_fused_block:
s_branch Lslowb3_fused_block
Lslow4_fused_block:
s_add_u32 s98, s36, 263680
v_and_b32 v249, 3, v255
v_lshlrev_b32 v249, 7, v249
s_mov_b32 s99, 0x20000
Lpoll5_fused_block:
buffer_load_dwordx2 v[250:251], v249, s[72:75], s98 offen sc1
s_waitcnt vmcnt(0)
v_cmp_ne_u64 vcc, s[82:83], v[250:251]
s_sub_u32 s99, s99, 1
s_cbranch_scc1 Lpolld5_fused_block
s_nop 2
s_cbranch_vccnz Lpoll5_fused_block
Lpolld5_fused_block:
s_branch Lslowb4_fused_block
Lslow5_fused_block:
s_add_u32 s98, s91, 393216
v_and_b32 v249, 3, v255
v_lshlrev_b32 v249, 7, v249
s_mov_b32 s99, 0x20000
Lpoll6_fused_block:
buffer_load_dwordx2 v[250:251], v249, s[72:75], s98 offen sc1
s_waitcnt vmcnt(0)
v_cmp_ne_u64 vcc, s[82:83], v[250:251]
s_sub_u32 s99, s99, 1
s_cbranch_scc1 Lpolld6_fused_block
s_nop 2
s_cbranch_vccnz Lpoll6_fused_block
Lpolld6_fused_block:
s_branch Lslowb5_fused_block
.p2align 6
s_branch Lv2stub0_fused_block
s_branch Lv2stub1_fused_block
s_branch Lv2stub2_fused_block
s_branch Lv2stub3_fused_block
Lvar2_fused_block:
v_mfma_f32_16x16x32_bf16 v[128:131], a[64:67], a[32:35], v[184:187]
v_exp_f32 v212, v144
Mvaa 128,68,36
s_add_u32 s90, s34, 77824
GL 128,234,72,90,0
Mvaa 128,72,40
s_add_u32 s20, s25, 16777216
v_exp_f32 v213, v145
Mvaa 128,76,44
Lv2stub0_fused_block:
s_cbranch_execz Lv2stub4_fused_block
GL 132,234,72,90,1024
CV 168,212,213
v_mfma_f32_16x16x32_bf16 v[132:135], a[92:95], a[44:47], v[184:187]
v_exp_f32 v214, v146
Mvaa 132,88,40
GL 136,234,72,90,2048
Mvaa 132,84,36
s_add_u32 s21, s25, 16908288
v_exp_f32 v215, v147
Mvaa 132,80,32
GL 140,234,72,90,3072
CV 169,214,215
v_mfma_f32_16x16x32_bf16 v[136:139], a[96:99], a[32:35], v[184:187]
s_add_u32 s90, s34, 208896
GL 144,234,72,90,0
v_exp_f32 v212, v148
Mvaa 136,100,36
Mvaa 136,104,40
s_add_u32 s22, s26, 16777216
GL 148,234,72,90,1024
v_exp_f32 v213, v149
Mvaa 136,108,44
CV 170,212,213
v_mfma_f32_16x16x32_bf16 v[140:143], a[124:127], a[44:47], v[184:187]
GL 152,234,72,90,2048
v_exp_f32 v214, v150
Mvaa 140,120,40
Mvaa 140,116,36
s_add_u32 s23, s26, 16908288
GL 156,234,72,90,3072
v_exp_f32 v215, v151
Mvaa 140,112,32
s_add_u32 s90, s34, 81920
GL 160,234,72,90,0
CV 171,214,215
Mvav 0,192,160
v_exp_f32 v212, v152
Mvav 0,196,164
GL 164,234,72,90,1024
Mvav 4,200,160
v_exp_f32 v213, v153
Mvav 4,204,164
GL 168,234,72,90,2048
CV 172,212,213
Mvav 8,208,160
v_exp_f32 v214, v154
Mvav 8,212,164
GL 172,234,72,90,3072
Mvav 12,216,160
s_add_u32 s90, s34, 212992
GL 176,234,72,90,0
v_exp_f32 v215, v155
Mvav 12,220,164
CV 173,214,215
Mvav 16,224,160
GL 180,234,72,90,1024
v_exp_f32 v212, v156
Mvav 16,228,164
Mvav 20,232,160
GL 184,234,72,90,2048
v_exp_f32 v213, v157
Mvav 20,236,164
CV 174,212,213
Mvav 24,240,160
GL 188,234,72,90,3072
v_exp_f32 v214, v158
Mvav 24,244,164
Mvav 28,248,160
v_exp_f32 v215, v159
Mvav 28,252,164
CV 175,214,215
v_smfmac_f32_16x16x64_bf16 v[192:195], v[208:211], v[160:167], v239
v_mfma_f32_16x16x32_bf16 v[144:147], a[64:67], a[48:51], v[188:191]
v_exp_f32 v212, v128
Mvaa 144,68,52
Mvaa 144,72,56
v_exp_f32 v213, v129
Mvaa 144,76,60
CV 160,212,213
v_mfma_f32_16x16x32_bf16 v[148:151], a[92:95], a[60:63], v[188:191]
v_exp_f32 v214, v130
Mvaa 148,88,56
Mvaa 148,84,52
v_exp_f32 v215, v131
v_add_f32 v192, v192, v193
Mvaa 148,80,48
CV 161,214,215
v_rcp_f32 v193, v192
v_mfma_f32_16x16x32_bf16 v[152:155], a[96:99], a[48:51], v[188:191]
v_exp_f32 v212, v132
v_mov_b32 v194, v194
Mvaa 152,100,52
v_mul_f32 v0, v0, v193
v_mul_f32 v1, v1, v193
v_mul_f32 v2, v2, v193
Mvaa 152,104,56
v_exp_f32 v213, v133
v_mul_f32 v3, v3, v193
Mvaa 152,108,60
CV 162,212,213
v_mul_f32 v4, v4, v193
v_mul_f32 v5, v5, v193
v_mfma_f32_16x16x32_bf16 v[156:159], a[124:127], a[60:63], v[188:191]
v_exp_f32 v214, v134
v_mul_f32 v6, v6, v193
Mvaa 156,120,56
v_mul_f32 v7, v7, v193
CV 0,0,1
CV 1,2,3
Mvaa 156,116,52
v_exp_f32 v215, v135
CV 2,4,5
Mvaa 156,112,48
CV 163,214,215
CV 3,6,7
v_mul_f32 v8, v8, v193
Mvav 32,192,168
buffer_store_dwordx4 v[0:3], v234, s[72:75], s20 offen offset:0 sc1
v_exp_f32 v212, v136
v_mul_f32 v9, v9, v193
Mvav 32,196,172
v_mul_f32 v10, v10, v193
v_mul_f32 v11, v11, v193
v_mul_f32 v12, v12, v193
Mvav 36,200,168
v_exp_f32 v213, v137
v_mul_f32 v13, v13, v193
Mvav 36,204,172
CV 164,212,213
v_mul_f32 v14, v14, v193
v_mul_f32 v15, v15, v193
Mvav 40,208,168
v_exp_f32 v214, v138
CV 8,8,9
Mvav 40,212,172
CV 9,10,11
CV 10,12,13
CV 11,14,15
Mvav 44,216,168
buffer_store_dwordx4 v[8:11], v234, s[72:75], s20 offen offset:1024 sc1
v_exp_f32 v215, v139
v_mul_f32 v16, v16, v193
Mvav 44,220,172
CV 165,214,215
v_mul_f32 v17, v17, v193
v_mul_f32 v18, v18, v193
Mvav 48,224,168
v_exp_f32 v212, v140
v_mul_f32 v19, v19, v193
Mvav 48,228,172
v_mul_f32 v20, v20, v193
v_mul_f32 v21, v21, v193
v_mul_f32 v22, v22, v193
Mvav 52,232,168
v_exp_f32 v213, v141
)"
R"(v_mul_f32 v23, v23, v193
Mvav 52,236,172
CV 166,212,213
CV 16,16,17
CV 17,18,19
Mvav 56,240,168
v_exp_f32 v214, v142
CV 18,20,21
Mvav 56,244,172
CV 19,22,23
v_mul_f32 v24, v24, v193
v_mul_f32 v25, v25, v193
Mvav 60,248,168
buffer_store_dwordx4 v[16:19], v234, s[72:75], s20 offen offset:2048 sc1
v_exp_f32 v215, v143
v_mul_f32 v26, v26, v193
Mvav 60,252,172
CV 167,214,215
v_mul_f32 v27, v27, v193
v_mul_f32 v28, v28, v193
v_smfmac_f32_16x16x64_bf16 v[196:199], v[208:211], v[168:175], v239
v_mul_f32 v29, v29, v193
v_mul_f32 v30, v30, v193
v_mul_f32 v31, v31, v193
Mvav 64,192,160
v_exp_f32 v212, v144
CV 24,24,25
Mvav 64,196,164
v_exp_f32 v213, v145
CV 25,26,27
Mvav 68,200,160
v_exp_f32 v214, v146
CV 168,212,213
Mvav 68,204,164
v_exp_f32 v215, v147
CV 26,28,29
Mvav 72,208,160
v_exp_f32 v212, v148
CV 169,214,215
Mvav 72,212,164
v_exp_f32 v213, v149
CV 27,30,31
Mvav 76,216,160
buffer_store_dwordx4 v[24:27], v234, s[72:75], s20 offen offset:3072 sc1
v_exp_f32 v214, v150
CV 170,212,213
Mvav 76,220,164
s_add_u32 s90, s34, 86016
GL 96,234,72,90,0
v_exp_f32 v215, v151
v_add_f32 v196, v196, v197
Mvav 80,224,160
v_exp_f32 v212, v152
CV 171,214,215
Mvav 80,228,164
GL 100,234,72,90,1024
v_exp_f32 v213, v153
Mvav 84,232,160
v_exp_f32 v214, v154
CV 172,212,213
Mvav 84,236,164
GL 104,234,72,90,2048
v_exp_f32 v215, v155
Mvav 88,240,160
v_exp_f32 v212, v156
CV 173,214,215
Mvav 88,244,164
GL 108,234,72,90,3072
v_exp_f32 v213, v157
Mvav 92,248,160
s_add_u32 s90, s34, 217088
GL 112,234,72,90,0
v_exp_f32 v214, v158
CV 174,212,213
Mvav 92,252,164
v_exp_f32 v215, v159
v_smfmac_f32_16x16x64_bf16 v[200:203], v[208:211], v[160:167], v239
GL 116,234,72,90,1024
CV 175,214,215
s_nop 0
Mvav 96,192,168
v_rcp_f32 v197, v196
v_add_f32 v192, v192, v196
Mvav 96,196,172
GL 120,234,72,90,2048
v_mul_f32 v32, v32, v197
v_mul_f32 v33, v33, v197
v_mul_f32 v34, v34, v197
Mvav 100,200,168
v_mul_f32 v35, v35, v197
v_mul_f32 v36, v36, v197
v_mul_f32 v37, v37, v197
Mvav 100,204,172
GL 124,234,72,90,3072
v_mul_f32 v38, v38, v197
v_mul_f32 v39, v39, v197
CV 32,32,33
Mvav 104,208,168
CV 33,34,35
CV 34,36,37
CV 35,38,39
Mvav 104,212,172
buffer_store_dwordx4 v[32:35], v234, s[72:75], s21 offen offset:0 sc1
TR 64,241,16384
v_mul_f32 v40, v40, v197
v_mul_f32 v41, v41, v197
Mvav 108,216,168
TR 66,241,20480
v_mul_f32 v42, v42, v197
v_mul_f32 v43, v43, v197
Mvav 108,220,172
TR 68,241,24576
v_mul_f32 v44, v44, v197
v_mul_f32 v45, v45, v197
Mvav 112,224,168
TR 70,241,28672
v_mul_f32 v46, v46, v197
v_mul_f32 v47, v47, v197
Mvav 112,228,172
TR 72,242,16384
CV 40,40,41
CV 41,42,43
Mvav 116,232,168
TR 74,242,20480
CV 42,44,45
CV 43,46,47
Mvav 116,236,172
TR 76,242,24576
v_mul_f32 v48, v48, v197
v_mul_f32 v49, v49, v197
Mvav 120,240,168
buffer_store_dwordx4 v[40:43], v234, s[72:75], s21 offen offset:1024 sc1
TR 78,242,28672
v_mul_f32 v50, v50, v197
v_mul_f32 v51, v51, v197
Mvav 120,244,172
TR 80,243,16384
v_mul_f32 v52, v52, v197
v_mul_f32 v53, v53, v197
Mvav 124,248,168
TR 82,243,20480
v_mul_f32 v54, v54, v197
v_mul_f32 v55, v55, v197
Mvav 124,252,172
TR 84,243,24576
CV 48,48,49
CV 49,50,51
v_smfmac_f32_16x16x64_bf16 v[204:207], v[208:211], v[168:175], v239
s_waitcnt vmcnt(22) lgkmcnt(7)
TR 86,243,28672
CV 50,52,53
v_mfma_f32_16x16x32_bf16 v[168:171], a[64:67], a[128:131], 0
TR 88,244,16384
CV 51,54,55
v_mul_f32 v56, v56, v197
Mvaa 168,68,132
TR 90,244,20480
v_mul_f32 v57, v57, v197
v_mul_f32 v58, v58, v197
v_mfma_f32_16x16x32_bf16 v[172:175], a[64:67], a[144:147], 0
TR 92,244,24576
v_mul_f32 v59, v59, v197
v_mul_f32 v60, v60, v197
Mvaa 172,68,148
WL 7
TR 94,244,28672
v_mul_f32 v61, v61, v197
v_mfma_f32_16x16x32_bf16 v[180:183], a[72:75], a[144:147], 0
TR 64,245,16384
v_mul_f32 v62, v62, v197
v_mul_f32 v63, v63, v197
Mvaa 180,76,148
TR 66,245,20480
CV 56,56,57
CV 57,58,59
v_mfma_f32_16x16x32_bf16 v[176:179], a[72:75], a[128:131], 0
buffer_store_dwordx4 v[48:51], v234, s[72:75], s21 offen offset:2048 sc1
TR 68,245,24576
CV 58,60,61
CV 59,62,63
Mvaa 176,76,132
WL 7
TR 70,245,28672
v_add_f32 v200, v200, v201
v_mfma_f32_16x16x32_bf16 v[184:187], a[80:83], a[128:131], 0
TR 72,246,16384
v_rcp_f32 v201, v200
Mvaa 184,84,132
TR 74,246,20480
v_add_f32 v192, v192, v200
v_mul_f32 v64, v64, v201
v_mfma_f32_16x16x32_bf16 v[188:191], a[80:83], a[144:147], 0
TR 76,246,24576
v_mul_f32 v65, v65, v201
v_mul_f32 v66, v66, v201
Mvaa 188,84,148
WL 7
TR 78,246,28672
v_mul_f32 v67, v67, v201
v_mfma_f32_16x16x32_bf16 v[212:215], a[88:91], a[144:147], 0
TR 80,247,16384
v_mul_f32 v68, v68, v201
v_mul_f32 v69, v69, v201
Mvaa 212,92,148
TR 82,247,20480
v_mul_f32 v70, v70, v201
v_mul_f32 v71, v71, v201
v_mfma_f32_16x16x32_bf16 v[208:211], a[88:91], a[128:131], 0
TR 84,247,24576
CV 64,64,65
CV 65,66,67
Mvaa 208,92,132
WL 7
TR 86,247,28672
CV 66,68,69
v_mfma_f32_16x16x32_bf16 v[128:131], a[64:67], a[128:131], 0
TR 88,248,16384
CV 67,70,71
v_mul_f32 v72, v72, v201
Mvaa 128,68,132
buffer_store_dwordx4 v[56:59], v234, s[72:75], s21 offen offset:3072 sc1
TR 90,248,20480
v_mul_f32 v73, v73, v201
v_mul_f32 v74, v74, v201
v_mfma_f32_16x16x32_bf16 v[132:135], a[64:67], a[144:147], 0
TR 92,248,24576
v_mul_f32 v75, v75, v201
v_mul_f32 v76, v76, v201
Mvaa 132,68,148
WL 7
TR 94,248,28672
v_mul_f32 v77, v77, v201
v_mfma_f32_16x16x32_bf16 v[140:143], a[72:75], a[144:147], 0
TR 64,240,4096
v_mul_f32 v78, v78, v201
v_mul_f32 v79, v79, v201
Mvaa 140,76,148
TR 66,240,4608
CV 72,72,73
CV 73,74,75
v_mfma_f32_16x16x32_bf16 v[136:139], a[72:75], a[128:131], 0
TR 68,240,5120
CV 74,76,77
CV 75,78,79
Mvaa 136,76,132
WL 7
TR 70,240,5632
v_mul_f32 v80, v80, v201
v_mfma_f32_16x16x32_bf16 v[152:155], a[80:83], a[128:131], 0
TR 72,220,16384
v_mul_f32 v81, v81, v201
v_mul_f32 v82, v82, v201
Mvaa 152,84,132
TR 74,220,20480
v_mul_f32 v83, v83, v201
v_mul_f32 v84, v84, v201
v_mfma_f32_16x16x32_bf16 v[156:159], a[80:83], a[144:147], 0
TR 76,220,24576
v_mul_f32 v85, v85, v201
v_mul_f32 v86, v86, v201
Mvaa 156,84,148
WL 7
TR 78,220,28672
v_mul_f32 v87, v87, v201
v_mfma_f32_16x16x32_bf16 v[164:167], a[88:91], a[144:147], 0
s_add_u32 m0, s84, 0
TR 80,221,16384
CV 80,80,81
CV 81,82,83
Mvaa 164,92,148
DM 235,64,87,0
TR 82,221,20480
CV 82,84,85
CV 83,86,87
v_mfma_f32_16x16x32_bf16 v[160:163], a[88:91], a[128:131], 0
TR 84,221,24576
v_mul_f32 v88, v88, v201
v_mul_f32 v89, v89, v201
Mvaa 160,92,132
WL 7
DM 236,64,87,1024
TR 86,221,28672
v_mul_f32 v90, v90, v201
v_mfma_f32_16x16x32_bf16 v[148:151], a[64:67], a[128:131], 0
TR 88,222,16384
v_mul_f32 v91, v91, v201
v_mul_f32 v92, v92, v201
Mvaa 148,68,132
TR 90,222,20480
v_mul_f32 v93, v93, v201
v_mul_f32 v94, v94, v201
v_mfma_f32_16x16x32_bf16 v[144:147], a[64:67], a[144:147], 0
TR 92,222,24576
v_mul_f32 v95, v95, v201
CV 88,88,89
Mvaa 144,68,148
WL 7
DM 237,64,87,2048
TR 94,222,28672
CV 89,90,91
Mvaa 172,72,152
TR 64,223,16384
CV 90,92,93
CV 91,94,95
Mvaa 172,76,156
TR 66,223,20480
v_add_f32 v204, v204, v205
Mvaa 168,72,136
DM 238,64,87,3072
TR 68,223,24576
v_rcp_f32 v205, v204
Mvaa 168,76,140
WL 7
TR 70,223,28672
v_add_f32 v192, v192, v204
Mvaa 176,80,136
TR 72,224,16384
v_cmp_nge_f32 vcc, 0x53800000, v192
s_lshl_b32 s96, s19, 2
s_add_u32 s96, s96, 147456
s_cmp_lg_u64 vcc, 0
s_cselect_b32 s95, 1, 0
v_mov_b32 v249, s96
v_mov_b32 v250, s95
ds_write_b32 v249, v250
v_mov_b32 v249, 147456
Mvaa 176,84,140
TR 74,224,20480
v_mul_f32 v96, v96, v205
v_mul_f32 v97, v97, v205
Mvaa 180,80,152
s_add_u32 m0, s85, 0
TR 76,224,24576
v_mul_f32 v98, v98, v205
v_mul_f32 v99, v99, v205
Mvaa 180,84,156
WL 8
DM 235,64,88,0
TR 78,224,28672
v_mul_f32 v100, v100, v205
Mvaa 188,88,152
TR 80,225,16384
v_mul_f32 v101, v101, v205
v_mul_f32 v102, v102, v205
Mvaa 188,92,156
DM 236,64,88,1024
TR 82,225,20480
v_mul_f32 v103, v103, v205
CV 96,96,97
Mvaa 184,88,136
TR 84,225,24576
CV 97,98,99
CV 98,100,101
Mvaa 184,92,140
WL 8
TR 86,225,28672
CV 99,102,103
Mvaa 208,64,136
TR 88,226,16384
v_mul_f32 v104, v104, v205
v_mul_f32 v105, v105, v205
Mvaa 208,68,140
DM 237,64,88,2048
TR 90,226,20480
v_mul_f32 v106, v106, v205
v_mul_f32 v107, v107, v205
Mvaa 212,64,152
TR 92,226,24576
v_mul_f32 v108, v108, v205
v_mul_f32 v109, v109, v205
Mvaa 212,68,156
WL 7
TR 94,226,28672
v_mul_f32 v110, v110, v205
Mvaa 132,72,152
DM 238,64,88,3072
TR 64,227,16384
v_mul_f32 v111, v111, v205
CV 104,104,105
Mvaa 132,76,156
TR 66,227,20480
CV 105,106,107
CV 106,108,109
Mvaa 128,72,136
TR 68,227,24576
CV 107,110,111
v_mul_f32 v112, v112, v205
Mvaa 128,76,140
WL 7
TR 70,227,28672
v_mul_f32 v113, v113, v205
Mvaa 136,80,136
s_add_u32 m0, s86, 0
TR 72,240,6144
v_mul_f32 v114, v114, v205
v_mul_f32 v115, v115, v205
Mvaa 136,84,140
buffer_load_dwordx4 v234, s[72:75], s89 offen offset:0 sc1 lds
TR 74,240,6656
v_mul_f32 v116, v116, v205
)"
R"(v_mul_f32 v117, v117, v205
Mvaa 140,80,152
TR 76,240,7168
v_mul_f32 v118, v118, v205
v_mul_f32 v119, v119, v205
Mvaa 140,84,156
WL 7
s_add_u32 s90, s34, 90112
GL 192,234,72,90,0
TR 78,240,7680
CV 112,112,113
Mvaa 156,88,152
WL 0
s_barrier
TR 80,241,32768
CV 113,114,115
Mvaa 156,92,156
TR 82,241,36864
CV 114,116,117
CV 115,118,119
Mvaa 152,88,136
TR 84,241,40960
ds_read_b128 v[4:7], v249
v_mul_f32 v120, v120, v205
v_mul_f32 v121, v121, v205
Mvaa 152,92,140
GL 196,234,72,90,1024
TR 86,241,45056
v_mul_f32 v122, v122, v205
Mvaa 160,64,136
TR 88,242,32768
v_mul_f32 v123, v123, v205
v_mul_f32 v124, v124, v205
Mvaa 160,68,140
TR 90,242,36864
v_mul_f32 v125, v125, v205
v_mul_f32 v126, v126, v205
Mvaa 164,64,152
GL 200,234,72,90,2048
TR 92,242,40960
v_mul_f32 v127, v127, v205
CV 120,120,121
Mvaa 164,68,156
TR 94,242,45056
CV 121,122,123
Mvaa 144,72,152
TR 64,243,32768
CV 122,124,125
CV 123,126,127
Mvaa 144,76,156
TR 66,243,36864
Mvaa 148,72,136
GL 204,234,72,90,3072
TR 68,243,40960
Mvaa 148,76,140
s_waitcnt vmcnt(29) lgkmcnt(7)
TR 70,243,45056
Mvaa 168,80,160
TR 72,244,32768
Mvaa 168,84,164
s_add_u32 s90, s34, 221184
GL 208,234,72,90,0
TR 74,244,36864
Mvaa 172,80,176
Lv2stub1_fused_block:
s_cbranch_execz Lv2stub5_fused_block
TR 76,244,40960
v_or3_b32 v249, v4, v5, v6
v_or_b32 v249, v249, v7
s_nop 0
v_readfirstlane_b32 s97, v249
s_nop 3
s_andn2_b32 s97, s97, s94
s_cbranch_scc1 Lretry_fused_block
Mvaa 172,84,180
WL 7
TR 78,244,45056
Mvaa 180,88,176
TR 80,245,32768
Mvaa 180,92,180
GL 212,234,72,90,1024
TR 82,245,36864
Mvaa 176,88,160
TR 84,245,40960
Mvaa 176,92,164
WL 7
TR 86,245,45056
Mvaa 184,64,160
GL 216,234,72,90,2048
TR 88,246,32768
Mvaa 184,68,164
TR 90,246,36864
Mvaa 188,64,176
TR 92,246,40960
Mvaa 188,68,180
WL 7
TR 94,246,45056
Mvaa 212,72,176
GL 220,234,72,90,3072
TR 64,247,32768
Mvaa 212,76,180
TR 66,247,36864
Mvaa 208,72,160
TR 68,247,40960
Mvaa 208,76,164
WL 7
buffer_store_dwordx4 v[64:67], v234, s[72:75], s22 offen offset:0 sc1
TR 70,247,45056
Mvaa 128,80,160
TR 72,248,32768
Mvaa 128,84,164
TR 74,248,36864
Mvaa 132,80,176
TR 76,248,40960
Mvaa 132,84,180
WL 7
TR 78,248,45056
Mvaa 140,88,176
TR 80,240,8192
Mvaa 140,92,180
TR 82,240,8704
Mvaa 136,88,160
TR 84,240,9216
Mvaa 136,92,164
WL 7
TR 86,240,9728
Mvaa 152,64,160
TR 88,220,32768
Mvaa 152,68,164
TR 90,220,36864
Mvaa 156,64,176
buffer_store_dwordx4 v[72:75], v234, s[72:75], s22 offen offset:1024 sc1
TR 92,220,40960
Mvaa 156,68,180
WL 7
TR 94,220,45056
Mvaa 164,72,176
TR 64,221,32768
Mvaa 164,76,180
TR 66,221,36864
Mvaa 160,72,160
TR 68,221,40960
Mvaa 160,76,164
WL 7
TR 70,221,45056
Mvaa 148,80,160
TR 72,222,32768
Mvaa 148,84,164
TR 74,222,36864
Mvaa 144,80,176
TR 76,222,40960
Mvaa 144,84,180
WL 7
TR 78,222,45056
Mvaa 172,88,184
TR 80,223,32768
Mvaa 172,92,188
buffer_store_dwordx4 v[80:83], v234, s[72:75], s22 offen offset:2048 sc1
TR 82,223,36864
Mvaa 168,88,168
TR 84,223,40960
Mvaa 168,92,172
WL 7
TR 86,223,45056
Mvaa 176,64,168
TR 88,224,32768
Mvaa 176,68,172
TR 90,224,36864
Mvaa 180,64,184
TR 92,224,40960
Mvaa 180,68,188
WL 7
TR 94,224,45056
Mvaa 188,72,184
TR 64,225,32768
Mvaa 188,76,188
TR 66,225,36864
Mvaa 184,72,168
TR 68,225,40960
Mvaa 184,76,172
WL 7
TR 70,225,45056
Mvaa 208,80,168
buffer_store_dwordx4 v[88:91], v234, s[72:75], s22 offen offset:3072 sc1
TR 72,226,32768
Mvaa 208,84,172
TR 74,226,36864
Mvaa 212,80,184
TR 76,226,40960
Mvaa 212,84,188
WL 7
TR 78,226,45056
Mvaa 132,88,184
TR 80,227,32768
Mvaa 132,92,188
TR 82,227,36864
Mvaa 128,88,168
TR 84,227,40960
Mvaa 128,92,172
WL 7
TR 86,227,45056
Mvaa 136,64,168
TR 88,240,10240
Mvaa 136,68,172
TR 90,240,10752
Mvaa 140,64,184
TR 92,240,11264
Mvaa 140,68,188
WL 7
s_add_u32 s87, s28, 3391488
s_add_u32 s88, s28, 3465216
TR 94,240,11776
Mvaa 156,72,184
WL 0
s_barrier
s_add_u32 s89, s29, 94208
TR 64,241,49152
Mvaa 156,76,188
s_add_u32 m0, s84, 16384
TR 66,241,53248
Mvaa 152,72,168
DM 235,64,87,0
TR 68,241,57344
Mvaa 152,76,172
TR 70,241,61440
Mvaa 160,80,168
DM 236,64,87,1024
TR 72,242,49152
Mvaa 160,84,172
TR 74,242,53248
Mvaa 164,80,184
DM 237,64,87,2048
TR 76,242,57344
Mvaa 164,84,188
TR 78,242,61440
Mvaa 144,88,184
TR 80,243,49152
Mvaa 144,92,188
TR 82,243,53248
Mvaa 148,88,168
DM 238,64,87,3072
TR 84,243,57344
Mvaa 148,92,172
s_waitcnt vmcnt(29) lgkmcnt(7)
TR 86,243,61440
Mvaa 168,64,96
TR 88,244,49152
Mvaa 168,68,100
s_add_u32 m0, s85, 16384
TR 90,244,53248
Mvaa 172,64,112
DM 235,64,88,0
TR 92,244,57344
Mvaa 172,68,116
WL 7
TR 94,244,61440
Mvaa 180,72,112
TR 64,245,49152
Mvaa 180,76,116
DM 236,64,88,1024
TR 66,245,53248
Mvaa 176,72,96
TR 68,245,57344
Mvaa 176,76,100
WL 7
TR 70,245,61440
Mvaa 184,80,96
DM 237,64,88,2048
TR 72,246,49152
Mvaa 184,84,100
TR 74,246,53248
Mvaa 188,80,112
TR 76,246,57344
Mvaa 188,84,116
WL 7
TR 78,246,61440
Mvaa 212,88,112
DM 238,64,88,3072
TR 80,247,49152
Mvaa 212,92,116
TR 82,247,53248
Mvaa 208,88,96
TR 84,247,57344
Mvaa 208,92,100
WL 7
s_add_u32 m0, s86, 4096
TR 86,247,61440
Mvaa 128,64,96
buffer_load_dwordx4 v234, s[72:75], s89 offen offset:0 sc1 lds
TR 88,248,49152
Mvaa 128,68,100
TR 90,248,53248
Mvaa 132,64,112
TR 92,248,57344
Mvaa 132,68,116
WL 7
s_add_u32 s90, s34, 94208
GL 224,234,72,90,0
TR 94,248,61440
Mvaa 140,72,112
TR 64,240,12288
Mvaa 140,76,116
TR 66,240,12800
Mvaa 136,72,96
GL 228,234,72,90,1024
TR 68,240,13312
Mvaa 136,76,100
WL 7
TR 70,240,13824
Mvaa 152,80,96
TR 72,220,49152
Mvaa 152,84,100
TR 74,220,53248
Mvaa 156,80,112
GL 232,234,72,90,2048
TR 76,220,57344
Mvaa 156,84,116
WL 7
TR 78,220,61440
Mvaa 164,88,112
TR 80,221,49152
Mvaa 164,92,116
GL 236,234,72,90,3072
TR 82,221,53248
Mvaa 160,88,96
TR 84,221,57344
Mvaa 160,92,100
WL 7
TR 86,221,61440
Mvaa 148,64,96
TR 88,222,49152
Mvaa 148,68,100
s_add_u32 s90, s34, 225280
GL 240,234,72,90,0
TR 90,222,53248
Mvaa 144,64,112
TR 92,222,57344
Mvaa 144,68,116
WL 7
TR 94,222,61440
Mvaa 172,72,120
GL 244,234,72,90,1024
TR 64,223,49152
Mvaa 172,76,124
TR 66,223,53248
Mvaa 168,72,104
TR 68,223,57344
Mvaa 168,76,108
WL 7
TR 70,223,61440
Mvaa 176,80,104
GL 248,234,72,90,2048
TR 72,224,49152
Mvaa 176,84,108
TR 74,224,53248
Mvaa 180,80,120
TR 76,224,57344
Mvaa 180,84,124
WL 7
GL 252,234,72,90,3072
TR 78,224,61440
Mvaa 188,88,120
TR 80,225,49152
Mvaa 188,92,124
TR 82,225,53248
Mvaa 184,88,104
TR 84,225,57344
Mvaa 184,92,108
WL 7
buffer_store_dwordx4 v[96:99], v234, s[72:75], s23 offen offset:0 sc1
TR 86,225,61440
Mvaa 208,64,104
TR 88,226,49152
Mvaa 208,68,108
TR 90,226,53248
Mvaa 212,64,120
TR 92,226,57344
Mvaa 212,68,124
WL 7
TR 94,226,61440
Mvaa 132,72,120
TR 64,227,49152
Mvaa 132,76,124
TR 66,227,53248
Mvaa 128,72,104
TR 68,227,57344
Mvaa 128,76,108
WL 7
TR 70,227,61440
Mvaa 136,80,104
TR 72,240,14336
Mvaa 136,84,108
TR 74,240,14848
Mvaa 140,80,120
buffer_store_dwordx4 v[104:107], v234, s[72:75], s23 offen offset:1024 sc1
TR 76,240,15360
Mvaa 140,84,124
WL 7
TR 78,240,15872
Mvaa 156,88,120
)"
R"(s_waitcnt vmcnt(31) lgkmcnt(0)
s_barrier
TR 80,241,0
Mvaa 156,92,124
TR 82,241,4096
Mvaa 152,88,104
TR 84,241,8192
Mvaa 152,92,108
TR 86,241,12288
Mvaa 160,64,104
TR 88,242,0
Mvaa 160,68,108
TR 90,242,4096
Mvaa 164,64,120
TR 92,242,8192
Mvaa 164,68,124
TR 94,242,12288
Mvaa 144,72,120
TR 64,243,0
Mvaa 144,76,124
buffer_store_dwordx4 v[112:115], v234, s[72:75], s23 offen offset:2048 sc1
TR 66,243,4096
Mvaa 148,72,104
TR 68,243,8192
Mvaa 148,76,108
s_waitcnt vmcnt(24) lgkmcnt(7)
TR 70,243,12288
Mvaa 168,80,192
TR 72,244,0
Mvaa 168,84,196
TR 74,244,4096
Mvaa 172,80,208
TR 76,244,8192
Mvaa 172,84,212
WL 7
TR 78,244,12288
Mvaa 180,88,208
TR 80,245,0
Mvaa 180,92,212
TR 82,245,4096
Mvaa 176,88,192
TR 84,245,8192
Mvaa 176,92,196
WL 7
TR 86,245,12288
Mvaa 184,64,192
buffer_store_dwordx4 v[120:123], v234, s[72:75], s23 offen offset:3072 sc1
TR 88,246,0
Mvaa 184,68,196
TR 90,246,4096
Mvaa 188,64,208
TR 92,246,8192
Mvaa 188,68,212
WL 7
TR 94,246,12288
Mvaa 212,72,208
TR 64,247,0
Mvaa 212,76,212
TR 66,247,4096
Mvaa 208,72,192
TR 68,247,8192
Mvaa 208,76,196
WL 7
TR 70,247,12288
Mvaa 128,80,192
TR 72,248,0
Mvaa 128,84,196
TR 74,248,4096
Mvaa 132,80,208
TR 76,248,8192
Mvaa 132,84,212
WL 7
s_add_u32 s87, s28, 3538944
s_add_u32 s88, s28, 3612672
TR 78,248,12288
Mvaa 140,88,208
s_add_u32 s89, s29, 98304
s_add_u32 m0, s84, 32768
TR 80,240,0
Mvaa 140,92,212
DM 235,64,87,0
TR 82,240,512
Mvaa 136,88,192
TR 84,240,1024
Mvaa 136,92,196
WL 7
DM 236,64,87,1024
TR 86,240,1536
Mvaa 152,64,192
TR 88,220,0
Mvaa 152,68,196
TR 90,220,4096
Mvaa 156,64,208
DM 237,64,87,2048
TR 92,220,8192
Mvaa 156,68,212
WL 7
TR 94,220,12288
Mvaa 164,72,208
TR 64,221,0
Mvaa 164,76,212
DM 238,64,87,3072
TR 66,221,4096
Mvaa 160,72,192
TR 68,221,8192
Mvaa 160,76,196
WL 7
TR 70,221,12288
Mvaa 148,80,192
TR 72,222,0
Mvaa 148,84,196
s_add_u32 m0, s85, 32768
TR 74,222,4096
Mvaa 144,80,208
DM 235,64,88,0
TR 76,222,8192
Mvaa 144,84,212
WL 7
TR 78,222,12288
Mvaa 172,88,216
DM 236,64,88,1024
TR 80,223,0
Mvaa 172,92,220
TR 82,223,4096
Mvaa 168,88,200
TR 84,223,8192
Mvaa 168,92,204
WL 7
TR 86,223,12288
Mvaa 176,64,200
DM 237,64,88,2048
TR 88,224,0
Mvaa 176,68,204
TR 90,224,4096
Mvaa 180,64,216
TR 92,224,8192
Mvaa 180,68,220
WL 7
DM 238,64,88,3072
TR 94,224,12288
Mvaa 188,72,216
TR 64,225,0
Mvaa 188,76,220
TR 66,225,4096
Mvaa 184,72,200
TR 68,225,8192
Mvaa 184,76,204
WL 7
s_add_u32 m0, s86, 8192
TR 70,225,12288
Mvaa 208,80,200
buffer_load_dwordx4 v234, s[72:75], s89 offen offset:0 sc1 lds
TR 72,226,0
Mvaa 208,84,204
TR 74,226,4096
Mvaa 212,80,216
s_add_u32 s90, s34, 98304
GL 128,234,72,90,0
TR 76,226,8192
Mvaa 212,84,220
WL 7
TR 78,226,12288
Mvaa 132,88,216
TR 80,227,0
Mvaa 132,92,220
TR 82,227,4096
Mvaa 128,88,200
GL 132,234,72,90,1024
TR 84,227,8192
Mvaa 128,92,204
WL 7
TR 86,227,12288
Mvaa 136,64,200
TR 88,240,2048
Mvaa 136,68,204
GL 136,234,72,90,2048
TR 90,240,2560
Mvaa 140,64,216
TR 92,240,3072
Mvaa 140,68,220
WL 7
TR 94,240,3584
Mvaa 156,72,216
s_waitcnt vmcnt(24) lgkmcnt(0)
s_barrier
TR 64,241,16384
Mvaa 156,76,220
GL 140,234,72,90,3072
TR 66,241,20480
Mvaa 152,72,200
TR 68,241,24576
Mvaa 152,76,204
TR 70,241,28672
Mvaa 160,80,200
s_add_u32 s90, s34, 229376
GL 144,234,72,90,0
TR 72,242,16384
Mvaa 160,84,204
TR 74,242,20480
Mvaa 164,80,216
TR 76,242,24576
Mvaa 164,84,220
TR 78,242,28672
Mvaa 144,88,216
GL 148,234,72,90,1024
TR 80,243,16384
Mvaa 144,92,220
Lv2stub2_fused_block:
s_cbranch_execz Lv2stub6_fused_block
TR 82,243,20480
Mvaa 148,88,200
TR 84,243,24576
Mvaa 148,92,204
s_waitcnt vmcnt(19) lgkmcnt(7)
GL 152,234,72,90,2048
TR 86,243,28672
Mvaa 168,64,224
TR 88,244,16384
Mvaa 168,68,228
TR 90,244,20480
Mvaa 172,64,240
TR 92,244,24576
Mvaa 172,68,244
WL 7
GL 156,234,72,90,3072
TR 94,244,28672
Mvaa 180,72,240
TR 64,245,16384
Mvaa 180,76,244
TR 66,245,20480
Mvaa 176,72,224
s_waitcnt vmcnt(17)
s_add_u32 s98, s37, 262144
TR 68,245,24576
v_mov_b32 v250, s82
Mvaa 176,76,228
WL 7
TR 70,245,28672
v_mov_b32 v251, s83
Mvaa 184,80,224
TR 72,246,16384
v_lshlrev_b32 v249, 3, v255
s_mov_b64 exec, 0xffff
buffer_store_dwordx2 v[250:251], v249, s[72:75], s98 offen sc1
s_mov_b64 exec, -1
Mvaa 184,84,228
s_add_u32 s87, s28, 3686400
s_add_u32 s88, s28, 3760128
s_add_u32 s89, s29, 102400
TR 74,246,20480
Mvaa 188,80,240
s_add_u32 m0, s84, 49152
TR 76,246,24576
Mvaa 188,84,244
WL 7
DM 235,64,87,0
TR 78,246,28672
Mvaa 212,88,240
TR 80,247,16384
Mvaa 212,92,244
DM 236,64,87,1024
TR 82,247,20480
Mvaa 208,88,224
TR 84,247,24576
Mvaa 208,92,228
WL 7
DM 237,64,87,2048
TR 86,247,28672
Mvaa 128,64,224
TR 88,248,16384
Mvaa 128,68,228
DM 238,64,87,3072
TR 90,248,20480
Mvaa 132,64,240
TR 92,248,24576
Mvaa 132,68,244
WL 7
TR 94,248,28672
Mvaa 140,72,240
s_add_u32 m0, s85, 49152
TR 64,240,4096
Mvaa 140,76,244
DM 235,64,88,0
TR 66,240,4608
Mvaa 136,72,224
TR 68,240,5120
Mvaa 136,76,228
WL 7
TR 70,240,5632
Mvaa 152,80,224
DM 236,64,88,1024
TR 72,220,16384
Mvaa 152,84,228
TR 74,220,20480
Mvaa 156,80,240
TR 76,220,24576
Mvaa 156,84,244
WL 7
DM 237,64,88,2048
TR 78,220,28672
Mvaa 164,88,240
TR 80,221,16384
Mvaa 164,92,244
TR 82,221,20480
Mvaa 160,88,224
TR 84,221,24576
Mvaa 160,92,228
WL 7
DM 238,64,88,3072
TR 86,221,28672
Mvaa 148,64,224
TR 88,222,16384
Mvaa 148,68,228
TR 90,222,20480
Mvaa 144,64,240
s_add_u32 m0, s86, 12288
TR 92,222,24576
Mvaa 144,68,244
WL 7
buffer_load_dwordx4 v234, s[72:75], s89 offen offset:0 sc1 lds
TR 94,222,28672
Mvaa 172,72,248
TR 64,223,16384
Mvaa 172,76,252
TR 66,223,20480
Mvaa 168,72,232
s_add_u32 s90, s34, 102400
GL 160,234,72,90,0
TR 68,223,24576
Mvaa 168,76,236
WL 7
TR 70,223,28672
Mvaa 176,80,232
TR 72,224,16384
Mvaa 176,84,236
GL 164,234,72,90,1024
TR 74,224,20480
Mvaa 180,80,248
TR 76,224,24576
Mvaa 180,84,252
WL 7
TR 78,224,28672
Mvaa 188,88,248
TR 80,225,16384
Mvaa 188,92,252
GL 168,234,72,90,2048
TR 82,225,20480
Mvaa 184,88,232
TR 84,225,24576
Mvaa 184,92,236
WL 7
TR 86,225,28672
Mvaa 208,64,232
GL 172,234,72,90,3072
TR 88,226,16384
Mvaa 208,68,236
TR 90,226,20480
Mvaa 212,64,248
TR 92,226,24576
Mvaa 212,68,252
WL 7
TR 94,226,28672
Mvaa 132,72,248
s_add_u32 s90, s34, 233472
GL 176,234,72,90,0
TR 64,227,16384
Mvaa 132,76,252
TR 66,227,20480
Mvaa 128,72,232
TR 68,227,24576
Mvaa 128,76,236
WL 7
GL 180,234,72,90,1024
TR 70,227,28672
Mvaa 136,80,232
TR 72,240,6144
Mvaa 136,84,236
TR 74,240,6656
Mvaa 140,80,248
TR 76,240,7168
Mvaa 140,84,252
WL 7
GL 184,234,72,90,2048
TR 78,240,7680
Mvaa 156,88,248
s_waitcnt vmcnt(25) lgkmcnt(0)
s_barrier
TR 80,241,32768
Mvaa 156,92,252
TR 82,241,36864
Mvaa 152,88,232
GL 188,234,72,90,3072
TR 84,241,40960
Mvaa 152,92,236
TR 86,241,45056
Mvaa 160,64,232
TR 88,242,32768
Mvaa 160,68,236
TR 90,242,36864
Mvaa 164,64,248
)"
R"(s_add_u32 s98, s36, 264192
TR 92,242,40960
v_and_b32 v249, 3, v255
Mvaa 164,68,252
TR 94,242,45056
v_lshlrev_b32 v249, 7, v249
Mvaa 144,72,248
buffer_load_dwordx2 v[250:251], v249, s[72:75], s98 offen sc1
TR 64,243,32768
Mvaa 144,76,252
s_add_u32 s87, s28, 5013504
s_add_u32 s88, s28, 5087232
s_add_u32 s89, s29, 139264
TR 66,243,36864
Mvaa 148,72,232
s_add_u32 m0, s84, 0
TR 68,243,40960
Mvaa 148,76,236
s_waitcnt vmcnt(19) lgkmcnt(7)
DM 235,64,87,0
TR 70,243,45056
Mvaa 168,80,128
TR 72,244,32768
Mvaa 168,84,132
DM 236,64,87,1024
TR 74,244,36864
Mvaa 172,80,144
TR 76,244,40960
Mvaa 172,84,148
WL 7
DM 237,64,87,2048
TR 78,244,45056
Mvaa 180,88,144
TR 80,245,32768
Mvaa 180,92,148
TR 82,245,36864
Mvaa 176,88,128
DM 238,64,87,3072
TR 84,245,40960
Mvaa 176,92,132
WL 7
TR 86,245,45056
Mvaa 184,64,128
TR 88,246,32768
Mvaa 184,68,132
s_add_u32 m0, s85, 0
TR 90,246,36864
Mvaa 188,64,144
DM 235,64,88,0
TR 92,246,40960
Mvaa 188,68,148
WL 7
TR 94,246,45056
Mvaa 212,72,144
TR 64,247,32768
Mvaa 212,76,148
DM 236,64,88,1024
TR 66,247,36864
Mvaa 208,72,128
TR 68,247,40960
Mvaa 208,76,132
WL 7
TR 70,247,45056
Mvaa 128,80,128
DM 237,64,88,2048
TR 72,248,32768
Mvaa 128,84,132
TR 74,248,36864
Mvaa 132,80,144
TR 76,248,40960
Mvaa 132,84,148
WL 7
TR 78,248,45056
Mvaa 140,88,144
DM 238,64,88,3072
TR 80,240,8192
Mvaa 140,92,148
TR 82,240,8704
Mvaa 136,88,128
TR 84,240,9216
Mvaa 136,92,132
WL 7
s_add_u32 m0, s86, 0
TR 86,240,9728
Mvaa 152,64,128
buffer_load_dwordx4 v234, s[72:75], s89 offen offset:0 sc1 lds
TR 88,220,32768
Mvaa 152,68,132
TR 90,220,36864
Mvaa 156,64,144
TR 92,220,40960
Mvaa 156,68,148
WL 7
s_add_u32 s90, s33, 8192
GL 96,234,72,90,0
TR 94,220,45056
Mvaa 164,72,144
TR 64,221,32768
Mvaa 164,76,148
TR 66,221,36864
Mvaa 160,72,128
GL 100,234,72,90,1024
TR 68,221,40960
Mvaa 160,76,132
WL 7
TR 70,221,45056
Mvaa 148,80,128
TR 72,222,32768
Mvaa 148,84,132
TR 74,222,36864
Mvaa 144,80,144
GL 104,234,72,90,2048
TR 76,222,40960
Mvaa 144,84,148
WL 7
TR 78,222,45056
Mvaa 172,88,152
TR 80,223,32768
Mvaa 172,92,156
GL 108,234,72,90,3072
TR 82,223,36864
Mvaa 168,88,136
TR 84,223,40960
Mvaa 168,92,140
WL 7
TR 86,223,45056
Mvaa 176,64,136
TR 88,224,32768
Mvaa 176,68,140
s_add_u32 s90, s33, 24576
GL 112,234,72,90,0
TR 90,224,36864
Mvaa 180,64,152
TR 92,224,40960
Mvaa 180,68,156
WL 7
TR 94,224,45056
Mvaa 188,72,152
GL 116,234,72,90,1024
TR 64,225,32768
Mvaa 188,76,156
TR 66,225,36864
Mvaa 184,72,136
TR 68,225,40960
Mvaa 184,76,140
WL 7
TR 70,225,45056
Mvaa 208,80,136
GL 120,234,72,90,2048
TR 72,226,32768
Mvaa 208,84,140
TR 74,226,36864
Mvaa 212,80,152
TR 76,226,40960
Mvaa 212,84,156
WL 7
GL 124,234,72,90,3072
TR 78,226,45056
Mvaa 132,88,152
TR 80,227,32768
Mvaa 132,92,156
TR 82,227,36864
Mvaa 128,88,136
TR 84,227,40960
Mvaa 128,92,140
WL 7
s_add_u32 s87, s28, 2359296
s_add_u32 s88, s28, 2433024
TR 86,227,45056
Mvaa 136,64,136
s_add_u32 s89, s29, 212992
s_add_u32 m0, s84, 16384
TR 88,240,10240
Mvaa 136,68,140
DM 235,56,87,0
TR 90,240,10752
Mvaa 140,64,152
TR 92,240,11264
Mvaa 140,68,156
WL 7
DM 236,56,87,1024
TR 94,240,11776
Mvaa 156,72,152
s_waitcnt vmcnt(28) lgkmcnt(0)
s_barrier
TR 64,241,49152
Mvaa 156,76,156
TR 66,241,53248
Mvaa 152,72,136
DM 237,56,87,2048
TR 68,241,57344
Mvaa 152,76,140
TR 70,241,61440
Mvaa 160,80,136
TR 72,242,49152
Mvaa 160,84,140
DM 238,56,87,3072
TR 74,242,53248
Mvaa 164,80,152
TR 76,242,57344
Mvaa 164,84,156
TR 78,242,61440
Mvaa 144,88,152
TR 80,243,49152
Mvaa 144,92,156
s_add_u32 m0, s85, 16384
TR 82,243,53248
Mvaa 148,88,136
DM 235,56,88,0
TR 84,243,57344
Mvaa 148,92,140
s_waitcnt vmcnt(23) lgkmcnt(7)
TR 86,243,61440
Mvaa 168,64,160
DM 236,56,88,1024
TR 88,244,49152
Mvaa 168,68,164
TR 90,244,53248
Mvaa 172,64,176
TR 92,244,57344
Mvaa 172,68,180
WL 7
TR 94,244,61440
Mvaa 180,72,176
DM 237,56,88,2048
TR 64,245,49152
Mvaa 180,76,180
TR 66,245,53248
Mvaa 176,72,160
TR 68,245,57344
Mvaa 176,76,164
WL 7
DM 238,56,88,3072
TR 70,245,61440
Mvaa 184,80,160
TR 72,246,49152
Mvaa 184,84,164
TR 74,246,53248
Mvaa 188,80,176
TR 76,246,57344
Mvaa 188,84,180
WL 7
s_add_u32 m0, s86, 4096
TR 78,246,61440
Mvaa 212,88,176
buffer_load_dwordx4 v234, s[72:75], s89 offen offset:0 sc1 lds
TR 80,247,49152
Mvaa 212,92,180
TR 82,247,53248
Mvaa 208,88,160
s_waitcnt vmcnt(26)
TR 84,247,57344
v_cmp_ne_u64 vcc, s[82:83], v[250:251]
s_nop 3
s_cbranch_vccnz Lslow6_fused_block
Lslowb6_fused_block:
Mvaa 208,92,164
WL 7
s_add_u32 s90, s34, 16842752
GL 192,234,72,90,0
TR 86,247,61440
Mvaa 128,64,160
TR 88,248,49152
Mvaa 128,68,164
TR 90,248,53248
Mvaa 132,64,176
GL 196,234,72,90,1024
TR 92,248,57344
Mvaa 132,68,180
WL 7
TR 94,248,61440
Mvaa 140,72,176
TR 64,240,12288
Mvaa 140,76,180
GL 200,234,72,90,2048
TR 66,240,12800
Mvaa 136,72,160
TR 68,240,13312
Mvaa 136,76,164
WL 7
TR 70,240,13824
Mvaa 152,80,160
TR 72,220,49152
Mvaa 152,84,164
GL 204,234,72,90,3072
TR 74,220,53248
Mvaa 156,80,176
TR 76,220,57344
Mvaa 156,84,180
WL 7
TR 78,220,61440
Mvaa 164,88,176
s_add_u32 s90, s34, 16973824
GL 208,234,72,90,0
TR 80,221,49152
Mvaa 164,92,180
TR 82,221,53248
Mvaa 160,88,160
TR 84,221,57344
Mvaa 160,92,164
WL 7
TR 86,221,61440
Mvaa 148,64,160
GL 212,234,72,90,1024
TR 88,222,49152
Mvaa 148,68,164
TR 90,222,53248
Mvaa 144,64,176
TR 92,222,57344
Mvaa 144,68,180
WL 7
GL 216,234,72,90,2048
TR 94,222,61440
Mvaa 172,72,184
TR 64,223,49152
Mvaa 172,76,188
TR 66,223,53248
Mvaa 168,72,168
TR 68,223,57344
Mvaa 168,76,172
WL 7
GL 220,234,72,90,3072
TR 70,223,61440
Mvaa 176,80,168
TR 72,224,49152
Mvaa 176,84,172
TR 74,224,53248
Mvaa 180,80,184
s_add_u32 s87, s28, 2506752
s_add_u32 s88, s28, 2580480
s_add_u32 s89, s29, 217088
TR 76,224,57344
Mvaa 180,84,188
WL 7
s_add_u32 m0, s84, 32768
TR 78,224,61440
Mvaa 188,88,184
DM 235,56,87,0
TR 80,225,49152
Mvaa 188,92,188
TR 82,225,53248
Mvaa 184,88,168
DM 236,56,87,1024
TR 84,225,57344
Mvaa 184,92,172
WL 7
TR 86,225,61440
Mvaa 208,64,168
TR 88,226,49152
Mvaa 208,68,172
DM 237,56,87,2048
TR 90,226,53248
Mvaa 212,64,184
Lv2stub3_fused_block:
s_cbranch_execz Lt2x_fused_block
TR 92,226,57344
Mvaa 212,68,188
WL 7
TR 94,226,61440
Mvaa 132,72,184
TR 64,227,49152
Mvaa 132,76,188
DM 238,56,87,3072
TR 66,227,53248
Mvaa 128,72,168
TR 68,227,57344
Mvaa 128,76,172
WL 7
TR 70,227,61440
Mvaa 136,80,168
s_add_u32 m0, s85, 32768
TR 72,240,14336
Mvaa 136,84,172
DM 235,56,88,0
TR 74,240,14848
Mvaa 140,80,184
TR 76,240,15360
Mvaa 140,84,188
WL 7
TR 78,240,15872
Mvaa 156,88,184
s_waitcnt vmcnt(30) lgkmcnt(0)
s_barrier
DM 236,56,88,1024
TR 80,241,0
Mvaa 156,92,188
TR 82,241,4096
Mvaa 152,88,168
TR 84,241,8192
Mvaa 152,92,172
DM 237,56,88,2048
TR 86,241,12288
Mvaa 160,64,168
TR 88,242,0
Mvaa 160,68,172
TR 90,242,4096
Mvaa 164,64,184
TR 92,242,8192
Mvaa 164,68,188
DM 238,56,88,3072
)"
R"(TR 94,242,12288
Mvaa 144,72,184
TR 64,243,0
Mvaa 144,76,188
TR 66,243,4096
Mvaa 148,72,168
s_add_u32 m0, s86, 8192
TR 68,243,8192
Mvaa 148,76,172
s_waitcnt vmcnt(25) lgkmcnt(7)
buffer_load_dwordx4 v234, s[72:75], s89 offen offset:0 sc1 lds
TR 70,243,12288
Mvaa 168,80,96
TR 72,244,0
v_lshrrev_b32 v79, 4, v255
v_and_b32 v78, 7, v255
Mvaa 168,84,100
TR 74,244,4096
v_lshl_add_u32 v78, v79, 3, v78
v_lshlrev_b32 v72, 3, v78
Mvaa 172,80,112
s_add_u32 s90, s34, 16846848
GL 224,234,72,90,0
TR 76,244,8192
v_and_b32 v78, 15, v255
v_mul_u32_u24 v77, 1152, v78
Mvaa 172,84,116
WL 7
TR 78,244,12288
v_lshl_add_u32 v75, v79, 3, v77
Mvaa 180,88,112
s_lshl_b32 s95, s80, 5
s_add_u32 s95, s95, 1024
TR 80,245,0
v_add_u32 v76, s95, v75
Mvaa 180,92,116
GL 228,234,72,90,1024
TR 82,245,4096
v_cmp_gt_u32 vcc, 8, v78
v_mov_b32 v78, 0x7ffffff0
s_nop 0
v_cndmask_b32 v73, v72, v78, vcc
v_cndmask_b32 v72, v78, v72, vcc
v_cndmask_b32 v76, v78, v76, vcc
Mvaa 176,88,96
TR 84,245,8192
v_mov_b32 v74, v72
v_and_b32 v78, 1, v79
Mvaa 176,92,100
WL 7
TR 86,245,12288
v_lshl_add_u32 v77, v78, 5, v77
Mvaa 184,64,96
TR 88,246,0
v_lshrrev_b32 v78, 1, v79
v_lshl_add_u32 v77, v78, 4, v77
Mvaa 184,68,100
s_lshl_b32 s95, s79, 2
s_add_u32 s95, s95, s19
s_mul_i32 s95, s95, 36864
GL 232,234,72,90,2048
TR 90,246,4096
Mvaa 188,64,112
s_add_u32 s31, s95, 18432
s_lshl_b32 s96, s80, 8
s_add_u32 s95, s95, s96
TR 92,246,8192
Mvaa 188,68,116
WL 7
s_add_u32 s25, s95, 128
s_add_u32 s32, s95, 18560
TR 94,246,12288
Mvaa 212,72,112
s_lshl_b32 s95, s35, 13
s_add_u32 s30, s95, 40189952
s_add_u32 s20, s95, 40181760
GL 236,234,72,90,3072
TR 64,247,0
Mvaa 212,76,116
s_add_u32 s21, s95, 40175872
s_add_u32 s22, s95, 40202752
s_lshl_b32 s91, s35, 7
TR 66,247,4096
Mvaa 208,72,96
s_add_u32 s92, s91, 256
TR 68,247,8192
Mvaa 208,76,100
WL 7
TR 70,247,12288
Mvaa 128,80,96
s_add_u32 s90, s34, 16977920
GL 240,234,72,90,0
TR 72,248,0
Mvaa 128,84,100
TR 74,248,4096
Mvaa 132,80,112
TR 76,248,8192
Mvaa 132,84,116
WL 7
GL 244,234,72,90,1024
TR 78,248,12288
Mvaa 140,88,112
TR 80,240,0
Mvaa 140,92,116
TR 82,240,512
Mvaa 136,88,96
TR 84,240,1024
Mvaa 136,92,100
WL 7
GL 248,234,72,90,2048
TR 86,240,1536
Mvaa 152,64,96
TR 88,220,0
Mvaa 152,68,100
TR 90,220,4096
Mvaa 156,64,112
GL 252,234,72,90,3072
TR 92,220,8192
Mvaa 156,68,116
WL 7
TR 94,220,12288
Mvaa 164,72,112
TR 64,221,0
Mvaa 164,76,116
TR 66,221,4096
Mvaa 160,72,96
s_add_u32 s87, s28, 2654208
s_add_u32 s88, s28, 2727936
s_add_u32 s89, s29, 221184
TR 68,221,8192
Mvaa 160,76,100
WL 7
s_add_u32 m0, s84, 49152
TR 70,221,12288
Mvaa 148,80,96
DM 235,56,87,0
TR 72,222,0
Mvaa 148,84,100
TR 74,222,4096
Mvaa 144,80,112
DM 236,56,87,1024
TR 76,222,8192
Mvaa 144,84,116
WL 7
TR 78,222,12288
Mvaa 172,88,120
TR 80,223,0
Mvaa 172,92,124
DM 237,56,87,2048
TR 82,223,4096
Mvaa 168,88,104
TR 84,223,8192
Mvaa 168,92,108
WL 7
TR 86,223,12288
Mvaa 176,64,104
DM 238,56,87,3072
TR 88,224,0
Mvaa 176,68,108
TR 90,224,4096
Mvaa 180,64,120
TR 92,224,8192
Mvaa 180,68,124
WL 7
TR 94,224,12288
Mvaa 188,72,120
s_add_u32 m0, s85, 49152
TR 64,225,0
Mvaa 188,76,124
DM 235,56,88,0
TR 66,225,4096
Mvaa 184,72,104
TR 68,225,8192
Mvaa 184,76,108
WL 7
DM 236,56,88,1024
TR 70,225,12288
Mvaa 208,80,104
TR 72,226,0
Mvaa 208,84,108
TR 74,226,4096
Mvaa 212,80,120
TR 76,226,8192
Mvaa 212,84,124
WL 7
DM 237,56,88,2048
TR 78,226,12288
Mvaa 132,88,120
TR 80,227,0
Mvaa 132,92,124
TR 82,227,4096
Mvaa 128,88,104
DM 238,56,88,3072
TR 84,227,8192
Mvaa 128,92,108
WL 7
TR 86,227,12288
Mvaa 136,64,104
TR 88,240,2048
Mvaa 136,68,108
TR 90,240,2560
Mvaa 140,64,120
s_add_u32 m0, s86, 12288
TR 92,240,3072
Mvaa 140,68,124
WL 7
buffer_load_dwordx4 v234, s[72:75], s89 offen offset:0 sc1 lds
TR 94,240,3584
Mvaa 156,72,120
s_waitcnt vmcnt(34) lgkmcnt(0)
s_barrier
TR 64,241,16384
Mvaa 156,76,124
s_add_u32 s90, s34, 16850944
GL 128,234,72,90,0
TR 66,241,20480
Mvaa 152,72,104
TR 68,241,24576
Mvaa 152,76,108
TR 70,241,28672
Mvaa 160,80,104
TR 72,242,16384
Mvaa 160,84,108
GL 132,234,72,90,1024
TR 74,242,20480
Mvaa 164,80,120
TR 76,242,24576
Mvaa 164,84,124
TR 78,242,28672
Mvaa 144,88,120
GL 136,234,72,90,2048
TR 80,243,16384
Mvaa 144,92,124
TR 82,243,20480
Mvaa 148,88,104
TR 84,243,24576
Mvaa 148,92,108
s_waitcnt vmcnt(29) lgkmcnt(7)
TR 86,243,28672
v_mfma_f32_16x16x32_bf16 v[40:43], a[64:67], a[192:195], 0
GL 140,234,72,90,3072
TR 88,244,16384
Mvaa 40,68,196
TR 90,244,20480
v_mfma_f32_16x16x32_bf16 v[44:47], a[64:67], a[208:211], 0
TR 92,244,24576
Mvaa 44,68,212
WL 7
s_add_u32 s90, s34, 16982016
GL 144,234,72,90,0
TR 94,244,28672
v_mfma_f32_16x16x32_bf16 v[52:55], a[72:75], a[208:211], 0
TR 64,245,16384
CV 148,148,149
Mvaa 52,76,212
TR 66,245,20480
CV 149,150,151
v_mfma_f32_16x16x32_bf16 v[48:51], a[72:75], a[192:195], 0
TR 68,245,24576
CV 150,144,145
Mvaa 48,76,196
WL 7
GL 148,234,72,90,1024
TR 70,245,28672
v_mfma_f32_16x16x32_bf16 v[56:59], a[80:83], a[192:195], 0
TR 72,246,16384
CV 151,146,147
Mvaa 56,84,196
TR 74,246,20480
CV 152,152,153
v_mfma_f32_16x16x32_bf16 v[60:63], a[80:83], a[208:211], 0
GL 152,234,72,90,2048
TR 76,246,24576
CV 153,154,155
Mvaa 60,84,212
WL 7
TR 78,246,28672
v_mfma_f32_16x16x32_bf16 v[68:71], a[88:91], a[208:211], 0
TR 80,247,16384
CV 154,156,157
Mvaa 68,92,212
TR 82,247,20480
CV 155,158,159
v_mfma_f32_16x16x32_bf16 v[64:67], a[88:91], a[192:195], 0
GL 156,234,72,90,3072
TR 84,247,24576
CV 160,160,161
Mvaa 64,92,196
WL 7
TR 86,247,28672
v_mfma_f32_16x16x32_bf16 v[0:3], a[64:67], a[192:195], 0
TR 88,248,16384
CV 161,162,163
Mvaa 0,68,196
TR 90,248,20480
CV 162,164,165
v_mfma_f32_16x16x32_bf16 v[4:7], a[64:67], a[208:211], 0
TR 92,248,24576
CV 163,166,167
Mvaa 4,68,212
WL 7
TR 94,248,28672
v_mfma_f32_16x16x32_bf16 v[12:15], a[72:75], a[208:211], 0
TR 64,240,4096
CV 168,168,169
Mvaa 12,76,212
TR 66,240,4608
CV 169,170,171
v_mfma_f32_16x16x32_bf16 v[8:11], a[72:75], a[192:195], 0
TR 68,240,5120
CV 170,172,173
Mvaa 8,76,196
WL 7
TR 70,240,5632
v_mfma_f32_16x16x32_bf16 v[24:27], a[80:83], a[192:195], 0
TR 72,220,16384
CV 171,174,175
Mvaa 24,84,196
TR 74,220,20480
CV 176,176,177
v_mfma_f32_16x16x32_bf16 v[28:31], a[80:83], a[208:211], 0
TR 76,220,24576
CV 177,178,179
Mvaa 28,84,212
WL 7
TR 78,220,28672
v_mfma_f32_16x16x32_bf16 v[36:39], a[88:91], a[208:211], 0
TR 80,221,16384
CV 178,180,181
Mvaa 36,92,212
TR 82,221,20480
CV 179,182,183
v_mfma_f32_16x16x32_bf16 v[32:35], a[88:91], a[192:195], 0
TR 84,221,24576
CV 184,184,185
Mvaa 32,92,196
WL 7
TR 86,221,28672
v_mfma_f32_16x16x32_bf16 v[20:23], a[64:67], a[192:195], 0
TR 88,222,16384
CV 185,186,187
Mvaa 20,68,196
TR 90,222,20480
CV 186,188,189
v_mfma_f32_16x16x32_bf16 v[16:19], a[64:67], a[208:211], 0
TR 92,222,24576
CV 187,190,191
Mvaa 16,68,212
WL 7
TR 94,222,28672
Mvaa 44,72,216
TR 64,223,16384
CV 208,208,209
Mvaa 44,76,220
TR 66,223,20480
CV 209,210,211
Mvaa 40,72,200
TR 68,223,24576
CV 210,212,213
Mvaa 40,76,204
WL 7
TR 70,223,28672
Mvaa 48,80,200
TR 72,224,16384
CV 211,214,215
Mvaa 48,84,204
s_add_u32 s16, s30, 2048
buffer_store_dwordx2 v[150:151], v73, s[72:75], s16 offen sc1
TR 74,224,20480
Mvaa 52,80,216
s_add_u32 s16, s30, 4352
buffer_store_dwordx2 v[148:149], v72, s[72:75], s16 offen sc1
TR 76,224,24576
Mvaa 52,84,220
WL 7
s_add_u32 s16, s30, 6656
buffer_store_dwordx2 v[148:149], v73, s[72:75], s16 offen sc1
TR 78,224,28672
Mvaa 60,88,216
s_add_u32 s16, s30, 0
buffer_store_dwordx4 v[152:155], v234, s[72:75], s16 offen sc1
TR 80,225,16384
Mvaa 60,92,220
s_add_u32 s16, s30, 1024
buffer_store_dwordx4 v[160:163], v234, s[72:75], s16 offen sc1
TR 82,225,20480
Mvaa 56,88,200
TR 84,225,24576
Mvaa 56,92,204
WL 7
TR 86,225,28672
Mvaa 64,64,200
TR 88,226,16384
Mvaa 64,68,204
s_add_u32 s98, s36, 264704
TR 90,226,20480
v_and_b32 v249, 3, v255
Mvaa 68,64,216
TR 92,226,24576
v_lshlrev_b32 v249, 7, v249
)"
R"(Mvaa 68,68,220
WL 7
buffer_load_dwordx2 v[250:251], v249, s[72:75], s98 offen sc1
TR 94,226,28672
Mvaa 4,72,216
s_add_u32 s87, s28, 2801664
s_add_u32 s88, s28, 2875392
s_add_u32 s89, s29, 225280
TR 64,227,16384
Mvaa 4,76,220
s_add_u32 m0, s84, 0
TR 66,227,20480
Mvaa 0,72,200
DM 235,56,87,0
TR 68,227,24576
Mvaa 0,76,204
WL 7
TR 70,227,28672
Mvaa 8,80,200
DM 236,56,87,1024
TR 72,240,6144
Mvaa 8,84,204
TR 74,240,6656
Mvaa 12,80,216
DM 237,56,87,2048
TR 76,240,7168
Mvaa 12,84,220
WL 7
TR 78,240,7680
Mvaa 28,88,216
s_waitcnt vmcnt(34) lgkmcnt(0)
s_barrier
TR 80,241,32768
Mvaa 28,92,220
DM 238,56,87,3072
TR 82,241,36864
Mvaa 24,88,200
TR 84,241,40960
Mvaa 24,92,204
TR 86,241,45056
Mvaa 32,64,200
s_add_u32 m0, s85, 0
TR 88,242,32768
Mvaa 32,68,204
DM 235,56,88,0
TR 90,242,36864
Mvaa 36,64,216
TR 92,242,40960
Mvaa 36,68,220
TR 94,242,45056
Mvaa 16,72,216
DM 236,56,88,1024
TR 64,243,32768
Mvaa 16,76,220
TR 66,243,36864
Mvaa 20,72,200
TR 68,243,40960
Mvaa 20,76,204
s_waitcnt vmcnt(29) lgkmcnt(7)
DM 237,56,88,2048
TR 70,243,45056
Mvaa 40,80,224
TR 72,244,32768
Mvaa 40,84,228
TR 74,244,36864
Mvaa 44,80,240
TR 76,244,40960
Mvaa 44,84,244
WL 7
DM 238,56,88,3072
TR 78,244,45056
Mvaa 52,88,240
TR 80,245,32768
Mvaa 52,92,244
TR 82,245,36864
Mvaa 48,88,224
s_add_u32 m0, s86, 0
TR 84,245,40960
Mvaa 48,92,228
WL 7
buffer_load_dwordx4 v234, s[72:75], s89 offen offset:0 sc1 lds
TR 86,245,45056
Mvaa 56,64,224
TR 88,246,32768
Mvaa 56,68,228
TR 90,246,36864
Mvaa 60,64,240
s_add_u32 s90, s34, 16855040
GL 160,234,72,90,0
TR 92,246,40960
Mvaa 60,68,244
WL 7
TR 94,246,45056
Mvaa 68,72,240
TR 64,247,32768
Mvaa 68,76,244
GL 164,234,72,90,1024
TR 66,247,36864
Mvaa 64,72,224
TR 68,247,40960
Mvaa 64,76,228
Lv2stub4_fused_block:
s_cbranch_execz Lt2x_fused_block
WL 7
TR 70,247,45056
Mvaa 0,80,224
TR 72,248,32768
Mvaa 0,84,228
GL 168,234,72,90,2048
TR 74,248,36864
Mvaa 4,80,240
TR 76,248,40960
Mvaa 4,84,244
WL 7
TR 78,248,45056
Mvaa 12,88,240
GL 172,234,72,90,3072
TR 80,240,8192
Mvaa 12,92,244
TR 82,240,8704
Mvaa 8,88,224
TR 84,240,9216
Mvaa 8,92,228
WL 7
TR 86,240,9728
Mvaa 24,64,224
s_add_u32 s90, s34, 16986112
GL 176,234,72,90,0
TR 88,220,32768
Mvaa 24,68,228
TR 90,220,36864
Mvaa 28,64,240
TR 92,220,40960
Mvaa 28,68,244
WL 7
GL 180,234,72,90,1024
TR 94,220,45056
Mvaa 36,72,240
TR 64,221,32768
Mvaa 36,76,244
TR 66,221,36864
Mvaa 32,72,224
TR 68,221,40960
Mvaa 32,76,228
WL 7
GL 184,234,72,90,2048
TR 70,221,45056
Mvaa 20,80,224
TR 72,222,32768
Mvaa 20,84,228
TR 74,222,36864
Mvaa 16,80,240
GL 188,234,72,90,3072
TR 76,222,40960
Mvaa 16,84,244
WL 7
TR 78,222,45056
Mvaa 44,88,248
TR 80,223,32768
Mvaa 44,92,252
TR 82,223,36864
Mvaa 40,88,232
s_add_u32 s16, s30, 2304
buffer_store_dwordx4 v[168:171], v234, s[72:75], s16 offen sc1
TR 84,223,40960
Mvaa 40,92,236
WL 7
TR 86,223,45056
Mvaa 48,64,232
TR 88,224,32768
Mvaa 48,68,236
TR 90,224,36864
Mvaa 52,64,248
TR 92,224,40960
Mvaa 52,68,252
WL 7
TR 94,224,45056
Mvaa 60,72,248
TR 64,225,32768
Mvaa 60,76,252
TR 66,225,36864
Mvaa 56,72,232
TR 68,225,40960
Mvaa 56,76,236
WL 7
TR 70,225,45056
Mvaa 64,80,232
TR 72,226,32768
Mvaa 64,84,236
s_add_u32 s16, s30, 3328
buffer_store_dwordx4 v[176:179], v234, s[72:75], s16 offen sc1
TR 74,226,36864
Mvaa 68,80,248
TR 76,226,40960
Mvaa 68,84,252
WL 7
TR 78,226,45056
Mvaa 4,88,248
TR 80,227,32768
Mvaa 4,92,252
TR 82,227,36864
Mvaa 0,88,232
TR 84,227,40960
Mvaa 0,92,236
WL 7
TR 86,227,45056
Mvaa 8,64,232
TR 88,240,10240
Mvaa 8,68,236
TR 90,240,10752
Mvaa 12,64,248
TR 92,240,11264
Mvaa 12,68,252
WL 7
TR 94,240,11776
Mvaa 28,72,248
s_waitcnt vmcnt(33) lgkmcnt(0)
s_barrier
s_add_u32 s16, s30, 4608
buffer_store_dwordx4 v[184:187], v234, s[72:75], s16 offen sc1
TR 64,241,49152
Mvaa 28,76,252
TR 66,241,53248
Mvaa 24,72,232
TR 68,241,57344
Mvaa 24,76,236
TR 70,241,61440
Mvaa 32,80,232
TR 72,242,49152
Mvaa 32,84,236
TR 74,242,53248
Mvaa 36,80,248
TR 76,242,57344
Mvaa 36,84,252
TR 78,242,61440
Mvaa 16,88,248
TR 80,243,49152
Mvaa 16,92,252
TR 82,243,53248
Mvaa 20,88,232
TR 84,243,57344
Mvaa 20,92,236
s_waitcnt vmcnt(26) lgkmcnt(7)
s_add_u32 s16, s30, 5632
buffer_store_dwordx4 v[208:211], v234, s[72:75], s16 offen sc1
TR 86,243,61440
Mvaa 40,64,128
TR 88,244,49152
Mvaa 40,68,132
TR 90,244,53248
Mvaa 44,64,144
TR 92,244,57344
Mvaa 44,68,148
WL 7
TR 94,244,61440
Mvaa 52,72,144
TR 64,245,49152
Mvaa 52,76,148
TR 66,245,53248
Mvaa 48,72,128
TR 68,245,57344
Mvaa 48,76,132
WL 7
TR 70,245,61440
Mvaa 56,80,128
TR 72,246,49152
Mvaa 56,84,132
TR 74,246,53248
Mvaa 60,80,144
s_add_u32 s87, s28, 2949120
s_add_u32 s88, s28, 3022848
s_add_u32 s89, s29, 229376
TR 76,246,57344
Mvaa 60,84,148
WL 7
s_add_u32 m0, s84, 16384
TR 78,246,61440
Mvaa 68,88,144
DM 235,56,87,0
TR 80,247,49152
Mvaa 68,92,148
TR 82,247,53248
Mvaa 64,88,128
DM 236,56,87,1024
TR 84,247,57344
Mvaa 64,92,132
WL 7
TR 86,247,61440
Mvaa 0,64,128
TR 88,248,49152
Mvaa 0,68,132
DM 237,56,87,2048
TR 90,248,53248
Mvaa 4,64,144
TR 92,248,57344
Mvaa 4,68,148
WL 7
TR 94,248,61440
Mvaa 12,72,144
DM 238,56,87,3072
TR 64,240,12288
Mvaa 12,76,148
TR 66,240,12800
Mvaa 8,72,128
TR 68,240,13312
Mvaa 8,76,132
WL 7
TR 70,240,13824
Mvaa 24,80,128
s_add_u32 m0, s85, 16384
TR 72,220,49152
Mvaa 24,84,132
DM 235,56,88,0
TR 74,220,53248
Mvaa 28,80,144
TR 76,220,57344
Mvaa 28,84,148
WL 7
DM 236,56,88,1024
TR 78,220,61440
Mvaa 36,88,144
TR 80,221,49152
Mvaa 36,92,148
TR 82,221,53248
Mvaa 32,88,128
TR 84,221,57344
Mvaa 32,92,132
WL 7
DM 237,56,88,2048
TR 86,221,61440
Mvaa 20,64,128
TR 88,222,49152
Mvaa 20,68,132
TR 90,222,53248
Mvaa 16,64,144
DM 238,56,88,3072
TR 92,222,57344
Mvaa 16,68,148
WL 7
TR 94,222,61440
Mvaa 44,72,152
TR 64,223,49152
Mvaa 44,76,156
TR 66,223,53248
Mvaa 40,72,136
s_add_u32 m0, s86, 4096
TR 68,223,57344
Mvaa 40,76,140
WL 7
buffer_load_dwordx4 v234, s[72:75], s89 offen offset:0 sc1 lds
TR 70,223,61440
Mvaa 48,80,136
TR 72,224,49152
Mvaa 48,84,140
s_waitcnt vmcnt(30)
TR 74,224,53248
v_cmp_ne_u64 vcc, s[82:83], v[250:251]
s_nop 3
s_cbranch_vccnz Lslow7_fused_block
Lslowb7_fused_block:
Mvaa 52,80,152
s_add_u32 s90, s34, 16859136
GL 96,234,72,90,0
TR 76,224,57344
Mvaa 52,84,156
WL 7
TR 78,224,61440
Mvaa 60,88,152
TR 80,225,49152
Mvaa 60,92,156
GL 100,234,72,90,1024
TR 82,225,53248
Mvaa 56,88,136
TR 84,225,57344
Mvaa 56,92,140
WL 7
TR 86,225,61440
Mvaa 64,64,136
GL 104,234,72,90,2048
TR 88,226,49152
Mvaa 64,68,140
TR 90,226,53248
Mvaa 68,64,152
TR 92,226,57344
Mvaa 68,68,156
WL 7
TR 94,226,61440
Mvaa 4,72,152
GL 108,234,72,90,3072
TR 64,227,49152
Mvaa 4,76,156
TR 66,227,53248
Mvaa 0,72,136
TR 68,227,57344
Mvaa 0,76,140
WL 7
s_add_u32 s90, s34, 16990208
GL 112,234,72,90,0
TR 70,227,61440
Mvaa 8,80,136
)"
R"(TR 72,240,14336
Mvaa 8,84,140
TR 74,240,14848
Mvaa 12,80,152
TR 76,240,15360
Mvaa 12,84,156
WL 7
GL 116,234,72,90,1024
TR 78,240,15872
Mvaa 28,88,152
s_waitcnt vmcnt(27) lgkmcnt(0)
s_barrier
TR 80,241,0
Mvaa 28,92,156
TR 82,241,4096
Mvaa 24,88,136
GL 120,234,72,90,2048
TR 84,241,8192
Mvaa 24,92,140
TR 86,241,12288
Mvaa 32,64,136
TR 88,242,0
Mvaa 32,68,140
TR 90,242,4096
Mvaa 36,64,152
GL 124,234,72,90,3072
TR 92,242,8192
Mvaa 36,68,156
TR 94,242,12288
Mvaa 16,72,152
TR 64,243,0
Mvaa 16,76,156
s_waitcnt vmcnt(17)
s_add_u32 s98, s92, 393216
TR 66,243,4096
v_mov_b32 v250, s82
Mvaa 20,72,136
TR 68,243,8192
v_mov_b32 v251, s83
v_lshlrev_b32 v249, 3, v255
Mvaa 20,76,140
WL 7
TR 70,243,12288
s_mov_b64 exec, 0xffff
buffer_store_dwordx2 v[250:251], v249, s[72:75], s98 offen sc1
s_mov_b64 exec, -1
Mvaa 40,80,160
s_add_u32 s87, s28, 3096576
s_add_u32 s88, s28, 3170304
s_add_u32 s89, s29, 233472
TR 72,244,0
Mvaa 40,84,164
s_add_u32 m0, s84, 32768
TR 74,244,4096
Mvaa 44,80,176
DM 235,56,87,0
TR 76,244,8192
Mvaa 44,84,180
WL 7
TR 78,244,12288
Mvaa 52,88,176
DM 236,56,87,1024
TR 80,245,0
Mvaa 52,92,180
TR 82,245,4096
Mvaa 48,88,160
DM 237,56,87,2048
TR 84,245,8192
Mvaa 48,92,164
WL 7
TR 86,245,12288
Mvaa 56,64,160
DM 238,56,87,3072
TR 88,246,0
Mvaa 56,68,164
TR 90,246,4096
Mvaa 60,64,176
TR 92,246,8192
Mvaa 60,68,180
WL 7
s_add_u32 m0, s85, 32768
TR 94,246,12288
Mvaa 68,72,176
DM 235,56,88,0
TR 64,247,0
Mvaa 68,76,180
TR 66,247,4096
Mvaa 64,72,160
TR 68,247,8192
Mvaa 64,76,164
WL 7
DM 236,56,88,1024
TR 70,247,12288
Mvaa 0,80,160
TR 72,248,0
Mvaa 0,84,164
TR 74,248,4096
Mvaa 4,80,176
DM 237,56,88,2048
TR 76,248,8192
Mvaa 4,84,180
WL 7
TR 78,248,12288
Mvaa 12,88,176
TR 80,240,0
Mvaa 12,92,180
TR 82,240,512
Mvaa 8,88,160
DM 238,56,88,3072
TR 84,240,1024
Mvaa 8,92,164
WL 7
TR 86,240,1536
Mvaa 24,64,160
TR 88,220,0
Mvaa 24,68,164
s_add_u32 m0, s86, 8192
TR 90,220,4096
Mvaa 28,64,176
buffer_load_dwordx4 v234, s[72:75], s89 offen offset:0 sc1 lds
TR 92,220,8192
Mvaa 28,68,180
WL 7
TR 94,220,12288
Mvaa 36,72,176
TR 64,221,0
Mvaa 36,76,180
s_add_u32 s90, s34, 16863232
GL 192,234,72,90,0
TR 66,221,4096
Mvaa 32,72,160
TR 68,221,8192
Mvaa 32,76,164
WL 7
TR 70,221,12288
Mvaa 20,80,160
GL 196,234,72,90,1024
TR 72,222,0
Mvaa 20,84,164
TR 74,222,4096
Mvaa 16,80,176
TR 76,222,8192
Mvaa 16,84,180
WL 7
TR 78,222,12288
Mvaa 44,88,184
GL 200,234,72,90,2048
TR 80,223,0
Mvaa 44,92,188
TR 82,223,4096
Mvaa 40,88,168
TR 84,223,8192
Mvaa 40,92,172
WL 7
GL 204,234,72,90,3072
TR 86,223,12288
Mvaa 48,64,168
TR 88,224,0
Mvaa 48,68,172
TR 90,224,4096
Mvaa 52,64,184
TR 92,224,8192
Mvaa 52,68,188
WL 7
s_add_u32 s90, s34, 16994304
GL 208,234,72,90,0
TR 94,224,12288
Mvaa 60,72,184
TR 64,225,0
Mvaa 60,76,188
TR 66,225,4096
Mvaa 56,72,168
GL 212,234,72,90,1024
TR 68,225,8192
Mvaa 56,76,172
WL 7
TR 70,225,12288
Mvaa 64,80,168
TR 72,226,0
Mvaa 64,84,172
TR 74,226,4096
Mvaa 68,80,184
GL 216,234,72,90,2048
TR 76,226,8192
Mvaa 68,84,188
WL 7
TR 78,226,12288
Mvaa 4,88,184
TR 80,227,0
Mvaa 4,92,188
GL 220,234,72,90,3072
TR 82,227,4096
Mvaa 0,88,168
TR 84,227,8192
Mvaa 0,92,172
WL 7
TR 86,227,12288
Mvaa 8,64,168
TR 88,240,2048
Mvaa 8,68,172
s_add_u32 s87, s28, 3244032
s_add_u32 s88, s28, 3317760
s_add_u32 s89, s29, 237568
TR 90,240,2560
Mvaa 12,64,184
s_add_u32 m0, s84, 49152
TR 92,240,3072
Mvaa 12,68,188
WL 7
DM 235,56,87,0
TR 94,240,3584
Mvaa 28,72,184
s_waitcnt vmcnt(27) lgkmcnt(0)
s_barrier
TR 64,241,16384
Mvaa 28,76,188
DM 236,56,87,1024
TR 66,241,20480
Mvaa 24,72,168
TR 68,241,24576
Mvaa 24,76,172
TR 70,241,28672
Mvaa 32,80,168
DM 237,56,87,2048
TR 72,242,16384
Mvaa 32,84,172
TR 74,242,20480
Mvaa 36,80,184
TR 76,242,24576
Mvaa 36,84,188
DM 238,56,87,3072
TR 78,242,28672
Mvaa 16,88,184
TR 80,243,16384
Mvaa 16,92,188
TR 82,243,20480
Mvaa 20,88,168
TR 84,243,24576
Mvaa 20,92,172
s_waitcnt vmcnt(22) lgkmcnt(7)
s_add_u32 m0, s85, 49152
TR 86,243,28672
Mvaa 40,64,96
DM 235,56,88,0
TR 88,244,16384
Mvaa 40,68,100
Lv2stub5_fused_block:
s_cbranch_execz Lt2x_fused_block
TR 90,244,20480
Mvaa 44,64,112
DM 236,56,88,1024
TR 92,244,24576
Mvaa 44,68,116
WL 7
TR 94,244,28672
Mvaa 52,72,112
TR 64,245,16384
Mvaa 52,76,116
TR 66,245,20480
Mvaa 48,72,96
DM 237,56,88,2048
TR 68,245,24576
Mvaa 48,76,100
WL 7
TR 70,245,28672
Mvaa 56,80,96
TR 72,246,16384
Mvaa 56,84,100
DM 238,56,88,3072
TR 74,246,20480
Mvaa 60,80,112
TR 76,246,24576
Mvaa 60,84,116
WL 7
TR 78,246,28672
Mvaa 68,88,112
TR 80,247,16384
Mvaa 68,92,116
s_add_u32 m0, s86, 12288
TR 82,247,20480
Mvaa 64,88,96
buffer_load_dwordx4 v234, s[72:75], s89 offen offset:0 sc1 lds
TR 84,247,24576
Mvaa 64,92,100
WL 7
TR 86,247,28672
Mvaa 0,64,96
s_add_u32 s90, s34, 16867328
GL 224,234,72,90,0
TR 88,248,16384
Mvaa 0,68,100
TR 90,248,20480
Mvaa 4,64,112
TR 92,248,24576
Mvaa 4,68,116
WL 7
TR 94,248,28672
Mvaa 12,72,112
GL 228,234,72,90,1024
TR 64,240,4096
Mvaa 12,76,116
TR 66,240,4608
Mvaa 8,72,96
TR 68,240,5120
Mvaa 8,76,100
WL 7
GL 232,234,72,90,2048
TR 70,240,5632
Mvaa 24,80,96
TR 72,220,16384
Mvaa 24,84,100
TR 74,220,20480
Mvaa 28,80,112
TR 76,220,24576
Mvaa 28,84,116
WL 7
GL 236,234,72,90,3072
TR 78,220,28672
Mvaa 36,88,112
TR 80,221,16384
Mvaa 36,92,116
TR 82,221,20480
Mvaa 32,88,96
s_add_u32 s90, s34, 16998400
GL 240,234,72,90,0
TR 84,221,24576
Mvaa 32,92,100
WL 7
TR 86,221,28672
Mvaa 20,64,96
TR 88,222,16384
Mvaa 20,68,100
TR 90,222,20480
Mvaa 16,64,112
GL 244,234,72,90,1024
TR 92,222,24576
Mvaa 16,68,116
WL 7
TR 94,222,28672
Mvaa 44,72,120
TR 64,223,16384
Mvaa 44,76,124
GL 248,234,72,90,2048
TR 66,223,20480
Mvaa 40,72,104
TR 68,223,24576
Mvaa 40,76,108
WL 7
TR 70,223,28672
Mvaa 48,80,104
TR 72,224,16384
Mvaa 48,84,108
GL 252,234,72,90,3072
TR 74,224,20480
Mvaa 52,80,120
TR 76,224,24576
Mvaa 52,84,124
WL 7
TR 78,224,28672
Mvaa 60,88,120
s_add_u32 s87, s28, 3391488
s_add_u32 s88, s28, 3465216
s_add_u32 s89, s29, 241664
TR 80,225,16384
Mvaa 60,92,124
s_add_u32 m0, s84, 0
TR 82,225,20480
Mvaa 56,88,104
DM 235,56,87,0
TR 84,225,24576
Mvaa 56,92,108
WL 7
TR 86,225,28672
Mvaa 64,64,104
DM 236,56,87,1024
TR 88,226,16384
Mvaa 64,68,108
TR 90,226,20480
Mvaa 68,64,120
TR 92,226,24576
Mvaa 68,68,124
WL 7
DM 237,56,87,2048
TR 94,226,28672
Mvaa 4,72,120
TR 64,227,16384
Mvaa 4,76,124
TR 66,227,20480
Mvaa 0,72,104
TR 68,227,24576
Mvaa 0,76,108
WL 7
DM 238,56,87,3072
TR 70,227,28672
Mvaa 8,80,104
TR 72,240,6144
Mvaa 8,84,108
TR 74,240,6656
Mvaa 12,80,120
s_add_u32 m0, s85, 0
)"
R"(TR 76,240,7168
Mvaa 12,84,124
WL 7
DM 235,56,88,0
TR 78,240,7680
Mvaa 28,88,120
s_waitcnt vmcnt(30) lgkmcnt(0)
s_barrier
TR 80,241,32768
Mvaa 28,92,124
TR 82,241,36864
Mvaa 24,88,104
DM 236,56,88,1024
TR 84,241,40960
Mvaa 24,92,108
TR 86,241,45056
Mvaa 32,64,104
TR 88,242,32768
Mvaa 32,68,108
DM 237,56,88,2048
TR 90,242,36864
Mvaa 36,64,120
TR 92,242,40960
Mvaa 36,68,124
TR 94,242,45056
Mvaa 16,72,120
TR 64,243,32768
Mvaa 16,76,124
DM 238,56,88,3072
TR 66,243,36864
Mvaa 20,72,104
TR 68,243,40960
Mvaa 20,76,108
s_waitcnt vmcnt(25) lgkmcnt(7)
TR 70,243,45056
Mvaa 40,80,192
s_add_u32 m0, s86, 0
TR 72,244,32768
Mvaa 40,84,196
buffer_load_dwordx4 v234, s[72:75], s89 offen offset:0 sc1 lds
TR 74,244,36864
Mvaa 44,80,208
TR 76,244,40960
Mvaa 44,84,212
WL 7
TR 78,244,45056
Mvaa 52,88,208
s_add_u32 s90, s34, 16871424
GL 128,234,72,90,0
TR 80,245,32768
Mvaa 52,92,212
TR 82,245,36864
Mvaa 48,88,192
TR 84,245,40960
Mvaa 48,92,196
WL 7
GL 132,234,72,90,1024
TR 86,245,45056
Mvaa 56,64,192
TR 88,246,32768
Mvaa 56,68,196
TR 90,246,36864
Mvaa 60,64,208
TR 92,246,40960
Mvaa 60,68,212
WL 7
GL 136,234,72,90,2048
TR 94,246,45056
Mvaa 68,72,208
TR 64,247,32768
Mvaa 68,76,212
TR 66,247,36864
Mvaa 64,72,192
GL 140,234,72,90,3072
TR 68,247,40960
Mvaa 64,76,196
WL 7
TR 70,247,45056
Mvaa 0,80,192
TR 72,248,32768
Mvaa 0,84,196
TR 74,248,36864
Mvaa 4,80,208
s_add_u32 s90, s34, 17002496
GL 144,234,72,90,0
TR 76,248,40960
Mvaa 4,84,212
WL 7
TR 78,248,45056
Mvaa 12,88,208
TR 80,240,8192
Mvaa 12,92,212
GL 148,234,72,90,1024
TR 82,240,8704
Mvaa 8,88,192
TR 84,240,9216
Mvaa 8,92,196
WL 7
TR 86,240,9728
Mvaa 24,64,192
TR 88,220,32768
Mvaa 24,68,196
GL 152,234,72,90,2048
TR 90,220,36864
Mvaa 28,64,208
TR 92,220,40960
Mvaa 28,68,212
WL 7
TR 94,220,45056
Mvaa 36,72,208
GL 156,234,72,90,3072
TR 64,221,32768
Mvaa 36,76,212
TR 66,221,36864
Mvaa 32,72,192
TR 68,221,40960
Mvaa 32,76,196
WL 7
TR 70,221,45056
Mvaa 20,80,192
s_add_u32 s98, s91, 393216
TR 72,222,32768
v_and_b32 v249, 3, v255
Mvaa 20,84,196
TR 74,222,36864
v_lshlrev_b32 v249, 7, v249
Mvaa 16,80,208
buffer_load_dwordx2 v[250:251], v249, s[72:75], s98 offen sc1
TR 76,222,40960
Mvaa 16,84,212
WL 7
TR 78,222,45056
Mvaa 44,88,216
buffer_load_dwordx2 v[80:81], v75, s[40:43], s25 offen offset:0
TR 80,223,32768
Mvaa 44,92,220
TR 82,223,36864
Mvaa 40,88,200
buffer_load_dwordx2 v[82:83], v75, s[40:43], s32 offen offset:0
TR 84,223,40960
Mvaa 40,92,204
WL 7
TR 86,223,45056
Mvaa 48,64,200
buffer_load_dwordx2 v[84:85], v75, s[40:43], s25 offen offset:32
TR 88,224,32768
Mvaa 48,68,204
TR 90,224,36864
Mvaa 52,64,216
buffer_load_dwordx2 v[86:87], v75, s[40:43], s32 offen offset:32
TR 92,224,40960
Mvaa 52,68,220
WL 7
TR 94,224,45056
Mvaa 60,72,216
buffer_load_dwordx2 v[88:89], v76, s[40:43], s31 offen
TR 64,225,32768
Mvaa 60,76,220
TR 66,225,36864
Mvaa 56,72,200
TR 68,225,40960
Mvaa 56,76,204
WL 7
TR 70,225,45056
Mvaa 64,80,200
TR 72,226,32768
Mvaa 64,84,204
TR 74,226,36864
Mvaa 68,80,216
TR 76,226,40960
Mvaa 68,84,220
WL 7
TR 78,226,45056
Mvaa 4,88,216
TR 80,227,32768
Mvaa 4,92,220
TR 82,227,36864
Mvaa 0,88,200
TR 84,227,40960
Mvaa 0,92,204
WL 7
TR 86,227,45056
Mvaa 8,64,200
TR 88,240,10240
Mvaa 8,68,204
TR 90,240,10752
Mvaa 12,64,216
TR 92,240,11264
Mvaa 12,68,220
WL 7
TR 94,240,11776
Mvaa 28,72,216
s_waitcnt vmcnt(14) lgkmcnt(0)
s_barrier
TR 64,240,12288
Mvaa 28,76,220
TR 66,240,12800
Mvaa 24,72,200
TR 68,240,13312
Mvaa 24,76,204
TR 70,240,13824
Mvaa 32,80,200
TR 72,240,14336
Mvaa 32,84,204
TR 74,240,14848
Mvaa 36,80,216
TR 76,240,15360
Mvaa 36,84,220
TR 78,240,15872
Mvaa 16,88,216
TR 80,240,0
Mvaa 16,92,220
TR 82,240,512
Mvaa 20,88,200
TR 84,240,1024
Mvaa 20,92,204
WL 7
TR 86,240,1536
Mvaa 20,64,224
TR 88,240,2048
Mvaa 20,68,228
TR 90,240,2560
Mvaa 16,64,240
TR 92,240,3072
Mvaa 16,68,244
WL 7
TR 94,240,3584
Mvaa 16,72,248
TR 64,247,49152
Mvaa 16,76,252
TR 66,247,53248
Mvaa 20,72,232
TR 68,247,57344
Mvaa 20,76,236
s_waitcnt vmcnt(6) lgkmcnt(7)
TR 70,247,61440
Mvaa 20,80,128
TR 72,226,49152
Mvaa 20,84,132
TR 74,226,53248
Mvaa 16,80,144
s_waitcnt vmcnt(5)
TR 76,226,57344
v_cmp_ne_u64 vcc, s[82:83], v[250:251]
s_nop 3
s_cbranch_vccnz Lslow8_fused_block
Lslowb8_fused_block:
Mvaa 16,84,148
WL 7
buffer_load_dwordx4 v[152:155], v234, s[72:75], s20 offen offset:0 sc1
TR 78,226,61440
Mvaa 16,88,152
TR 80,247,0
Mvaa 16,92,156
buffer_load_dwordx4 v[156:159], v234, s[72:75], s20 offen offset:1024 sc1
TR 82,247,4096
Mvaa 20,88,136
TR 84,247,8192
Mvaa 20,92,140
WL 7
buffer_load_dwordx2 v[160:161], v74, s[72:75], s20 offen offset:2048 sc1
TR 86,247,12288
Mvaa 24,64,224
TR 88,226,0
Mvaa 24,68,228
buffer_load_dwordx4 v[162:165], v234, s[72:75], s21 offen offset:0 sc1
TR 90,226,4096
Mvaa 28,64,240
TR 92,226,8192
Mvaa 28,68,244
WL 7
buffer_load_dwordx4 v[166:169], v234, s[72:75], s21 offen offset:1024 sc1
TR 94,226,12288
CV 20,20,21
Mvaa 28,72,248
TR 64,248,49152
CV 21,22,23
CV 22,16,17
Mvaa 28,76,252
buffer_load_dwordx2 v[170:171], v74, s[72:75], s21 offen offset:2048 sc1
TR 66,248,53248
CV 23,18,19
Mvaa 24,72,232
TR 68,248,57344
Mvaa 24,76,236
WL 7
buffer_load_dwordx4 v[172:175], v234, s[72:75], s22 offen offset:0 sc1
TR 70,248,61440
Mvaa 24,80,128
TR 72,227,49152
Mvaa 24,84,132
buffer_load_dwordx4 v[176:179], v234, s[72:75], s22 offen offset:1024 sc1
TR 74,227,53248
Mvaa 28,80,144
TR 76,227,57344
Mvaa 28,84,148
WL 7
buffer_load_dwordx2 v[180:181], v74, s[72:75], s22 offen offset:2048 sc1
TR 78,227,61440
Mvaa 28,88,152
s_add_u32 s20, s20, 8388608
s_add_u32 s21, s21, 8388608
s_add_u32 s22, s22, 8388608
TR 80,248,0
Mvaa 28,92,156
s_add_u32 s16, s30, 8390656
buffer_store_dwordx2 v[22:23], v73, s[72:75], s16 offen sc1
TR 82,248,4096
Mvaa 24,88,136
s_add_u32 s16, s30, 8392960
buffer_store_dwordx2 v[20:21], v72, s[72:75], s16 offen sc1
TR 84,248,8192
Mvaa 24,92,140
WL 7
s_add_u32 s16, s30, 8395264
buffer_store_dwordx2 v[20:21], v73, s[72:75], s16 offen sc1
TR 86,248,12288
Mvaa 32,64,224
TR 88,227,0
Mvaa 32,68,228
TR 90,227,4096
Mvaa 36,64,240
TR 92,227,8192
Mvaa 36,68,244
WL 7
TR 94,227,12288
CV 24,24,25
Mvaa 36,72,248
TR 64,241,49152
CV 25,26,27
CV 26,28,29
Mvaa 36,76,252
TR 66,241,53248
CV 27,30,31
Mvaa 32,72,232
s_add_u32 s16, s30, 8388608
buffer_store_dwordx4 v[24:27], v234, s[72:75], s16 offen sc1
TR 68,241,57344
Mvaa 32,76,236
WL 7
TR 70,241,61440
Mvaa 32,80,128
TR 72,220,49152
Mvaa 32,84,132
TR 74,220,53248
Mvaa 36,80,144
TR 76,220,57344
Mvaa 36,84,148
WL 7
TR 78,220,61440
Mvaa 36,88,152
TR 80,241,0
Mvaa 36,92,156
TR 82,241,4096
Mvaa 32,88,136
TR 84,241,8192
Mvaa 32,92,140
WL 7
TR 86,241,12288
Mvaa 40,64,224
TR 88,220,0
Mvaa 40,68,228
TR 90,220,4096
Mvaa 44,64,240
TR 92,220,8192
Mvaa 44,68,244
WL 7
TR 94,220,12288
CV 32,32,33
Mvaa 44,72,248
TR 64,242,49152
CV 33,34,35
CV 34,36,37
Mvaa 44,76,252
TR 66,242,53248
CV 35,38,39
Mvaa 40,72,232
s_add_u32 s16, s30, 8389632
buffer_store_dwordx4 v[32:35], v234, s[72:75], s16 offen sc1
TR 68,242,57344
Mvaa 40,76,236
WL 7
TR 70,242,61440
Mvaa 40,80,128
TR 72,221,49152
Mvaa 40,84,132
TR 74,221,53248
Mvaa 44,80,144
Lv2stub6_fused_block:
s_cbranch_execz Lt2x_fused_block
TR 76,221,57344
Mvaa 44,84,148
WL 7
TR 78,221,61440
Mvaa 44,88,152
TR 80,242,0
Mvaa 44,92,156
TR 82,242,4096
Mvaa 40,88,136
TR 84,242,8192
)"
R"(Mvaa 40,92,140
WL 7
TR 86,242,12288
Mvaa 48,64,224
TR 88,221,0
Mvaa 48,68,228
TR 90,221,4096
Mvaa 52,64,240
TR 92,221,8192
Mvaa 52,68,244
WL 7
TR 94,221,12288
CV 40,40,41
Mvaa 52,72,248
TR 64,243,49152
CV 41,42,43
CV 42,44,45
Mvaa 52,76,252
TR 66,243,53248
CV 43,46,47
Mvaa 48,72,232
s_add_u32 s16, s30, 8390912
buffer_store_dwordx4 v[40:43], v234, s[72:75], s16 offen sc1
TR 68,243,57344
Mvaa 48,76,236
WL 7
TR 70,243,61440
Mvaa 48,80,128
TR 72,222,49152
Mvaa 48,84,132
TR 74,222,53248
Mvaa 52,80,144
TR 76,222,57344
Mvaa 52,84,148
WL 7
TR 78,222,61440
Mvaa 52,88,152
TR 80,243,0
Mvaa 52,92,156
TR 82,243,4096
Mvaa 48,88,136
TR 84,243,8192
Mvaa 48,92,140
WL 7
TR 86,243,12288
Mvaa 56,64,224
TR 88,222,0
Mvaa 56,68,228
TR 90,222,4096
Mvaa 60,64,240
TR 92,222,8192
Mvaa 60,68,244
WL 7
TR 94,222,12288
CV 48,48,49
Mvaa 60,72,248
TR 64,244,49152
CV 49,50,51
CV 50,52,53
Mvaa 60,76,252
TR 66,244,53248
CV 51,54,55
Mvaa 56,72,232
s_add_u32 s16, s30, 8391936
buffer_store_dwordx4 v[48:51], v234, s[72:75], s16 offen sc1
TR 68,244,57344
Mvaa 56,76,236
WL 7
TR 70,244,61440
Mvaa 56,80,128
TR 72,223,49152
Mvaa 56,84,132
TR 74,223,53248
Mvaa 60,80,144
TR 76,223,57344
Mvaa 60,84,148
WL 7
TR 78,223,61440
Mvaa 60,88,152
TR 80,244,0
Mvaa 60,92,156
TR 82,244,4096
Mvaa 56,88,136
TR 84,244,8192
Mvaa 56,92,140
WL 7
TR 86,244,12288
Mvaa 64,64,224
TR 88,223,0
Mvaa 64,68,228
TR 90,223,4096
Mvaa 68,64,240
TR 92,223,8192
Mvaa 68,68,244
WL 7
TR 94,223,12288
CV 56,56,57
Mvaa 68,72,248
TR 64,245,49152
CV 57,58,59
CV 58,60,61
Mvaa 68,76,252
TR 66,245,53248
CV 59,62,63
Mvaa 64,72,232
s_add_u32 s16, s30, 8393216
buffer_store_dwordx4 v[56:59], v234, s[72:75], s16 offen sc1
TR 68,245,57344
Mvaa 64,76,236
WL 7
TR 70,245,61440
Mvaa 64,80,128
TR 72,224,49152
Mvaa 64,84,132
TR 74,224,53248
Mvaa 68,80,144
TR 76,224,57344
Mvaa 68,84,148
WL 7
TR 78,224,61440
Mvaa 68,88,152
TR 80,245,0
Mvaa 68,92,156
TR 82,245,4096
Mvaa 64,88,136
TR 84,245,8192
Mvaa 64,92,140
WL 7
TR 86,245,12288
Mvaa 0,64,224
TR 88,224,0
Mvaa 0,68,228
TR 90,224,4096
Mvaa 4,64,240
TR 92,224,8192
Mvaa 4,68,244
WL 7
TR 94,224,12288
CV 64,64,65
Mvaa 4,72,248
TR 64,246,49152
CV 65,66,67
CV 66,68,69
Mvaa 4,76,252
TR 66,246,53248
CV 67,70,71
Mvaa 0,72,232
s_add_u32 s16, s30, 8394240
buffer_store_dwordx4 v[64:67], v234, s[72:75], s16 offen sc1
TR 68,246,57344
Mvaa 0,76,236
WL 7
TR 70,246,61440
Mvaa 0,80,128
TR 72,225,49152
Mvaa 0,84,132
TR 74,225,53248
Mvaa 4,80,144
TR 76,225,57344
Mvaa 4,84,148
WL 7
TR 78,225,61440
Mvaa 4,88,152
TR 80,246,0
Mvaa 4,92,156
TR 82,246,4096
Mvaa 0,88,136
TR 84,246,8192
Mvaa 0,92,140
WL 7
TR 86,246,12288
Mvaa 8,64,224
TR 88,225,0
Mvaa 8,68,228
TR 90,225,4096
Mvaa 12,64,240
TR 92,225,8192
Mvaa 12,68,244
WL 7
TR 94,225,12288
Mvaa 12,72,248
Mvaa 12,76,252
Mvaa 8,72,232
Mvaa 8,76,236
WL 4
Mvaa 8,80,128
Mvaa 8,84,132
Mvaa 12,80,144
Mvaa 12,84,148
WL 0
Mvaa 12,88,152
Mvaa 12,92,156
Mvaa 8,88,136
Mvaa 8,92,140
buffer_load_dwordx4 v[96:99], v234, s[72:75], s20 offen offset:0 sc1
buffer_load_dwordx4 v[100:103], v234, s[72:75], s20 offen offset:1024 sc1
buffer_load_dwordx2 v[104:105], v74, s[72:75], s20 offen offset:2048 sc1
buffer_load_dwordx4 v[106:109], v234, s[72:75], s21 offen offset:0 sc1
buffer_load_dwordx4 v[110:113], v234, s[72:75], s21 offen offset:1024 sc1
buffer_load_dwordx2 v[114:115], v74, s[72:75], s21 offen offset:2048 sc1
buffer_load_dwordx4 v[116:119], v234, s[72:75], s22 offen offset:0 sc1
buffer_load_dwordx4 v[120:123], v234, s[72:75], s22 offen offset:1024 sc1
buffer_load_dwordx2 v[124:125], v74, s[72:75], s22 offen offset:2048 sc1
s_waitcnt vmcnt(18)
s_branch Ltail_fused_block
Lslow6_fused_block:
s_add_u32 s98, s36, 264192
v_and_b32 v249, 3, v255
v_lshlrev_b32 v249, 7, v249
s_mov_b32 s99, 0x20000
Lpoll7_fused_block:
buffer_load_dwordx2 v[250:251], v249, s[72:75], s98 offen sc1
s_waitcnt vmcnt(0)
v_cmp_ne_u64 vcc, s[82:83], v[250:251]
s_sub_u32 s99, s99, 1
s_cbranch_scc1 Lpolld7_fused_block
s_nop 2
s_cbranch_vccnz Lpoll7_fused_block
Lpolld7_fused_block:
s_branch Lslowb6_fused_block
Lslow7_fused_block:
s_add_u32 s98, s36, 264704
v_and_b32 v249, 3, v255
v_lshlrev_b32 v249, 7, v249
s_mov_b32 s99, 0x20000
Lpoll8_fused_block:
buffer_load_dwordx2 v[250:251], v249, s[72:75], s98 offen sc1
s_waitcnt vmcnt(0)
v_cmp_ne_u64 vcc, s[82:83], v[250:251]
s_sub_u32 s99, s99, 1
s_cbranch_scc1 Lpolld8_fused_block
s_nop 2
s_cbranch_vccnz Lpoll8_fused_block
Lpolld8_fused_block:
s_branch Lslowb7_fused_block
Lslow8_fused_block:
s_add_u32 s98, s91, 393216
v_and_b32 v249, 3, v255
v_lshlrev_b32 v249, 7, v249
s_mov_b32 s99, 0x20000
Lpoll9_fused_block:
buffer_load_dwordx2 v[250:251], v249, s[72:75], s98 offen sc1
s_waitcnt vmcnt(0)
v_cmp_ne_u64 vcc, s[82:83], v[250:251]
s_sub_u32 s99, s99, 1
s_cbranch_scc1 Lpolld9_fused_block
s_nop 2
s_cbranch_vccnz Lpoll9_fused_block
Lpolld9_fused_block:
s_branch Lslowb8_fused_block
.p2align 6
s_branch Lv3stub0_fused_block
s_branch Lv3stub1_fused_block
s_branch Lv3stub2_fused_block
s_branch Lv3stub3_fused_block
Lvar3_fused_block:
v_mfma_f32_16x16x32_bf16 v[128:131], a[64:67], a[32:35], v[184:187]
v_exp_f32 v212, v144
Mvaa 128,68,36
s_add_u32 s90, s34, 106496
GL 128,234,72,90,0
Mvaa 128,72,40
s_add_u32 s20, s25, 16777216
v_exp_f32 v213, v145
Mvaa 128,76,44
GL 132,234,72,90,1024
CV 168,212,213
v_mfma_f32_16x16x32_bf16 v[132:135], a[92:95], a[44:47], v[184:187]
v_exp_f32 v214, v146
Mvaa 132,88,40
GL 136,234,72,90,2048
Mvaa 132,84,36
s_add_u32 s21, s25, 16908288
v_exp_f32 v215, v147
Mvaa 132,80,32
GL 140,234,72,90,3072
CV 169,214,215
v_mfma_f32_16x16x32_bf16 v[136:139], a[96:99], a[32:35], v[184:187]
s_add_u32 s90, s34, 237568
GL 144,234,72,90,0
v_exp_f32 v212, v148
Mvaa 136,100,36
Mvaa 136,104,40
s_add_u32 s22, s26, 16777216
GL 148,234,72,90,1024
v_exp_f32 v213, v149
Mvaa 136,108,44
CV 170,212,213
v_mfma_f32_16x16x32_bf16 v[140:143], a[124:127], a[44:47], v[184:187]
GL 152,234,72,90,2048
v_exp_f32 v214, v150
Mvaa 140,120,40
Mvaa 140,116,36
s_add_u32 s23, s26, 16908288
GL 156,234,72,90,3072
v_exp_f32 v215, v151
Mvaa 140,112,32
s_add_u32 s90, s34, 110592
GL 160,234,72,90,0
CV 171,214,215
Mvav 0,192,160
v_exp_f32 v212, v152
Mvav 0,196,164
GL 164,234,72,90,1024
Mvav 4,200,160
v_exp_f32 v213, v153
Mvav 4,204,164
GL 168,234,72,90,2048
CV 172,212,213
Mvav 8,208,160
v_exp_f32 v214, v154
Mvav 8,212,164
GL 172,234,72,90,3072
Mvav 12,216,160
s_add_u32 s90, s34, 241664
GL 176,234,72,90,0
v_exp_f32 v215, v155
Mvav 12,220,164
CV 173,214,215
Mvav 16,224,160
GL 180,234,72,90,1024
v_exp_f32 v212, v156
Mvav 16,228,164
Mvav 20,232,160
GL 184,234,72,90,2048
v_exp_f32 v213, v157
Mvav 20,236,164
CV 174,212,213
Mvav 24,240,160
GL 188,234,72,90,3072
v_exp_f32 v214, v158
Mvav 24,244,164
Mvav 28,248,160
v_exp_f32 v215, v159
Mvav 28,252,164
CV 175,214,215
v_smfmac_f32_16x16x64_bf16 v[192:195], v[208:211], v[160:167], v239
v_mfma_f32_16x16x32_bf16 v[144:147], a[64:67], a[48:51], v[188:191]
v_exp_f32 v212, v128
Mvaa 144,68,52
Mvaa 144,72,56
v_exp_f32 v213, v129
Mvaa 144,76,60
CV 160,212,213
v_mfma_f32_16x16x32_bf16 v[148:151], a[92:95], a[60:63], v[188:191]
v_exp_f32 v214, v130
Mvaa 148,88,56
Mvaa 148,84,52
v_exp_f32 v215, v131
v_add_f32 v192, v192, v193
Mvaa 148,80,48
CV 161,214,215
v_rcp_f32 v193, v192
v_mfma_f32_16x16x32_bf16 v[152:155], a[96:99], a[48:51], v[188:191]
v_exp_f32 v212, v132
v_mov_b32 v194, v194
Mvaa 152,100,52
v_mul_f32 v0, v0, v193
v_mul_f32 v1, v1, v193
v_mul_f32 v2, v2, v193
Mvaa 152,104,56
v_exp_f32 v213, v133
v_mul_f32 v3, v3, v193
Mvaa 152,108,60
CV 162,212,213
v_mul_f32 v4, v4, v193
v_mul_f32 v5, v5, v193
v_mfma_f32_16x16x32_bf16 v[156:159], a[124:127], a[60:63], v[188:191]
v_exp_f32 v214, v134
v_mul_f32 v6, v6, v193
Mvaa 156,120,56
v_mul_f32 v7, v7, v193
CV 0,0,1
CV 1,2,3
Mvaa 156,116,52
v_exp_f32 v215, v135
CV 2,4,5
Mvaa 156,112,48
CV 163,214,215
CV 3,6,7
v_mul_f32 v8, v8, v193
Mvav 32,192,168
buffer_store_dwordx4 v[0:3], v234, s[72:75], s20 offen offset:0 sc1
v_exp_f32 v212, v136
v_mul_f32 v9, v9, v193
Mvav 32,196,172
v_mul_f32 v10, v10, v193
v_mul_f32 v11, v11, v193
v_mul_f32 v12, v12, v193
Mvav 36,200,168
v_exp_f32 v213, v137
v_mul_f32 v13, v13, v193
Mvav 36,204,172
CV 164,212,213
v_mul_f32 v14, v14, v193
v_mul_f32 v15, v15, v193
Mvav 40,208,168
v_exp_f32 v214, v138
CV 8,8,9
Mvav 40,212,172
)"
R"(CV 9,10,11
CV 10,12,13
CV 11,14,15
Mvav 44,216,168
buffer_store_dwordx4 v[8:11], v234, s[72:75], s20 offen offset:1024 sc1
v_exp_f32 v215, v139
v_mul_f32 v16, v16, v193
Mvav 44,220,172
CV 165,214,215
v_mul_f32 v17, v17, v193
v_mul_f32 v18, v18, v193
Mvav 48,224,168
v_exp_f32 v212, v140
v_mul_f32 v19, v19, v193
Mvav 48,228,172
v_mul_f32 v20, v20, v193
v_mul_f32 v21, v21, v193
v_mul_f32 v22, v22, v193
Mvav 52,232,168
v_exp_f32 v213, v141
v_mul_f32 v23, v23, v193
Mvav 52,236,172
CV 166,212,213
CV 16,16,17
CV 17,18,19
Mvav 56,240,168
v_exp_f32 v214, v142
CV 18,20,21
Mvav 56,244,172
CV 19,22,23
v_mul_f32 v24, v24, v193
v_mul_f32 v25, v25, v193
Mvav 60,248,168
buffer_store_dwordx4 v[16:19], v234, s[72:75], s20 offen offset:2048 sc1
v_exp_f32 v215, v143
v_mul_f32 v26, v26, v193
Mvav 60,252,172
CV 167,214,215
v_mul_f32 v27, v27, v193
v_mul_f32 v28, v28, v193
v_smfmac_f32_16x16x64_bf16 v[196:199], v[208:211], v[168:175], v239
v_mul_f32 v29, v29, v193
v_mul_f32 v30, v30, v193
v_mul_f32 v31, v31, v193
Mvav 64,192,160
v_exp_f32 v212, v144
CV 24,24,25
Mvav 64,196,164
v_exp_f32 v213, v145
CV 25,26,27
Mvav 68,200,160
v_exp_f32 v214, v146
CV 168,212,213
Mvav 68,204,164
v_exp_f32 v215, v147
CV 26,28,29
Mvav 72,208,160
v_exp_f32 v212, v148
CV 169,214,215
Mvav 72,212,164
v_exp_f32 v213, v149
CV 27,30,31
Mvav 76,216,160
buffer_store_dwordx4 v[24:27], v234, s[72:75], s20 offen offset:3072 sc1
v_exp_f32 v214, v150
CV 170,212,213
Mvav 76,220,164
s_add_u32 s90, s34, 114688
GL 96,234,72,90,0
v_exp_f32 v215, v151
v_add_f32 v196, v196, v197
Mvav 80,224,160
v_exp_f32 v212, v152
CV 171,214,215
Mvav 80,228,164
GL 100,234,72,90,1024
v_exp_f32 v213, v153
Mvav 84,232,160
v_exp_f32 v214, v154
CV 172,212,213
Mvav 84,236,164
GL 104,234,72,90,2048
v_exp_f32 v215, v155
Mvav 88,240,160
v_exp_f32 v212, v156
CV 173,214,215
Mvav 88,244,164
GL 108,234,72,90,3072
v_exp_f32 v213, v157
Mvav 92,248,160
s_add_u32 s90, s34, 245760
GL 112,234,72,90,0
v_exp_f32 v214, v158
CV 174,212,213
Mvav 92,252,164
v_exp_f32 v215, v159
v_smfmac_f32_16x16x64_bf16 v[200:203], v[208:211], v[160:167], v239
GL 116,234,72,90,1024
CV 175,214,215
s_nop 0
Mvav 96,192,168
v_rcp_f32 v197, v196
v_add_f32 v192, v192, v196
Mvav 96,196,172
GL 120,234,72,90,2048
v_mul_f32 v32, v32, v197
v_mul_f32 v33, v33, v197
v_mul_f32 v34, v34, v197
Mvav 100,200,168
v_mul_f32 v35, v35, v197
v_mul_f32 v36, v36, v197
v_mul_f32 v37, v37, v197
Mvav 100,204,172
GL 124,234,72,90,3072
v_mul_f32 v38, v38, v197
v_mul_f32 v39, v39, v197
CV 32,32,33
Mvav 104,208,168
CV 33,34,35
CV 34,36,37
CV 35,38,39
Mvav 104,212,172
buffer_store_dwordx4 v[32:35], v234, s[72:75], s21 offen offset:0 sc1
TR 64,241,16384
v_mul_f32 v40, v40, v197
v_mul_f32 v41, v41, v197
Mvav 108,216,168
TR 66,241,20480
v_mul_f32 v42, v42, v197
v_mul_f32 v43, v43, v197
Mvav 108,220,172
TR 68,241,24576
v_mul_f32 v44, v44, v197
v_mul_f32 v45, v45, v197
Mvav 112,224,168
TR 70,241,28672
v_mul_f32 v46, v46, v197
v_mul_f32 v47, v47, v197
Mvav 112,228,172
TR 72,242,16384
CV 40,40,41
CV 41,42,43
Mvav 116,232,168
TR 74,242,20480
CV 42,44,45
CV 43,46,47
Mvav 116,236,172
TR 76,242,24576
v_mul_f32 v48, v48, v197
v_mul_f32 v49, v49, v197
Mvav 120,240,168
buffer_store_dwordx4 v[40:43], v234, s[72:75], s21 offen offset:1024 sc1
TR 78,242,28672
v_mul_f32 v50, v50, v197
v_mul_f32 v51, v51, v197
Mvav 120,244,172
TR 80,243,16384
v_mul_f32 v52, v52, v197
v_mul_f32 v53, v53, v197
Mvav 124,248,168
TR 82,243,20480
v_mul_f32 v54, v54, v197
v_mul_f32 v55, v55, v197
Mvav 124,252,172
TR 84,243,24576
CV 48,48,49
CV 49,50,51
v_smfmac_f32_16x16x64_bf16 v[204:207], v[208:211], v[168:175], v239
s_waitcnt vmcnt(22) lgkmcnt(7)
TR 86,243,28672
CV 50,52,53
v_mfma_f32_16x16x32_bf16 v[152:155], a[64:67], a[128:131], 0
TR 88,244,16384
CV 51,54,55
v_mul_f32 v56, v56, v197
Mvaa 152,68,132
TR 90,244,20480
v_mul_f32 v57, v57, v197
v_mul_f32 v58, v58, v197
v_mfma_f32_16x16x32_bf16 v[156:159], a[64:67], a[144:147], 0
TR 92,244,24576
v_mul_f32 v59, v59, v197
v_mul_f32 v60, v60, v197
Mvaa 156,68,148
WL 7
TR 94,244,28672
v_mul_f32 v61, v61, v197
v_mfma_f32_16x16x32_bf16 v[164:167], a[72:75], a[144:147], 0
TR 64,245,16384
v_mul_f32 v62, v62, v197
v_mul_f32 v63, v63, v197
Mvaa 164,76,148
TR 66,245,20480
CV 56,56,57
CV 57,58,59
v_mfma_f32_16x16x32_bf16 v[160:163], a[72:75], a[128:131], 0
Lv3stub0_fused_block:
s_cbranch_execz Lv3stub4_fused_block
buffer_store_dwordx4 v[48:51], v234, s[72:75], s21 offen offset:2048 sc1
TR 68,245,24576
CV 58,60,61
CV 59,62,63
Mvaa 160,76,132
WL 7
TR 70,245,28672
v_add_f32 v200, v200, v201
v_mfma_f32_16x16x32_bf16 v[168:171], a[80:83], a[128:131], 0
TR 72,246,16384
v_rcp_f32 v201, v200
Mvaa 168,84,132
TR 74,246,20480
v_add_f32 v192, v192, v200
v_mul_f32 v64, v64, v201
v_mfma_f32_16x16x32_bf16 v[172:175], a[80:83], a[144:147], 0
TR 76,246,24576
v_mul_f32 v65, v65, v201
v_mul_f32 v66, v66, v201
Mvaa 172,84,148
WL 7
TR 78,246,28672
v_mul_f32 v67, v67, v201
v_mfma_f32_16x16x32_bf16 v[180:183], a[88:91], a[144:147], 0
TR 80,247,16384
v_mul_f32 v68, v68, v201
v_mul_f32 v69, v69, v201
Mvaa 180,92,148
TR 82,247,20480
v_mul_f32 v70, v70, v201
v_mul_f32 v71, v71, v201
v_mfma_f32_16x16x32_bf16 v[176:179], a[88:91], a[128:131], 0
TR 84,247,24576
CV 64,64,65
CV 65,66,67
Mvaa 176,92,132
WL 7
TR 86,247,28672
CV 66,68,69
v_mfma_f32_16x16x32_bf16 v[184:187], a[64:67], a[128:131], 0
TR 88,248,16384
CV 67,70,71
v_mul_f32 v72, v72, v201
Mvaa 184,68,132
buffer_store_dwordx4 v[56:59], v234, s[72:75], s21 offen offset:3072 sc1
TR 90,248,20480
v_mul_f32 v73, v73, v201
v_mul_f32 v74, v74, v201
v_mfma_f32_16x16x32_bf16 v[188:191], a[64:67], a[144:147], 0
TR 92,248,24576
v_mul_f32 v75, v75, v201
v_mul_f32 v76, v76, v201
Mvaa 188,68,148
WL 7
TR 94,248,28672
v_mul_f32 v77, v77, v201
v_mfma_f32_16x16x32_bf16 v[212:215], a[72:75], a[144:147], 0
TR 64,240,4096
v_mul_f32 v78, v78, v201
v_mul_f32 v79, v79, v201
Mvaa 212,76,148
TR 66,240,4608
CV 72,72,73
CV 73,74,75
v_mfma_f32_16x16x32_bf16 v[208:211], a[72:75], a[128:131], 0
TR 68,240,5120
CV 74,76,77
CV 75,78,79
Mvaa 208,76,132
WL 7
TR 70,240,5632
v_mul_f32 v80, v80, v201
v_mfma_f32_16x16x32_bf16 v[128:131], a[80:83], a[128:131], 0
TR 72,220,16384
v_mul_f32 v81, v81, v201
v_mul_f32 v82, v82, v201
Mvaa 128,84,132
TR 74,220,20480
v_mul_f32 v83, v83, v201
v_mul_f32 v84, v84, v201
v_mfma_f32_16x16x32_bf16 v[132:135], a[80:83], a[144:147], 0
TR 76,220,24576
v_mul_f32 v85, v85, v201
v_mul_f32 v86, v86, v201
Mvaa 132,84,148
WL 7
TR 78,220,28672
v_mul_f32 v87, v87, v201
v_mfma_f32_16x16x32_bf16 v[140:143], a[88:91], a[144:147], 0
s_add_u32 m0, s84, 0
TR 80,221,16384
CV 80,80,81
CV 81,82,83
Mvaa 140,92,148
DM 235,64,87,0
TR 82,221,20480
CV 82,84,85
CV 83,86,87
v_mfma_f32_16x16x32_bf16 v[136:139], a[88:91], a[128:131], 0
TR 84,221,24576
v_mul_f32 v88, v88, v201
v_mul_f32 v89, v89, v201
Mvaa 136,92,132
WL 7
DM 236,64,87,1024
TR 86,221,28672
v_mul_f32 v90, v90, v201
v_mfma_f32_16x16x32_bf16 v[148:151], a[64:67], a[128:131], 0
TR 88,222,16384
v_mul_f32 v91, v91, v201
v_mul_f32 v92, v92, v201
Mvaa 148,68,132
TR 90,222,20480
v_mul_f32 v93, v93, v201
v_mul_f32 v94, v94, v201
v_mfma_f32_16x16x32_bf16 v[144:147], a[64:67], a[144:147], 0
TR 92,222,24576
v_mul_f32 v95, v95, v201
CV 88,88,89
Mvaa 144,68,148
WL 7
DM 237,64,87,2048
TR 94,222,28672
CV 89,90,91
Mvaa 156,72,152
TR 64,223,16384
CV 90,92,93
CV 91,94,95
Mvaa 156,76,156
TR 66,223,20480
v_add_f32 v204, v204, v205
Mvaa 152,72,136
DM 238,64,87,3072
TR 68,223,24576
v_rcp_f32 v205, v204
Mvaa 152,76,140
WL 7
TR 70,223,28672
v_add_f32 v192, v192, v204
Mvaa 160,80,136
TR 72,224,16384
v_cmp_nge_f32 vcc, 0x53800000, v192
s_lshl_b32 s96, s19, 2
s_add_u32 s96, s96, 147456
s_cmp_lg_u64 vcc, 0
s_cselect_b32 s95, 1, 0
v_mov_b32 v249, s96
v_mov_b32 v250, s95
ds_write_b32 v249, v250
v_mov_b32 v249, 147456
Mvaa 160,84,140
TR 74,224,20480
v_mul_f32 v96, v96, v205
v_mul_f32 v97, v97, v205
Mvaa 164,80,152
s_add_u32 m0, s85, 0
TR 76,224,24576
v_mul_f32 v98, v98, v205
v_mul_f32 v99, v99, v205
Mvaa 164,84,156
WL 8
DM 235,64,88,0
TR 78,224,28672
v_mul_f32 v100, v100, v205
Mvaa 172,88,152
TR 80,225,16384
v_mul_f32 v101, v101, v205
v_mul_f32 v102, v102, v205
Mvaa 172,92,156
DM 236,64,88,1024
TR 82,225,20480
v_mul_f32 v103, v103, v205
CV 96,96,97
Mvaa 168,88,136
TR 84,225,24576
CV 97,98,99
CV 98,100,101
Mvaa 168,92,140
WL 8
TR 86,225,28672
CV 99,102,103
Mvaa 176,64,136
TR 88,226,16384
v_mul_f32 v104, v104, v205
v_mul_f32 v105, v105, v205
Mvaa 176,68,140
DM 237,64,88,2048
TR 90,226,20480
v_mul_f32 v106, v106, v205
v_mul_f32 v107, v107, v205
Mvaa 180,64,152
TR 92,226,24576
v_mul_f32 v108, v108, v205
v_mul_f32 v109, v109, v205
Mvaa 180,68,156
WL 7
TR 94,226,28672
v_mul_f32 v110, v110, v205
Mvaa 188,72,152
DM 238,64,88,3072
TR 64,227,16384
v_mul_f32 v111, v111, v205
)"
R"(CV 104,104,105
Mvaa 188,76,156
TR 66,227,20480
CV 105,106,107
CV 106,108,109
Mvaa 184,72,136
TR 68,227,24576
CV 107,110,111
v_mul_f32 v112, v112, v205
Mvaa 184,76,140
WL 7
TR 70,227,28672
v_mul_f32 v113, v113, v205
Mvaa 208,80,136
s_add_u32 m0, s86, 0
TR 72,240,6144
v_mul_f32 v114, v114, v205
v_mul_f32 v115, v115, v205
Mvaa 208,84,140
buffer_load_dwordx4 v234, s[72:75], s89 offen offset:0 sc1 lds
TR 74,240,6656
v_mul_f32 v116, v116, v205
v_mul_f32 v117, v117, v205
Mvaa 212,80,152
TR 76,240,7168
v_mul_f32 v118, v118, v205
v_mul_f32 v119, v119, v205
Mvaa 212,84,156
WL 7
s_add_u32 s90, s34, 118784
GL 192,234,72,90,0
TR 78,240,7680
CV 112,112,113
Mvaa 132,88,152
WL 0
s_barrier
TR 80,241,32768
CV 113,114,115
Mvaa 132,92,156
TR 82,241,36864
CV 114,116,117
CV 115,118,119
Mvaa 128,88,136
TR 84,241,40960
ds_read_b128 v[4:7], v249
v_mul_f32 v120, v120, v205
v_mul_f32 v121, v121, v205
Mvaa 128,92,140
GL 196,234,72,90,1024
TR 86,241,45056
v_mul_f32 v122, v122, v205
Mvaa 136,64,136
TR 88,242,32768
v_mul_f32 v123, v123, v205
v_mul_f32 v124, v124, v205
Mvaa 136,68,140
TR 90,242,36864
v_mul_f32 v125, v125, v205
v_mul_f32 v126, v126, v205
Mvaa 140,64,152
GL 200,234,72,90,2048
TR 92,242,40960
v_mul_f32 v127, v127, v205
CV 120,120,121
Mvaa 140,68,156
TR 94,242,45056
CV 121,122,123
Mvaa 144,72,152
TR 64,243,32768
CV 122,124,125
CV 123,126,127
Mvaa 144,76,156
TR 66,243,36864
Mvaa 148,72,136
GL 204,234,72,90,3072
TR 68,243,40960
Mvaa 148,76,140
s_waitcnt vmcnt(29) lgkmcnt(7)
TR 70,243,45056
Mvaa 152,80,160
TR 72,244,32768
Mvaa 152,84,164
s_add_u32 s90, s34, 249856
GL 208,234,72,90,0
TR 74,244,36864
Mvaa 156,80,176
TR 76,244,40960
v_or3_b32 v249, v4, v5, v6
v_or_b32 v249, v249, v7
s_nop 0
v_readfirstlane_b32 s97, v249
s_nop 3
s_andn2_b32 s97, s97, s94
s_cbranch_scc1 Lretry_fused_block
Mvaa 156,84,180
WL 7
TR 78,244,45056
Mvaa 164,88,176
TR 80,245,32768
Mvaa 164,92,180
GL 212,234,72,90,1024
TR 82,245,36864
Mvaa 160,88,160
TR 84,245,40960
Mvaa 160,92,164
WL 7
TR 86,245,45056
Mvaa 168,64,160
GL 216,234,72,90,2048
TR 88,246,32768
Mvaa 168,68,164
TR 90,246,36864
Mvaa 172,64,176
TR 92,246,40960
Mvaa 172,68,180
WL 7
TR 94,246,45056
Mvaa 180,72,176
GL 220,234,72,90,3072
TR 64,247,32768
Mvaa 180,76,180
TR 66,247,36864
Mvaa 176,72,160
TR 68,247,40960
Mvaa 176,76,164
WL 7
buffer_store_dwordx4 v[64:67], v234, s[72:75], s22 offen offset:0 sc1
TR 70,247,45056
Mvaa 184,80,160
TR 72,248,32768
Mvaa 184,84,164
TR 74,248,36864
Mvaa 188,80,176
TR 76,248,40960
Mvaa 188,84,180
WL 7
TR 78,248,45056
Mvaa 212,88,176
TR 80,240,8192
Mvaa 212,92,180
TR 82,240,8704
Mvaa 208,88,160
TR 84,240,9216
Mvaa 208,92,164
WL 7
TR 86,240,9728
Mvaa 128,64,160
TR 88,220,32768
Mvaa 128,68,164
TR 90,220,36864
Mvaa 132,64,176
buffer_store_dwordx4 v[72:75], v234, s[72:75], s22 offen offset:1024 sc1
TR 92,220,40960
Mvaa 132,68,180
WL 7
TR 94,220,45056
Mvaa 140,72,176
TR 64,221,32768
Mvaa 140,76,180
TR 66,221,36864
Mvaa 136,72,160
TR 68,221,40960
Mvaa 136,76,164
WL 7
TR 70,221,45056
Mvaa 148,80,160
TR 72,222,32768
Mvaa 148,84,164
TR 74,222,36864
Mvaa 144,80,176
TR 76,222,40960
Mvaa 144,84,180
WL 7
TR 78,222,45056
Mvaa 156,88,184
TR 80,223,32768
Mvaa 156,92,188
buffer_store_dwordx4 v[80:83], v234, s[72:75], s22 offen offset:2048 sc1
TR 82,223,36864
Mvaa 152,88,168
TR 84,223,40960
Mvaa 152,92,172
WL 7
TR 86,223,45056
Mvaa 160,64,168
TR 88,224,32768
Mvaa 160,68,172
TR 90,224,36864
Mvaa 164,64,184
TR 92,224,40960
Mvaa 164,68,188
WL 7
TR 94,224,45056
Mvaa 172,72,184
TR 64,225,32768
Mvaa 172,76,188
TR 66,225,36864
Mvaa 168,72,168
TR 68,225,40960
Mvaa 168,76,172
WL 7
TR 70,225,45056
Mvaa 176,80,168
buffer_store_dwordx4 v[88:91], v234, s[72:75], s22 offen offset:3072 sc1
TR 72,226,32768
Mvaa 176,84,172
TR 74,226,36864
Mvaa 180,80,184
TR 76,226,40960
Mvaa 180,84,188
WL 7
TR 78,226,45056
Mvaa 188,88,184
TR 80,227,32768
Mvaa 188,92,188
TR 82,227,36864
Mvaa 184,88,168
TR 84,227,40960
Mvaa 184,92,172
WL 7
TR 86,227,45056
Mvaa 208,64,168
TR 88,240,10240
Mvaa 208,68,172
TR 90,240,10752
Mvaa 212,64,184
TR 92,240,11264
Mvaa 212,68,188
WL 7
s_add_u32 s87, s28, 4423680
s_add_u32 s88, s28, 4497408
TR 94,240,11776
Mvaa 132,72,184
WL 0
s_barrier
s_add_u32 s89, s29, 122880
TR 64,241,49152
Mvaa 132,76,188
s_add_u32 m0, s84, 16384
TR 66,241,53248
Mvaa 128,72,168
DM 235,64,87,0
TR 68,241,57344
Mvaa 128,76,172
TR 70,241,61440
Mvaa 136,80,168
DM 236,64,87,1024
TR 72,242,49152
Mvaa 136,84,172
TR 74,242,53248
Mvaa 140,80,184
DM 237,64,87,2048
TR 76,242,57344
Mvaa 140,84,188
TR 78,242,61440
Mvaa 144,88,184
TR 80,243,49152
Mvaa 144,92,188
TR 82,243,53248
Mvaa 148,88,168
DM 238,64,87,3072
TR 84,243,57344
Mvaa 148,92,172
s_waitcnt vmcnt(29) lgkmcnt(7)
TR 86,243,61440
Mvaa 152,64,96
TR 88,244,49152
Mvaa 152,68,100
s_add_u32 m0, s85, 16384
TR 90,244,53248
Mvaa 156,64,112
DM 235,64,88,0
TR 92,244,57344
Mvaa 156,68,116
WL 7
TR 94,244,61440
Mvaa 164,72,112
TR 64,245,49152
Mvaa 164,76,116
DM 236,64,88,1024
TR 66,245,53248
Mvaa 160,72,96
TR 68,245,57344
Mvaa 160,76,100
WL 7
TR 70,245,61440
Mvaa 168,80,96
DM 237,64,88,2048
TR 72,246,49152
Mvaa 168,84,100
TR 74,246,53248
Mvaa 172,80,112
TR 76,246,57344
Mvaa 172,84,116
WL 7
TR 78,246,61440
Mvaa 180,88,112
DM 238,64,88,3072
TR 80,247,49152
Mvaa 180,92,116
TR 82,247,53248
Mvaa 176,88,96
TR 84,247,57344
Mvaa 176,92,100
WL 7
s_add_u32 m0, s86, 4096
TR 86,247,61440
Mvaa 184,64,96
buffer_load_dwordx4 v234, s[72:75], s89 offen offset:0 sc1 lds
TR 88,248,49152
Mvaa 184,68,100
TR 90,248,53248
Mvaa 188,64,112
TR 92,248,57344
Mvaa 188,68,116
WL 7
s_add_u32 s90, s34, 122880
GL 224,234,72,90,0
TR 94,248,61440
Mvaa 212,72,112
TR 64,240,12288
Mvaa 212,76,116
TR 66,240,12800
Mvaa 208,72,96
GL 228,234,72,90,1024
TR 68,240,13312
Mvaa 208,76,100
WL 7
TR 70,240,13824
Mvaa 128,80,96
TR 72,220,49152
Mvaa 128,84,100
TR 74,220,53248
Mvaa 132,80,112
GL 232,234,72,90,2048
TR 76,220,57344
Mvaa 132,84,116
WL 7
TR 78,220,61440
Mvaa 140,88,112
TR 80,221,49152
Mvaa 140,92,116
GL 236,234,72,90,3072
TR 82,221,53248
Mvaa 136,88,96
TR 84,221,57344
Mvaa 136,92,100
WL 7
TR 86,221,61440
Mvaa 148,64,96
TR 88,222,49152
Mvaa 148,68,100
s_add_u32 s90, s34, 253952
GL 240,234,72,90,0
TR 90,222,53248
Mvaa 144,64,112
TR 92,222,57344
Mvaa 144,68,116
WL 7
TR 94,222,61440
Mvaa 156,72,120
GL 244,234,72,90,1024
TR 64,223,49152
Mvaa 156,76,124
TR 66,223,53248
Mvaa 152,72,104
TR 68,223,57344
Mvaa 152,76,108
WL 7
TR 70,223,61440
Mvaa 160,80,104
GL 248,234,72,90,2048
TR 72,224,49152
Mvaa 160,84,108
Lv3stub1_fused_block:
s_cbranch_execz Lv3stub5_fused_block
TR 74,224,53248
Mvaa 164,80,120
TR 76,224,57344
Mvaa 164,84,124
WL 7
GL 252,234,72,90,3072
TR 78,224,61440
Mvaa 172,88,120
TR 80,225,49152
Mvaa 172,92,124
TR 82,225,53248
Mvaa 168,88,104
TR 84,225,57344
Mvaa 168,92,108
WL 7
buffer_store_dwordx4 v[96:99], v234, s[72:75], s23 offen offset:0 sc1
TR 86,225,61440
Mvaa 176,64,104
TR 88,226,49152
Mvaa 176,68,108
TR 90,226,53248
Mvaa 180,64,120
TR 92,226,57344
Mvaa 180,68,124
)"
R"(WL 7
TR 94,226,61440
Mvaa 188,72,120
TR 64,227,49152
Mvaa 188,76,124
TR 66,227,53248
Mvaa 184,72,104
TR 68,227,57344
Mvaa 184,76,108
WL 7
TR 70,227,61440
Mvaa 208,80,104
TR 72,240,14336
Mvaa 208,84,108
TR 74,240,14848
Mvaa 212,80,120
buffer_store_dwordx4 v[104:107], v234, s[72:75], s23 offen offset:1024 sc1
TR 76,240,15360
Mvaa 212,84,124
WL 7
TR 78,240,15872
Mvaa 132,88,120
s_waitcnt vmcnt(31) lgkmcnt(0)
s_barrier
TR 80,241,0
Mvaa 132,92,124
TR 82,241,4096
Mvaa 128,88,104
TR 84,241,8192
Mvaa 128,92,108
TR 86,241,12288
Mvaa 136,64,104
TR 88,242,0
Mvaa 136,68,108
TR 90,242,4096
Mvaa 140,64,120
TR 92,242,8192
Mvaa 140,68,124
TR 94,242,12288
Mvaa 144,72,120
TR 64,243,0
Mvaa 144,76,124
buffer_store_dwordx4 v[112:115], v234, s[72:75], s23 offen offset:2048 sc1
TR 66,243,4096
Mvaa 148,72,104
TR 68,243,8192
Mvaa 148,76,108
s_waitcnt vmcnt(24) lgkmcnt(7)
TR 70,243,12288
Mvaa 152,80,192
TR 72,244,0
Mvaa 152,84,196
TR 74,244,4096
Mvaa 156,80,208
TR 76,244,8192
Mvaa 156,84,212
WL 7
TR 78,244,12288
Mvaa 164,88,208
TR 80,245,0
Mvaa 164,92,212
TR 82,245,4096
Mvaa 160,88,192
TR 84,245,8192
Mvaa 160,92,196
WL 7
TR 86,245,12288
Mvaa 168,64,192
buffer_store_dwordx4 v[120:123], v234, s[72:75], s23 offen offset:3072 sc1
TR 88,246,0
Mvaa 168,68,196
TR 90,246,4096
Mvaa 172,64,208
TR 92,246,8192
Mvaa 172,68,212
WL 7
TR 94,246,12288
Mvaa 180,72,208
TR 64,247,0
Mvaa 180,76,212
TR 66,247,4096
Mvaa 176,72,192
TR 68,247,8192
Mvaa 176,76,196
WL 7
TR 70,247,12288
Mvaa 184,80,192
TR 72,248,0
Mvaa 184,84,196
TR 74,248,4096
Mvaa 188,80,208
TR 76,248,8192
Mvaa 188,84,212
WL 7
s_add_u32 s87, s28, 4571136
s_add_u32 s88, s28, 4644864
TR 78,248,12288
Mvaa 212,88,208
s_add_u32 s89, s29, 126976
s_add_u32 m0, s84, 32768
TR 80,240,0
Mvaa 212,92,212
DM 235,64,87,0
TR 82,240,512
Mvaa 208,88,192
TR 84,240,1024
Mvaa 208,92,196
WL 7
DM 236,64,87,1024
TR 86,240,1536
Mvaa 128,64,192
TR 88,220,0
Mvaa 128,68,196
TR 90,220,4096
Mvaa 132,64,208
DM 237,64,87,2048
TR 92,220,8192
Mvaa 132,68,212
WL 7
TR 94,220,12288
Mvaa 140,72,208
TR 64,221,0
Mvaa 140,76,212
DM 238,64,87,3072
TR 66,221,4096
Mvaa 136,72,192
TR 68,221,8192
Mvaa 136,76,196
WL 7
TR 70,221,12288
Mvaa 148,80,192
TR 72,222,0
Mvaa 148,84,196
s_add_u32 m0, s85, 32768
TR 74,222,4096
Mvaa 144,80,208
DM 235,64,88,0
TR 76,222,8192
Mvaa 144,84,212
WL 7
TR 78,222,12288
Mvaa 156,88,216
DM 236,64,88,1024
TR 80,223,0
Mvaa 156,92,220
TR 82,223,4096
Mvaa 152,88,200
TR 84,223,8192
Mvaa 152,92,204
WL 7
TR 86,223,12288
Mvaa 160,64,200
DM 237,64,88,2048
TR 88,224,0
Mvaa 160,68,204
TR 90,224,4096
Mvaa 164,64,216
TR 92,224,8192
Mvaa 164,68,220
WL 7
DM 238,64,88,3072
TR 94,224,12288
Mvaa 172,72,216
TR 64,225,0
Mvaa 172,76,220
TR 66,225,4096
Mvaa 168,72,200
TR 68,225,8192
Mvaa 168,76,204
WL 7
s_add_u32 m0, s86, 8192
TR 70,225,12288
Mvaa 176,80,200
buffer_load_dwordx4 v234, s[72:75], s89 offen offset:0 sc1 lds
TR 72,226,0
Mvaa 176,84,204
TR 74,226,4096
Mvaa 180,80,216
s_add_u32 s90, s34, 126976
GL 128,234,72,90,0
TR 76,226,8192
Mvaa 180,84,220
WL 7
TR 78,226,12288
Mvaa 188,88,216
TR 80,227,0
Mvaa 188,92,220
TR 82,227,4096
Mvaa 184,88,200
GL 132,234,72,90,1024
TR 84,227,8192
Mvaa 184,92,204
WL 7
TR 86,227,12288
Mvaa 208,64,200
TR 88,240,2048
Mvaa 208,68,204
GL 136,234,72,90,2048
TR 90,240,2560
Mvaa 212,64,216
TR 92,240,3072
Mvaa 212,68,220
WL 7
TR 94,240,3584
Mvaa 132,72,216
s_waitcnt vmcnt(24) lgkmcnt(0)
s_barrier
TR 64,241,16384
Mvaa 132,76,220
GL 140,234,72,90,3072
TR 66,241,20480
Mvaa 128,72,200
TR 68,241,24576
Mvaa 128,76,204
TR 70,241,28672
Mvaa 136,80,200
s_add_u32 s90, s34, 258048
GL 144,234,72,90,0
TR 72,242,16384
Mvaa 136,84,204
TR 74,242,20480
Mvaa 140,80,216
TR 76,242,24576
Mvaa 140,84,220
TR 78,242,28672
Mvaa 144,88,216
GL 148,234,72,90,1024
TR 80,243,16384
Mvaa 144,92,220
TR 82,243,20480
Mvaa 148,88,200
TR 84,243,24576
Mvaa 148,92,204
s_waitcnt vmcnt(19) lgkmcnt(7)
GL 152,234,72,90,2048
TR 86,243,28672
Mvaa 152,64,224
TR 88,244,16384
Mvaa 152,68,228
TR 90,244,20480
Mvaa 156,64,240
TR 92,244,24576
Mvaa 156,68,244
WL 7
GL 156,234,72,90,3072
TR 94,244,28672
Mvaa 164,72,240
TR 64,245,16384
Mvaa 164,76,244
TR 66,245,20480
Mvaa 160,72,224
s_waitcnt vmcnt(17)
s_add_u32 s98, s37, 262144
TR 68,245,24576
v_mov_b32 v250, s82
Mvaa 160,76,228
WL 7
TR 70,245,28672
v_mov_b32 v251, s83
Mvaa 168,80,224
TR 72,246,16384
v_lshlrev_b32 v249, 3, v255
s_mov_b64 exec, 0xffff
buffer_store_dwordx2 v[250:251], v249, s[72:75], s98 offen sc1
s_mov_b64 exec, -1
Mvaa 168,84,228
s_add_u32 s98, s36, 265216
TR 74,246,20480
v_and_b32 v249, 3, v255
Mvaa 172,80,240
TR 76,246,24576
v_lshlrev_b32 v249, 7, v249
Mvaa 172,84,244
WL 7
buffer_load_dwordx2 v[250:251], v249, s[72:75], s98 offen sc1
TR 78,246,28672
Mvaa 180,88,240
s_add_u32 s87, s28, 5160960
s_add_u32 s88, s28, 5234688
s_add_u32 s89, s29, 143360
TR 80,247,16384
Mvaa 180,92,244
s_add_u32 m0, s84, 49152
TR 82,247,20480
Mvaa 176,88,224
DM 235,64,87,0
TR 84,247,24576
Mvaa 176,92,228
WL 7
TR 86,247,28672
Mvaa 184,64,224
DM 236,64,87,1024
TR 88,248,16384
Mvaa 184,68,228
TR 90,248,20480
Mvaa 188,64,240
DM 237,64,87,2048
TR 92,248,24576
Mvaa 188,68,244
WL 7
TR 94,248,28672
Mvaa 212,72,240
DM 238,64,87,3072
TR 64,240,4096
Mvaa 212,76,244
TR 66,240,4608
Mvaa 208,72,224
s_add_u32 m0, s85, 49152
TR 68,240,5120
Mvaa 208,76,228
WL 7
DM 235,64,88,0
TR 70,240,5632
Mvaa 128,80,224
TR 72,220,16384
Mvaa 128,84,228
DM 236,64,88,1024
TR 74,220,20480
Mvaa 132,80,240
TR 76,220,24576
Mvaa 132,84,244
WL 7
TR 78,220,28672
Mvaa 140,88,240
TR 80,221,16384
Mvaa 140,92,244
DM 237,64,88,2048
TR 82,221,20480
Mvaa 136,88,224
TR 84,221,24576
Mvaa 136,92,228
WL 7
TR 86,221,28672
Mvaa 148,64,224
DM 238,64,88,3072
TR 88,222,16384
Mvaa 148,68,228
TR 90,222,20480
Mvaa 144,64,240
TR 92,222,24576
Mvaa 144,68,244
WL 7
TR 94,222,28672
Mvaa 156,72,248
s_add_u32 m0, s86, 12288
TR 64,223,16384
Mvaa 156,76,252
buffer_load_dwordx4 v234, s[72:75], s89 offen offset:0 sc1 lds
TR 66,223,20480
Mvaa 152,72,232
TR 68,223,24576
Mvaa 152,76,236
WL 7
s_add_u32 s90, s33, 12288
GL 160,234,72,90,0
TR 70,223,28672
Mvaa 160,80,232
TR 72,224,16384
Mvaa 160,84,236
TR 74,224,20480
Mvaa 164,80,248
TR 76,224,24576
Mvaa 164,84,252
WL 7
GL 164,234,72,90,1024
TR 78,224,28672
Mvaa 172,88,248
TR 80,225,16384
Mvaa 172,92,252
TR 82,225,20480
Mvaa 168,88,232
GL 168,234,72,90,2048
TR 84,225,24576
Mvaa 168,92,236
WL 7
TR 86,225,28672
Mvaa 176,64,232
TR 88,226,16384
Mvaa 176,68,236
TR 90,226,20480
Mvaa 180,64,248
GL 172,234,72,90,3072
TR 92,226,24576
Mvaa 180,68,252
WL 7
TR 94,226,28672
Mvaa 188,72,248
TR 64,227,16384
Mvaa 188,76,252
s_add_u32 s90, s33, 28672
GL 176,234,72,90,0
TR 66,227,20480
Mvaa 184,72,232
TR 68,227,24576
Mvaa 184,76,236
WL 7
TR 70,227,28672
Mvaa 208,80,232
TR 72,240,6144
Mvaa 208,84,236
)"
R"(GL 180,234,72,90,1024
TR 74,240,6656
Mvaa 212,80,248
TR 76,240,7168
Mvaa 212,84,252
WL 7
TR 78,240,7680
Mvaa 132,88,248
s_waitcnt vmcnt(25) lgkmcnt(0)
s_barrier
GL 184,234,72,90,2048
TR 80,241,32768
Mvaa 132,92,252
TR 82,241,36864
Mvaa 128,88,232
TR 84,241,40960
Mvaa 128,92,236
TR 86,241,45056
Mvaa 136,64,232
GL 188,234,72,90,3072
TR 88,242,32768
Mvaa 136,68,236
TR 90,242,36864
Mvaa 140,64,248
TR 92,242,40960
Mvaa 140,68,252
s_add_u32 s87, s28, 3538944
s_add_u32 s88, s28, 3612672
TR 94,242,45056
Mvaa 144,72,248
s_add_u32 s89, s29, 245760
s_add_u32 m0, s84, 0
TR 64,243,32768
Mvaa 144,76,252
DM 235,56,87,0
TR 66,243,36864
Mvaa 148,72,232
TR 68,243,40960
Mvaa 148,76,236
s_waitcnt vmcnt(20) lgkmcnt(7)
DM 236,56,87,1024
TR 70,243,45056
Mvaa 152,80,128
TR 72,244,32768
Mvaa 152,84,132
TR 74,244,36864
Mvaa 156,80,144
DM 237,56,87,2048
TR 76,244,40960
Mvaa 156,84,148
WL 7
TR 78,244,45056
Mvaa 164,88,144
TR 80,245,32768
Mvaa 164,92,148
TR 82,245,36864
Mvaa 160,88,128
DM 238,56,87,3072
TR 84,245,40960
Mvaa 160,92,132
WL 7
TR 86,245,45056
Mvaa 168,64,128
TR 88,246,32768
Mvaa 168,68,132
s_add_u32 m0, s85, 0
TR 90,246,36864
Mvaa 172,64,144
DM 235,56,88,0
TR 92,246,40960
Mvaa 172,68,148
WL 7
TR 94,246,45056
Mvaa 180,72,144
TR 64,247,32768
Mvaa 180,76,148
DM 236,56,88,1024
TR 66,247,36864
Mvaa 176,72,128
TR 68,247,40960
Mvaa 176,76,132
WL 7
TR 70,247,45056
Mvaa 184,80,128
DM 237,56,88,2048
TR 72,248,32768
Mvaa 184,84,132
TR 74,248,36864
Mvaa 188,80,144
TR 76,248,40960
Mvaa 188,84,148
WL 7
TR 78,248,45056
Mvaa 212,88,144
DM 238,56,88,3072
TR 80,240,8192
Mvaa 212,92,148
TR 82,240,8704
Mvaa 208,88,128
TR 84,240,9216
Mvaa 208,92,132
WL 7
s_add_u32 m0, s86, 0
TR 86,240,9728
Mvaa 128,64,128
buffer_load_dwordx4 v234, s[72:75], s89 offen offset:0 sc1 lds
TR 88,220,32768
Mvaa 128,68,132
TR 90,220,36864
Mvaa 132,64,144
TR 92,220,40960
Mvaa 132,68,148
WL 7
s_waitcnt vmcnt(26)
TR 94,220,45056
v_cmp_ne_u64 vcc, s[82:83], v[250:251]
s_nop 3
s_cbranch_vccnz Lslow9_fused_block
Lslowb9_fused_block:
Mvaa 140,72,144
Lv3stub2_fused_block:
s_cbranch_execz Lt2x_fused_block
s_add_u32 s90, s34, 16875520
GL 96,234,72,90,0
TR 64,221,32768
Mvaa 140,76,148
TR 66,221,36864
Mvaa 136,72,128
GL 100,234,72,90,1024
TR 68,221,40960
Mvaa 136,76,132
WL 7
TR 70,221,45056
Mvaa 148,80,128
TR 72,222,32768
Mvaa 148,84,132
TR 74,222,36864
Mvaa 144,80,144
GL 104,234,72,90,2048
TR 76,222,40960
Mvaa 144,84,148
WL 7
TR 78,222,45056
Mvaa 156,88,152
TR 80,223,32768
Mvaa 156,92,156
GL 108,234,72,90,3072
TR 82,223,36864
Mvaa 152,88,136
TR 84,223,40960
Mvaa 152,92,140
WL 7
TR 86,223,45056
Mvaa 160,64,136
TR 88,224,32768
Mvaa 160,68,140
s_add_u32 s90, s34, 17006592
GL 112,234,72,90,0
TR 90,224,36864
Mvaa 164,64,152
TR 92,224,40960
Mvaa 164,68,156
WL 7
TR 94,224,45056
Mvaa 172,72,152
GL 116,234,72,90,1024
TR 64,225,32768
Mvaa 172,76,156
TR 66,225,36864
Mvaa 168,72,136
TR 68,225,40960
Mvaa 168,76,140
WL 7
TR 70,225,45056
Mvaa 176,80,136
GL 120,234,72,90,2048
TR 72,226,32768
Mvaa 176,84,140
TR 74,226,36864
Mvaa 180,80,152
TR 76,226,40960
Mvaa 180,84,156
WL 7
GL 124,234,72,90,3072
TR 78,226,45056
Mvaa 188,88,152
TR 80,227,32768
Mvaa 188,92,156
TR 82,227,36864
Mvaa 184,88,136
TR 84,227,40960
Mvaa 184,92,140
WL 7
s_add_u32 s87, s28, 3686400
s_add_u32 s88, s28, 3760128
TR 86,227,45056
Mvaa 208,64,136
s_add_u32 s89, s29, 249856
s_add_u32 m0, s84, 16384
TR 88,240,10240
Mvaa 208,68,140
DM 235,56,87,0
TR 90,240,10752
Mvaa 212,64,152
TR 92,240,11264
Mvaa 212,68,156
WL 7
DM 236,56,87,1024
TR 94,240,11776
Mvaa 132,72,152
s_waitcnt vmcnt(27) lgkmcnt(0)
s_barrier
TR 64,241,49152
Mvaa 132,76,156
TR 66,241,53248
Mvaa 128,72,136
DM 237,56,87,2048
TR 68,241,57344
Mvaa 128,76,140
TR 70,241,61440
Mvaa 136,80,136
TR 72,242,49152
Mvaa 136,84,140
DM 238,56,87,3072
TR 74,242,53248
Mvaa 140,80,152
TR 76,242,57344
Mvaa 140,84,156
TR 78,242,61440
Mvaa 144,88,152
TR 80,243,49152
Mvaa 144,92,156
s_add_u32 m0, s85, 16384
TR 82,243,53248
Mvaa 148,88,136
DM 235,56,88,0
TR 84,243,57344
Mvaa 148,92,140
s_waitcnt vmcnt(22) lgkmcnt(7)
TR 86,243,61440
Mvaa 152,64,160
DM 236,56,88,1024
TR 88,244,49152
v_lshrrev_b32 v79, 4, v255
v_and_b32 v78, 7, v255
Mvaa 152,68,164
TR 90,244,53248
v_lshl_add_u32 v78, v79, 3, v78
v_lshlrev_b32 v72, 3, v78
Mvaa 156,64,176
TR 92,244,57344
v_and_b32 v78, 15, v255
v_mul_u32_u24 v77, 1152, v78
Mvaa 156,68,180
WL 7
TR 94,244,61440
v_lshl_add_u32 v75, v79, 3, v77
Mvaa 164,72,176
s_lshl_b32 s95, s80, 5
s_add_u32 s95, s95, 1024
DM 237,56,88,2048
TR 64,245,49152
v_add_u32 v76, s95, v75
Mvaa 164,76,180
TR 66,245,53248
v_cmp_gt_u32 vcc, 8, v78
v_mov_b32 v78, 0x7ffffff0
s_nop 0
v_cndmask_b32 v73, v72, v78, vcc
v_cndmask_b32 v72, v78, v72, vcc
v_cndmask_b32 v76, v76, v78, vcc
Mvaa 160,72,160
TR 68,245,57344
v_mov_b32 v74, v73
v_and_b32 v78, 1, v79
Mvaa 160,76,164
WL 7
DM 238,56,88,3072
TR 70,245,61440
v_lshl_add_u32 v77, v78, 5, v77
Mvaa 168,80,160
TR 72,246,49152
v_lshrrev_b32 v78, 1, v79
v_lshl_add_u32 v77, v78, 4, v77
Mvaa 168,84,164
s_lshl_b32 s95, s79, 2
s_add_u32 s95, s95, s19
s_mul_i32 s95, s95, 36864
TR 74,246,53248
Mvaa 172,80,176
s_add_u32 s31, s95, 18432
s_lshl_b32 s96, s80, 8
s_add_u32 s95, s95, s96
TR 76,246,57344
Mvaa 172,84,180
WL 7
s_add_u32 s25, s95, 192
s_add_u32 s32, s95, 18624
TR 78,246,61440
Mvaa 180,88,176
s_lshl_b32 s95, s35, 13
s_add_u32 s30, s95, 40198144
s_add_u32 s20, s95, 40189952
TR 80,247,49152
Mvaa 180,92,180
s_add_u32 s21, s95, 40184064
s_add_u32 s22, s95, 40178176
s_lshl_b32 s91, s35, 7
TR 82,247,53248
Mvaa 176,88,160
s_add_u32 s92, s91, 384
s_add_u32 m0, s86, 4096
TR 84,247,57344
Mvaa 176,92,164
WL 7
buffer_load_dwordx4 v234, s[72:75], s89 offen offset:0 sc1 lds
TR 86,247,61440
Mvaa 184,64,160
s_add_u32 s90, s34, 16879616
GL 192,234,72,90,0
TR 88,248,49152
Mvaa 184,68,164
TR 90,248,53248
Mvaa 188,64,176
GL 196,234,72,90,1024
TR 92,248,57344
Mvaa 188,68,180
WL 7
TR 94,248,61440
Mvaa 212,72,176
TR 64,240,12288
Mvaa 212,76,180
GL 200,234,72,90,2048
TR 66,240,12800
Mvaa 208,72,160
TR 68,240,13312
Mvaa 208,76,164
WL 7
TR 70,240,13824
Mvaa 128,80,160
TR 72,220,49152
Mvaa 128,84,164
GL 204,234,72,90,3072
TR 74,220,53248
Mvaa 132,80,176
TR 76,220,57344
Mvaa 132,84,180
WL 7
TR 78,220,61440
Mvaa 140,88,176
s_add_u32 s90, s34, 17010688
GL 208,234,72,90,0
TR 80,221,49152
Mvaa 140,92,180
TR 82,221,53248
Mvaa 136,88,160
TR 84,221,57344
Mvaa 136,92,164
WL 7
TR 86,221,61440
Mvaa 148,64,160
GL 212,234,72,90,1024
TR 88,222,49152
Mvaa 148,68,164
TR 90,222,53248
Mvaa 144,64,176
TR 92,222,57344
Mvaa 144,68,180
WL 7
GL 216,234,72,90,2048
TR 94,222,61440
Mvaa 156,72,184
TR 64,223,49152
Mvaa 156,76,188
TR 66,223,53248
Mvaa 152,72,168
TR 68,223,57344
Mvaa 152,76,172
WL 7
GL 220,234,72,90,3072
TR 70,223,61440
Mvaa 160,80,168
TR 72,224,49152
Mvaa 160,84,172
TR 74,224,53248
Mvaa 164,80,184
s_add_u32 s87, s28, 3833856
s_add_u32 s88, s28, 3907584
s_add_u32 s89, s29, 253952
TR 76,224,57344
Mvaa 164,84,188
WL 7
s_add_u32 m0, s84, 32768
TR 78,224,61440
Mvaa 172,88,184
DM 235,56,87,0
TR 80,225,49152
Mvaa 172,92,188
TR 82,225,53248
Mvaa 168,88,168
)"
R"(DM 236,56,87,1024
TR 84,225,57344
Mvaa 168,92,172
WL 7
TR 86,225,61440
Mvaa 176,64,168
TR 88,226,49152
Mvaa 176,68,172
DM 237,56,87,2048
TR 90,226,53248
Mvaa 180,64,184
TR 92,226,57344
Mvaa 180,68,188
WL 7
TR 94,226,61440
Mvaa 188,72,184
TR 64,227,49152
Mvaa 188,76,188
DM 238,56,87,3072
TR 66,227,53248
Mvaa 184,72,168
TR 68,227,57344
Mvaa 184,76,172
WL 7
TR 70,227,61440
Mvaa 208,80,168
s_add_u32 m0, s85, 32768
TR 72,240,14336
Mvaa 208,84,172
DM 235,56,88,0
TR 74,240,14848
Mvaa 212,80,184
TR 76,240,15360
Mvaa 212,84,188
WL 7
TR 78,240,15872
Mvaa 132,88,184
s_waitcnt vmcnt(30) lgkmcnt(0)
s_barrier
DM 236,56,88,1024
TR 80,241,0
Mvaa 132,92,188
TR 82,241,4096
Mvaa 128,88,168
TR 84,241,8192
Mvaa 128,92,172
DM 237,56,88,2048
TR 86,241,12288
Mvaa 136,64,168
TR 88,242,0
Mvaa 136,68,172
TR 90,242,4096
Mvaa 140,64,184
TR 92,242,8192
Mvaa 140,68,188
DM 238,56,88,3072
TR 94,242,12288
Mvaa 144,72,184
TR 64,243,0
Mvaa 144,76,188
TR 66,243,4096
Mvaa 148,72,168
s_add_u32 m0, s86, 8192
TR 68,243,8192
Mvaa 148,76,172
s_waitcnt vmcnt(25) lgkmcnt(7)
buffer_load_dwordx4 v234, s[72:75], s89 offen offset:0 sc1 lds
TR 70,243,12288
v_mfma_f32_16x16x32_bf16 v[24:27], a[80:83], a[96:99], 0
TR 72,244,0
Mvaa 24,84,100
TR 74,244,4096
v_mfma_f32_16x16x32_bf16 v[28:31], a[80:83], a[112:115], 0
s_add_u32 s90, s34, 16883712
GL 224,234,72,90,0
TR 76,244,8192
Mvaa 28,84,116
WL 7
TR 78,244,12288
v_mfma_f32_16x16x32_bf16 v[36:39], a[88:91], a[112:115], 0
TR 80,245,0
CV 148,148,149
Mvaa 36,92,116
GL 228,234,72,90,1024
TR 82,245,4096
CV 149,150,151
v_mfma_f32_16x16x32_bf16 v[32:35], a[88:91], a[96:99], 0
TR 84,245,8192
CV 150,144,145
Mvaa 32,92,100
WL 7
TR 86,245,12288
v_mfma_f32_16x16x32_bf16 v[40:43], a[64:67], a[96:99], 0
TR 88,246,0
CV 151,146,147
Mvaa 40,68,100
GL 232,234,72,90,2048
TR 90,246,4096
CV 152,152,153
v_mfma_f32_16x16x32_bf16 v[44:47], a[64:67], a[112:115], 0
TR 92,246,8192
CV 153,154,155
Mvaa 44,68,116
WL 7
TR 94,246,12288
v_mfma_f32_16x16x32_bf16 v[52:55], a[72:75], a[112:115], 0
GL 236,234,72,90,3072
TR 64,247,0
CV 154,156,157
Mvaa 52,76,116
TR 66,247,4096
CV 155,158,159
v_mfma_f32_16x16x32_bf16 v[48:51], a[72:75], a[96:99], 0
TR 68,247,8192
CV 160,160,161
Mvaa 48,76,100
WL 7
TR 70,247,12288
v_mfma_f32_16x16x32_bf16 v[56:59], a[80:83], a[96:99], 0
s_add_u32 s90, s34, 17014784
GL 240,234,72,90,0
TR 72,248,0
CV 161,162,163
Mvaa 56,84,100
TR 74,248,4096
CV 162,164,165
v_mfma_f32_16x16x32_bf16 v[60:63], a[80:83], a[112:115], 0
TR 76,248,8192
CV 163,166,167
Mvaa 60,84,116
WL 7
GL 244,234,72,90,1024
TR 78,248,12288
v_mfma_f32_16x16x32_bf16 v[68:71], a[88:91], a[112:115], 0
TR 80,240,0
CV 168,168,169
Mvaa 68,92,116
TR 82,240,512
CV 169,170,171
v_mfma_f32_16x16x32_bf16 v[64:67], a[88:91], a[96:99], 0
TR 84,240,1024
CV 170,172,173
Mvaa 64,92,100
WL 7
GL 248,234,72,90,2048
TR 86,240,1536
v_mfma_f32_16x16x32_bf16 v[0:3], a[64:67], a[96:99], 0
TR 88,220,0
CV 171,174,175
Mvaa 0,68,100
TR 90,220,4096
CV 176,176,177
v_mfma_f32_16x16x32_bf16 v[4:7], a[64:67], a[112:115], 0
GL 252,234,72,90,3072
TR 92,220,8192
CV 177,178,179
Mvaa 4,68,116
WL 7
TR 94,220,12288
v_mfma_f32_16x16x32_bf16 v[12:15], a[72:75], a[112:115], 0
TR 64,221,0
CV 178,180,181
Mvaa 12,76,116
TR 66,221,4096
CV 179,182,183
v_mfma_f32_16x16x32_bf16 v[8:11], a[72:75], a[96:99], 0
TR 68,221,8192
CV 184,184,185
Mvaa 8,76,100
WL 7
TR 70,221,12288
v_mfma_f32_16x16x32_bf16 v[20:23], a[80:83], a[96:99], 0
TR 72,222,0
CV 185,186,187
Mvaa 20,84,100
TR 74,222,4096
CV 186,188,189
v_mfma_f32_16x16x32_bf16 v[16:19], a[80:83], a[112:115], 0
TR 76,222,8192
CV 187,190,191
Mvaa 16,84,116
WL 7
TR 78,222,12288
Mvaa 28,88,120
TR 80,223,0
CV 208,208,209
Mvaa 28,92,124
TR 82,223,4096
CV 209,210,211
Mvaa 24,88,104
TR 84,223,8192
CV 210,212,213
Mvaa 24,92,108
WL 7
TR 86,223,12288
Mvaa 32,64,104
TR 88,224,0
CV 211,214,215
Mvaa 32,68,108
s_add_u32 s16, s30, 2048
buffer_store_dwordx2 v[148:149], v72, s[72:75], s16 offen sc1
TR 90,224,4096
Mvaa 36,64,120
s_add_u32 s16, s30, 4352
buffer_store_dwordx2 v[148:149], v73, s[72:75], s16 offen sc1
TR 92,224,8192
Mvaa 36,68,124
WL 7
s_add_u32 s16, s30, 6656
buffer_store_dwordx2 v[150:151], v72, s[72:75], s16 offen sc1
TR 94,224,12288
Mvaa 44,72,120
s_add_u32 s16, s30, 0
buffer_store_dwordx4 v[152:155], v234, s[72:75], s16 offen sc1
TR 64,225,0
Mvaa 44,76,124
TR 66,225,4096
Mvaa 40,72,104
TR 68,225,8192
Mvaa 40,76,108
WL 7
TR 70,225,12288
Mvaa 48,80,104
TR 72,226,0
Mvaa 48,84,108
TR 74,226,4096
Mvaa 52,80,120
s_add_u32 s16, s30, 1024
buffer_store_dwordx4 v[160:163], v234, s[72:75], s16 offen sc1
TR 76,226,8192
Mvaa 52,84,124
WL 7
TR 78,226,12288
Mvaa 60,88,120
TR 80,227,0
Mvaa 60,92,124
TR 82,227,4096
Mvaa 56,88,104
TR 84,227,8192
Mvaa 56,92,108
WL 7
TR 86,227,12288
Mvaa 64,64,104
TR 88,240,2048
Mvaa 64,68,108
TR 90,240,2560
Mvaa 68,64,120
TR 92,240,3072
Mvaa 68,68,124
WL 7
TR 94,240,3584
Mvaa 4,72,120
s_waitcnt vmcnt(30) lgkmcnt(0)
s_barrier
TR 64,241,16384
Mvaa 4,76,124
s_add_u32 s98, s36, 265728
TR 66,241,20480
v_and_b32 v249, 3, v255
Mvaa 0,72,104
TR 68,241,24576
v_lshlrev_b32 v249, 7, v249
Mvaa 0,76,108
buffer_load_dwordx2 v[250:251], v249, s[72:75], s98 offen sc1
TR 70,241,28672
Mvaa 8,80,104
s_add_u32 s87, s28, 3981312
s_add_u32 s88, s28, 4055040
s_add_u32 s89, s29, 258048
TR 72,242,16384
Mvaa 8,84,108
Lv3stub3_fused_block:
s_cbranch_execz Lt2x_fused_block
s_add_u32 m0, s84, 49152
TR 74,242,20480
Mvaa 12,80,120
DM 235,56,87,0
TR 76,242,24576
Mvaa 12,84,124
TR 78,242,28672
Mvaa 16,88,120
DM 236,56,87,1024
TR 80,243,16384
Mvaa 16,92,124
TR 82,243,20480
Mvaa 20,88,104
DM 237,56,87,2048
TR 84,243,24576
Mvaa 20,92,108
s_waitcnt vmcnt(26) lgkmcnt(7)
TR 86,243,28672
Mvaa 24,64,192
TR 88,244,16384
Mvaa 24,68,196
DM 238,56,87,3072
TR 90,244,20480
Mvaa 28,64,208
TR 92,244,24576
Mvaa 28,68,212
WL 7
TR 94,244,28672
Mvaa 36,72,208
TR 64,245,16384
Mvaa 36,76,212
s_add_u32 m0, s85, 49152
TR 66,245,20480
Mvaa 32,72,192
DM 235,56,88,0
TR 68,245,24576
Mvaa 32,76,196
WL 7
TR 70,245,28672
Mvaa 40,80,192
DM 236,56,88,1024
TR 72,246,16384
Mvaa 40,84,196
TR 74,246,20480
Mvaa 44,80,208
TR 76,246,24576
Mvaa 44,84,212
WL 7
TR 78,246,28672
Mvaa 52,88,208
DM 237,56,88,2048
TR 80,247,16384
Mvaa 52,92,212
TR 82,247,20480
Mvaa 48,88,192
TR 84,247,24576
Mvaa 48,92,196
WL 7
DM 238,56,88,3072
TR 86,247,28672
Mvaa 56,64,192
TR 88,248,16384
Mvaa 56,68,196
TR 90,248,20480
Mvaa 60,64,208
TR 92,248,24576
Mvaa 60,68,212
WL 7
s_add_u32 m0, s86, 12288
TR 94,248,28672
Mvaa 68,72,208
buffer_load_dwordx4 v234, s[72:75], s89 offen offset:0 sc1 lds
TR 64,240,4096
Mvaa 68,76,212
TR 66,240,4608
Mvaa 64,72,192
s_add_u32 s90, s34, 16887808
GL 128,234,72,90,0
TR 68,240,5120
Mvaa 64,76,196
WL 7
TR 70,240,5632
Mvaa 0,80,192
TR 72,220,16384
Mvaa 0,84,196
TR 74,220,20480
Mvaa 4,80,208
GL 132,234,72,90,1024
TR 76,220,24576
Mvaa 4,84,212
WL 7
TR 78,220,28672
Mvaa 12,88,208
TR 80,221,16384
Mvaa 12,92,212
GL 136,234,72,90,2048
TR 82,221,20480
Mvaa 8,88,192
TR 84,221,24576
Mvaa 8,92,196
WL 7
TR 86,221,28672
Mvaa 20,64,192
TR 88,222,16384
Mvaa 20,68,196
GL 140,234,72,90,3072
TR 90,222,20480
Mvaa 16,64,208
TR 92,222,24576
Mvaa 16,68,212
WL 7
TR 94,222,28672
Mvaa 28,72,216
s_add_u32 s90, s34, 17018880
GL 144,234,72,90,0
TR 64,223,16384
Mvaa 28,76,220
TR 66,223,20480
Mvaa 24,72,200
TR 68,223,24576
Mvaa 24,76,204
WL 7
TR 70,223,28672
Mvaa 32,80,200
GL 148,234,72,90,1024
TR 72,224,16384
Mvaa 32,84,204
)"
R"(TR 74,224,20480
Mvaa 36,80,216
TR 76,224,24576
Mvaa 36,84,220
WL 7
GL 152,234,72,90,2048
TR 78,224,28672
Mvaa 44,88,216
TR 80,225,16384
Mvaa 44,92,220
TR 82,225,20480
Mvaa 40,88,200
TR 84,225,24576
Mvaa 40,92,204
WL 7
GL 156,234,72,90,3072
TR 86,225,28672
Mvaa 48,64,200
TR 88,226,16384
Mvaa 48,68,204
TR 90,226,20480
Mvaa 52,64,216
s_add_u32 s16, s30, 2304
buffer_store_dwordx4 v[168:171], v234, s[72:75], s16 offen sc1
TR 92,226,24576
Mvaa 52,68,220
WL 7
TR 94,226,28672
Mvaa 60,72,216
TR 64,227,16384
Mvaa 60,76,220
TR 66,227,20480
Mvaa 56,72,200
TR 68,227,24576
Mvaa 56,76,204
WL 7
TR 70,227,28672
Mvaa 64,80,200
TR 72,240,6144
Mvaa 64,84,204
TR 74,240,6656
Mvaa 68,80,216
TR 76,240,7168
Mvaa 68,84,220
WL 7
TR 78,240,7680
Mvaa 4,88,216
s_waitcnt vmcnt(32) lgkmcnt(0)
s_barrier
TR 80,241,32768
Mvaa 4,92,220
s_add_u32 s16, s30, 3328
buffer_store_dwordx4 v[176:179], v234, s[72:75], s16 offen sc1
TR 82,241,36864
Mvaa 0,88,200
TR 84,241,40960
Mvaa 0,92,204
TR 86,241,45056
Mvaa 8,64,200
TR 88,242,32768
Mvaa 8,68,204
TR 90,242,36864
Mvaa 12,64,216
TR 92,242,40960
Mvaa 12,68,220
TR 94,242,45056
Mvaa 16,72,216
TR 64,243,32768
Mvaa 16,76,220
TR 66,243,36864
Mvaa 20,72,200
TR 68,243,40960
Mvaa 20,76,204
s_waitcnt vmcnt(25) lgkmcnt(7)
TR 70,243,45056
Mvaa 24,80,224
s_add_u32 s16, s30, 4608
buffer_store_dwordx4 v[184:187], v234, s[72:75], s16 offen sc1
TR 72,244,32768
Mvaa 24,84,228
TR 74,244,36864
Mvaa 28,80,240
TR 76,244,40960
Mvaa 28,84,244
WL 7
TR 78,244,45056
Mvaa 36,88,240
TR 80,245,32768
Mvaa 36,92,244
TR 82,245,36864
Mvaa 32,88,224
TR 84,245,40960
Mvaa 32,92,228
WL 7
TR 86,245,45056
Mvaa 40,64,224
TR 88,246,32768
Mvaa 40,68,228
TR 90,246,36864
Mvaa 44,64,240
TR 92,246,40960
Mvaa 44,68,244
WL 7
s_add_u32 s16, s30, 5632
buffer_store_dwordx4 v[208:211], v234, s[72:75], s16 offen sc1
TR 94,246,45056
Mvaa 52,72,240
TR 64,247,32768
Mvaa 52,76,244
TR 66,247,36864
Mvaa 48,72,224
TR 68,247,40960
Mvaa 48,76,228
WL 7
TR 70,247,45056
Mvaa 56,80,224
TR 72,248,32768
Mvaa 56,84,228
TR 74,248,36864
Mvaa 60,80,240
TR 76,248,40960
Mvaa 60,84,244
WL 7
TR 78,248,45056
Mvaa 68,88,240
TR 80,240,8192
Mvaa 68,92,244
TR 82,240,8704
Mvaa 64,88,224
s_add_u32 s87, s28, 4128768
s_add_u32 s88, s28, 4202496
s_add_u32 s89, s29, 262144
TR 84,240,9216
Mvaa 64,92,228
WL 7
s_add_u32 m0, s84, 0
TR 86,240,9728
Mvaa 0,64,224
DM 235,56,87,0
TR 88,220,32768
Mvaa 0,68,228
TR 90,220,36864
Mvaa 4,64,240
DM 236,56,87,1024
TR 92,220,40960
Mvaa 4,68,244
WL 7
TR 94,220,45056
Mvaa 12,72,240
TR 64,221,32768
Mvaa 12,76,244
DM 237,56,87,2048
TR 66,221,36864
Mvaa 8,72,224
TR 68,221,40960
Mvaa 8,76,228
WL 7
TR 70,221,45056
Mvaa 20,80,224
TR 72,222,32768
Mvaa 20,84,228
DM 238,56,87,3072
TR 74,222,36864
Mvaa 16,80,240
TR 76,222,40960
Mvaa 16,84,244
WL 7
TR 78,222,45056
Mvaa 28,88,248
s_add_u32 m0, s85, 0
TR 80,223,32768
Mvaa 28,92,252
DM 235,56,88,0
TR 82,223,36864
Mvaa 24,88,232
TR 84,223,40960
Mvaa 24,92,236
WL 7
TR 86,223,45056
Mvaa 32,64,232
DM 236,56,88,1024
TR 88,224,32768
Mvaa 32,68,236
TR 90,224,36864
Mvaa 36,64,248
TR 92,224,40960
Mvaa 36,68,252
WL 7
DM 237,56,88,2048
TR 94,224,45056
Mvaa 44,72,248
TR 64,225,32768
Mvaa 44,76,252
TR 66,225,36864
Mvaa 40,72,232
TR 68,225,40960
Mvaa 40,76,236
WL 7
DM 238,56,88,3072
TR 70,225,45056
Mvaa 48,80,232
TR 72,226,32768
Mvaa 48,84,236
TR 74,226,36864
Mvaa 52,80,248
s_add_u32 m0, s86, 0
TR 76,226,40960
Mvaa 52,84,252
WL 7
buffer_load_dwordx4 v234, s[72:75], s89 offen offset:0 sc1 lds
TR 78,226,45056
Mvaa 60,88,248
TR 80,227,32768
Mvaa 60,92,252
TR 82,227,36864
Mvaa 56,88,232
s_waitcnt vmcnt(30)
TR 84,227,40960
v_cmp_ne_u64 vcc, s[82:83], v[250:251]
s_nop 3
s_cbranch_vccnz Lslow10_fused_block
Lslowb10_fused_block:
Mvaa 56,92,236
WL 7
s_add_u32 s90, s34, 16891904
GL 160,234,72,90,0
TR 86,227,45056
Mvaa 64,64,232
TR 88,240,10240
Mvaa 64,68,236
GL 164,234,72,90,1024
TR 90,240,10752
Mvaa 68,64,248
TR 92,240,11264
Mvaa 68,68,252
WL 7
TR 94,240,11776
Mvaa 4,72,248
s_waitcnt vmcnt(23) lgkmcnt(0)
s_barrier
TR 64,241,49152
Mvaa 4,76,252
GL 168,234,72,90,2048
TR 66,241,53248
Mvaa 0,72,232
TR 68,241,57344
Mvaa 0,76,236
TR 70,241,61440
Mvaa 8,80,232
GL 172,234,72,90,3072
TR 72,242,49152
Mvaa 8,84,236
TR 74,242,53248
Mvaa 12,80,248
TR 76,242,57344
Mvaa 12,84,252
TR 78,242,61440
Mvaa 16,88,248
s_add_u32 s90, s34, 17022976
GL 176,234,72,90,0
TR 80,243,49152
Mvaa 16,92,252
TR 82,243,53248
Mvaa 20,88,232
TR 84,243,57344
Mvaa 20,92,236
s_waitcnt vmcnt(18) lgkmcnt(7)
GL 180,234,72,90,1024
TR 86,243,61440
Mvaa 24,64,128
TR 88,244,49152
Mvaa 24,68,132
TR 90,244,53248
Mvaa 28,64,144
TR 92,244,57344
Mvaa 28,68,148
WL 7
GL 184,234,72,90,2048
TR 94,244,61440
Mvaa 36,72,144
TR 64,245,49152
Mvaa 36,76,148
TR 66,245,53248
Mvaa 32,72,128
GL 188,234,72,90,3072
TR 68,245,57344
Mvaa 32,76,132
WL 7
TR 70,245,61440
Mvaa 40,80,128
TR 72,246,49152
Mvaa 40,84,132
TR 74,246,53248
Mvaa 44,80,144
s_waitcnt vmcnt(17)
s_add_u32 s98, s92, 393216
TR 76,246,57344
v_mov_b32 v250, s82
Mvaa 44,84,148
WL 7
TR 78,246,61440
v_mov_b32 v251, s83
Mvaa 52,88,144
TR 80,247,49152
v_lshlrev_b32 v249, 3, v255
s_mov_b64 exec, 0xffff
buffer_store_dwordx2 v[250:251], v249, s[72:75], s98 offen sc1
s_mov_b64 exec, -1
Mvaa 52,92,148
s_add_u32 s87, s28, 4276224
s_add_u32 s88, s28, 4349952
s_add_u32 s89, s29, 266240
TR 82,247,53248
Mvaa 48,88,128
s_add_u32 m0, s84, 16384
TR 84,247,57344
Mvaa 48,92,132
WL 7
DM 235,56,87,0
TR 86,247,61440
Mvaa 56,64,128
TR 88,248,49152
Mvaa 56,68,132
DM 236,56,87,1024
TR 90,248,53248
Mvaa 60,64,144
TR 92,248,57344
Mvaa 60,68,148
WL 7
DM 237,56,87,2048
TR 94,248,61440
Mvaa 68,72,144
TR 64,240,12288
Mvaa 68,76,148
DM 238,56,87,3072
TR 66,240,12800
Mvaa 64,72,128
TR 68,240,13312
Mvaa 64,76,132
WL 7
TR 70,240,13824
Mvaa 0,80,128
s_add_u32 m0, s85, 16384
TR 72,220,49152
Mvaa 0,84,132
DM 235,56,88,0
TR 74,220,53248
Mvaa 4,80,144
TR 76,220,57344
Mvaa 4,84,148
WL 7
DM 236,56,88,1024
TR 78,220,61440
Mvaa 12,88,144
TR 80,221,49152
Mvaa 12,92,148
TR 82,221,53248
Mvaa 8,88,128
TR 84,221,57344
Mvaa 8,92,132
WL 7
DM 237,56,88,2048
TR 86,221,61440
Mvaa 20,64,128
TR 88,222,49152
Mvaa 20,68,132
TR 90,222,53248
Mvaa 16,64,144
DM 238,56,88,3072
TR 92,222,57344
Mvaa 16,68,148
WL 7
TR 94,222,61440
Mvaa 28,72,152
TR 64,223,49152
Mvaa 28,76,156
TR 66,223,53248
Mvaa 24,72,136
s_add_u32 m0, s86, 4096
TR 68,223,57344
Mvaa 24,76,140
WL 7
buffer_load_dwordx4 v234, s[72:75], s89 offen offset:0 sc1 lds
TR 70,223,61440
Mvaa 32,80,136
TR 72,224,49152
Mvaa 32,84,140
s_add_u32 s90, s34, 16896000
GL 96,234,72,90,0
TR 74,224,53248
Mvaa 36,80,152
TR 76,224,57344
Mvaa 36,84,156
WL 7
TR 78,224,61440
Mvaa 44,88,152
TR 80,225,49152
Mvaa 44,92,156
GL 100,234,72,90,1024
TR 82,225,53248
)"
R"(Mvaa 40,88,136
TR 84,225,57344
Mvaa 40,92,140
WL 7
TR 86,225,61440
Mvaa 48,64,136
GL 104,234,72,90,2048
TR 88,226,49152
Mvaa 48,68,140
TR 90,226,53248
Mvaa 52,64,152
TR 92,226,57344
Mvaa 52,68,156
Lv3stub4_fused_block:
s_cbranch_execz Lt2x_fused_block
WL 7
TR 94,226,61440
Mvaa 60,72,152
GL 108,234,72,90,3072
TR 64,227,49152
Mvaa 60,76,156
TR 66,227,53248
Mvaa 56,72,136
TR 68,227,57344
Mvaa 56,76,140
WL 7
s_add_u32 s90, s34, 17027072
GL 112,234,72,90,0
TR 70,227,61440
Mvaa 64,80,136
TR 72,240,14336
Mvaa 64,84,140
TR 74,240,14848
Mvaa 68,80,152
TR 76,240,15360
Mvaa 68,84,156
WL 7
GL 116,234,72,90,1024
TR 78,240,15872
Mvaa 4,88,152
s_waitcnt vmcnt(24) lgkmcnt(0)
s_barrier
TR 80,241,0
Mvaa 4,92,156
TR 82,241,4096
Mvaa 0,88,136
GL 120,234,72,90,2048
TR 84,241,8192
Mvaa 0,92,140
TR 86,241,12288
Mvaa 8,64,136
TR 88,242,0
Mvaa 8,68,140
TR 90,242,4096
Mvaa 12,64,152
GL 124,234,72,90,3072
TR 92,242,8192
Mvaa 12,68,156
TR 94,242,12288
Mvaa 16,72,152
TR 64,243,0
Mvaa 16,76,156
s_add_u32 s87, s28, 4423680
s_add_u32 s88, s28, 4497408
s_add_u32 s89, s29, 270336
TR 66,243,4096
Mvaa 20,72,136
s_add_u32 m0, s84, 32768
TR 68,243,8192
Mvaa 20,76,140
s_waitcnt vmcnt(18) lgkmcnt(7)
DM 235,56,87,0
TR 70,243,12288
Mvaa 24,80,160
TR 72,244,0
Mvaa 24,84,164
DM 236,56,87,1024
TR 74,244,4096
Mvaa 28,80,176
TR 76,244,8192
Mvaa 28,84,180
WL 7
TR 78,244,12288
Mvaa 36,88,176
DM 237,56,87,2048
TR 80,245,0
Mvaa 36,92,180
TR 82,245,4096
Mvaa 32,88,160
TR 84,245,8192
Mvaa 32,92,164
WL 7
TR 86,245,12288
Mvaa 40,64,160
DM 238,56,87,3072
TR 88,246,0
Mvaa 40,68,164
TR 90,246,4096
Mvaa 44,64,176
TR 92,246,8192
Mvaa 44,68,180
WL 7
s_add_u32 m0, s85, 32768
TR 94,246,12288
Mvaa 52,72,176
DM 235,56,88,0
TR 64,247,0
Mvaa 52,76,180
TR 66,247,4096
Mvaa 48,72,160
TR 68,247,8192
Mvaa 48,76,164
WL 7
DM 236,56,88,1024
TR 70,247,12288
Mvaa 56,80,160
TR 72,248,0
Mvaa 56,84,164
TR 74,248,4096
Mvaa 60,80,176
DM 237,56,88,2048
TR 76,248,8192
Mvaa 60,84,180
WL 7
TR 78,248,12288
Mvaa 68,88,176
TR 80,240,0
Mvaa 68,92,180
TR 82,240,512
Mvaa 64,88,160
DM 238,56,88,3072
TR 84,240,1024
Mvaa 64,92,164
WL 7
TR 86,240,1536
Mvaa 0,64,160
TR 88,220,0
Mvaa 0,68,164
s_add_u32 m0, s86, 8192
TR 90,220,4096
Mvaa 4,64,176
buffer_load_dwordx4 v234, s[72:75], s89 offen offset:0 sc1 lds
TR 92,220,8192
Mvaa 4,68,180
WL 7
TR 94,220,12288
Mvaa 12,72,176
TR 64,221,0
Mvaa 12,76,180
s_add_u32 s90, s34, 16900096
GL 192,234,72,90,0
TR 66,221,4096
Mvaa 8,72,160
TR 68,221,8192
Mvaa 8,76,164
WL 7
TR 70,221,12288
Mvaa 20,80,160
GL 196,234,72,90,1024
TR 72,222,0
Mvaa 20,84,164
TR 74,222,4096
Mvaa 16,80,176
TR 76,222,8192
Mvaa 16,84,180
WL 7
TR 78,222,12288
Mvaa 28,88,184
GL 200,234,72,90,2048
TR 80,223,0
Mvaa 28,92,188
TR 82,223,4096
Mvaa 24,88,168
TR 84,223,8192
Mvaa 24,92,172
WL 7
GL 204,234,72,90,3072
TR 86,223,12288
Mvaa 32,64,168
TR 88,224,0
Mvaa 32,68,172
TR 90,224,4096
Mvaa 36,64,184
TR 92,224,8192
Mvaa 36,68,188
WL 7
s_add_u32 s90, s34, 17031168
GL 208,234,72,90,0
TR 94,224,12288
Mvaa 44,72,184
TR 64,225,0
Mvaa 44,76,188
TR 66,225,4096
Mvaa 40,72,168
GL 212,234,72,90,1024
TR 68,225,8192
Mvaa 40,76,172
WL 7
TR 70,225,12288
Mvaa 48,80,168
TR 72,226,0
Mvaa 48,84,172
TR 74,226,4096
Mvaa 52,80,184
GL 216,234,72,90,2048
TR 76,226,8192
Mvaa 52,84,188
WL 7
TR 78,226,12288
Mvaa 60,88,184
TR 80,227,0
Mvaa 60,92,188
GL 220,234,72,90,3072
TR 82,227,4096
Mvaa 56,88,168
TR 84,227,8192
Mvaa 56,92,172
WL 7
TR 86,227,12288
Mvaa 64,64,168
TR 88,240,2048
Mvaa 64,68,172
s_add_u32 s87, s28, 4571136
s_add_u32 s88, s28, 4644864
s_add_u32 s89, s29, 274432
TR 90,240,2560
Mvaa 68,64,184
s_add_u32 m0, s84, 49152
TR 92,240,3072
Mvaa 68,68,188
WL 7
DM 235,56,87,0
TR 94,240,3584
Mvaa 4,72,184
s_waitcnt vmcnt(26) lgkmcnt(0)
s_barrier
TR 64,241,16384
Mvaa 4,76,188
DM 236,56,87,1024
TR 66,241,20480
Mvaa 0,72,168
TR 68,241,24576
Mvaa 0,76,172
TR 70,241,28672
Mvaa 8,80,168
DM 237,56,87,2048
TR 72,242,16384
Mvaa 8,84,172
TR 74,242,20480
Mvaa 12,80,184
TR 76,242,24576
Mvaa 12,84,188
DM 238,56,87,3072
TR 78,242,28672
Mvaa 16,88,184
TR 80,243,16384
Mvaa 16,92,188
TR 82,243,20480
Mvaa 20,88,168
s_add_u32 m0, s85, 49152
TR 84,243,24576
Mvaa 20,92,172
s_waitcnt vmcnt(21) lgkmcnt(7)
DM 235,56,88,0
TR 86,243,28672
Mvaa 24,64,96
TR 88,244,16384
Mvaa 24,68,100
DM 236,56,88,1024
TR 90,244,20480
Mvaa 28,64,112
TR 92,244,24576
Mvaa 28,68,116
WL 7
TR 94,244,28672
Mvaa 36,72,112
DM 237,56,88,2048
TR 64,245,16384
Mvaa 36,76,116
TR 66,245,20480
Mvaa 32,72,96
TR 68,245,24576
Mvaa 32,76,100
WL 7
DM 238,56,88,3072
TR 70,245,28672
Mvaa 40,80,96
TR 72,246,16384
Mvaa 40,84,100
TR 74,246,20480
Mvaa 44,80,112
s_add_u32 m0, s86, 12288
TR 76,246,24576
Mvaa 44,84,116
WL 7
buffer_load_dwordx4 v234, s[72:75], s89 offen offset:0 sc1 lds
TR 78,246,28672
Mvaa 52,88,112
TR 80,247,16384
Mvaa 52,92,116
s_add_u32 s90, s34, 16904192
GL 224,234,72,90,0
TR 82,247,20480
Mvaa 48,88,96
TR 84,247,24576
Mvaa 48,92,100
WL 7
TR 86,247,28672
Mvaa 56,64,96
GL 228,234,72,90,1024
TR 88,248,16384
Mvaa 56,68,100
TR 90,248,20480
Mvaa 60,64,112
TR 92,248,24576
Mvaa 60,68,116
WL 7
GL 232,234,72,90,2048
TR 94,248,28672
Mvaa 68,72,112
TR 64,240,4096
Mvaa 68,76,116
TR 66,240,4608
Mvaa 64,72,96
GL 236,234,72,90,3072
TR 68,240,5120
Mvaa 64,76,100
WL 7
TR 70,240,5632
Mvaa 0,80,96
TR 72,220,16384
Mvaa 0,84,100
s_add_u32 s90, s34, 17035264
GL 240,234,72,90,0
TR 74,220,20480
Mvaa 4,80,112
TR 76,220,24576
Mvaa 4,84,116
WL 7
TR 78,220,28672
Mvaa 12,88,112
GL 244,234,72,90,1024
TR 80,221,16384
Mvaa 12,92,116
TR 82,221,20480
Mvaa 8,88,96
TR 84,221,24576
Mvaa 8,92,100
WL 7
GL 248,234,72,90,2048
TR 86,221,28672
Mvaa 20,64,96
TR 88,222,16384
Mvaa 20,68,100
TR 90,222,20480
Mvaa 16,64,112
GL 252,234,72,90,3072
TR 92,222,24576
Mvaa 16,68,116
WL 7
TR 94,222,28672
Mvaa 28,72,120
TR 64,223,16384
Mvaa 28,76,124
TR 66,223,20480
Mvaa 24,72,104
TR 68,223,24576
Mvaa 24,76,108
WL 7
TR 70,223,28672
Mvaa 32,80,104
TR 72,224,16384
Mvaa 32,84,108
TR 74,224,20480
Mvaa 36,80,120
TR 76,224,24576
Mvaa 36,84,124
WL 7
TR 78,224,28672
Mvaa 44,88,120
s_add_u32 s98, s91, 393216
TR 80,225,16384
v_and_b32 v249, 3, v255
Mvaa 44,92,124
TR 82,225,20480
v_lshlrev_b32 v249, 7, v249
Mvaa 40,88,104
buffer_load_dwordx2 v[250:251], v249, s[72:75], s98 offen sc1
TR 84,225,24576
Mvaa 40,92,108
WL 7
TR 86,225,28672
Mvaa 48,64,104
buffer_load_dwordx2 v[80:81], v75, s[40:43], s25 offen offset:0
TR 88,226,16384
Mvaa 48,68,108
TR 90,226,20480
Mvaa 52,64,120
buffer_load_dwordx2 v[82:83], v75, s[40:43], s32 offen offset:0
TR 92,226,24576
)"
R"(Mvaa 52,68,124
WL 7
TR 94,226,28672
Mvaa 60,72,120
buffer_load_dwordx2 v[84:85], v75, s[40:43], s25 offen offset:32
TR 64,227,16384
Mvaa 60,76,124
TR 66,227,20480
Mvaa 56,72,104
buffer_load_dwordx2 v[86:87], v75, s[40:43], s32 offen offset:32
TR 68,227,24576
Mvaa 56,76,108
WL 7
TR 70,227,28672
Mvaa 64,80,104
buffer_load_dwordx2 v[88:89], v76, s[40:43], s31 offen
TR 72,240,6144
Mvaa 64,84,108
TR 74,240,6656
Mvaa 68,80,120
TR 76,240,7168
Mvaa 68,84,124
WL 7
TR 78,240,7680
Mvaa 4,88,120
s_waitcnt vmcnt(14) lgkmcnt(0)
s_barrier
TR 80,240,8192
Mvaa 4,92,124
TR 82,240,8704
Mvaa 0,88,104
TR 84,240,9216
Mvaa 0,92,108
TR 86,240,9728
Mvaa 8,64,104
TR 88,240,10240
Mvaa 8,68,108
TR 90,240,10752
Mvaa 12,64,120
TR 92,240,11264
Mvaa 12,68,124
TR 94,240,11776
Mvaa 16,72,120
TR 64,240,12288
Mvaa 16,76,124
TR 66,240,12800
Mvaa 20,72,104
TR 68,240,13312
Mvaa 20,76,108
WL 7
TR 70,240,13824
Mvaa 20,80,192
TR 72,240,14336
Mvaa 20,84,196
TR 74,240,14848
Mvaa 16,80,208
TR 76,240,15360
Mvaa 16,84,212
WL 7
TR 78,240,15872
Mvaa 16,88,216
TR 80,241,32768
Mvaa 16,92,220
TR 82,241,36864
Mvaa 20,88,200
TR 84,241,40960
Mvaa 20,92,204
s_waitcnt vmcnt(6) lgkmcnt(7)
TR 86,241,45056
Mvaa 20,64,224
TR 88,220,32768
Mvaa 20,68,228
TR 90,220,36864
Mvaa 16,64,240
s_waitcnt vmcnt(5)
TR 92,220,40960
v_cmp_ne_u64 vcc, s[82:83], v[250:251]
s_nop 3
s_cbranch_vccnz Lslow11_fused_block
Lslowb11_fused_block:
Mvaa 16,68,244
WL 7
buffer_load_dwordx4 v[152:155], v234, s[72:75], s20 offen offset:0 sc1
TR 94,220,45056
Mvaa 16,72,248
TR 64,241,49152
Mvaa 16,76,252
buffer_load_dwordx4 v[156:159], v234, s[72:75], s20 offen offset:1024 sc1
TR 66,241,53248
Mvaa 20,72,232
TR 68,241,57344
Mvaa 20,76,236
WL 7
buffer_load_dwordx2 v[160:161], v74, s[72:75], s20 offen offset:2048 sc1
TR 70,241,61440
Mvaa 24,80,192
TR 72,220,49152
Mvaa 24,84,196
buffer_load_dwordx4 v[162:165], v234, s[72:75], s21 offen offset:0 sc1
TR 74,220,53248
Mvaa 28,80,208
TR 76,220,57344
Mvaa 28,84,212
WL 7
buffer_load_dwordx4 v[166:169], v234, s[72:75], s21 offen offset:1024 sc1
TR 78,220,61440
CV 20,20,21
Mvaa 28,88,216
TR 80,242,32768
CV 21,22,23
CV 22,16,17
Mvaa 28,92,220
buffer_load_dwordx2 v[170:171], v74, s[72:75], s21 offen offset:2048 sc1
TR 82,242,36864
CV 23,18,19
Mvaa 24,88,200
TR 84,242,40960
Mvaa 24,92,204
WL 7
buffer_load_dwordx4 v[172:175], v234, s[72:75], s22 offen offset:0 sc1
TR 86,242,45056
Mvaa 24,64,224
TR 88,221,32768
Mvaa 24,68,228
buffer_load_dwordx4 v[176:179], v234, s[72:75], s22 offen offset:1024 sc1
TR 90,221,36864
Mvaa 28,64,240
TR 92,221,40960
Mvaa 28,68,244
WL 7
buffer_load_dwordx2 v[180:181], v74, s[72:75], s22 offen offset:2048 sc1
TR 94,221,45056
Mvaa 28,72,248
s_add_u32 s20, s20, 8388608
s_add_u32 s21, s21, 8388608
s_add_u32 s22, s22, 8388608
TR 64,242,49152
Mvaa 28,76,252
s_add_u32 s16, s30, 8390656
buffer_store_dwordx2 v[20:21], v72, s[72:75], s16 offen sc1
TR 66,242,53248
Mvaa 24,72,232
s_add_u32 s16, s30, 8392960
buffer_store_dwordx2 v[20:21], v73, s[72:75], s16 offen sc1
TR 68,242,57344
Mvaa 24,76,236
WL 7
s_add_u32 s16, s30, 8395264
buffer_store_dwordx2 v[22:23], v72, s[72:75], s16 offen sc1
TR 70,242,61440
Mvaa 32,80,192
TR 72,221,49152
Mvaa 32,84,196
TR 74,221,53248
Mvaa 36,80,208
Lv3stub5_fused_block:
s_cbranch_execz Lt2x_fused_block
TR 76,221,57344
Mvaa 36,84,212
WL 7
TR 78,221,61440
CV 24,24,25
Mvaa 36,88,216
TR 80,243,32768
CV 25,26,27
CV 26,28,29
Mvaa 36,92,220
TR 82,243,36864
CV 27,30,31
Mvaa 32,88,200
s_add_u32 s16, s30, 8388608
buffer_store_dwordx4 v[24:27], v234, s[72:75], s16 offen sc1
TR 84,243,40960
Mvaa 32,92,204
WL 7
TR 86,243,45056
Mvaa 32,64,224
TR 88,222,32768
Mvaa 32,68,228
TR 90,222,36864
Mvaa 36,64,240
TR 92,222,40960
Mvaa 36,68,244
WL 7
TR 94,222,45056
Mvaa 36,72,248
TR 64,243,49152
Mvaa 36,76,252
TR 66,243,53248
Mvaa 32,72,232
TR 68,243,57344
Mvaa 32,76,236
WL 7
TR 70,243,61440
Mvaa 40,80,192
TR 72,222,49152
Mvaa 40,84,196
TR 74,222,53248
Mvaa 44,80,208
TR 76,222,57344
Mvaa 44,84,212
WL 7
TR 78,222,61440
CV 32,32,33
Mvaa 44,88,216
TR 80,244,32768
CV 33,34,35
CV 34,36,37
Mvaa 44,92,220
TR 82,244,36864
CV 35,38,39
Mvaa 40,88,200
s_add_u32 s16, s30, 8389632
buffer_store_dwordx4 v[32:35], v234, s[72:75], s16 offen sc1
TR 84,244,40960
Mvaa 40,92,204
WL 7
TR 86,244,45056
Mvaa 40,64,224
TR 88,223,32768
Mvaa 40,68,228
TR 90,223,36864
Mvaa 44,64,240
TR 92,223,40960
Mvaa 44,68,244
WL 7
TR 94,223,45056
Mvaa 44,72,248
TR 64,244,49152
Mvaa 44,76,252
TR 66,244,53248
Mvaa 40,72,232
TR 68,244,57344
Mvaa 40,76,236
WL 7
TR 70,244,61440
Mvaa 48,80,192
TR 72,223,49152
Mvaa 48,84,196
TR 74,223,53248
Mvaa 52,80,208
TR 76,223,57344
Mvaa 52,84,212
WL 7
TR 78,223,61440
CV 40,40,41
Mvaa 52,88,216
TR 80,245,32768
CV 41,42,43
CV 42,44,45
Mvaa 52,92,220
TR 82,245,36864
CV 43,46,47
Mvaa 48,88,200
s_add_u32 s16, s30, 8390912
buffer_store_dwordx4 v[40:43], v234, s[72:75], s16 offen sc1
TR 84,245,40960
Mvaa 48,92,204
WL 7
TR 86,245,45056
Mvaa 48,64,224
TR 88,224,32768
Mvaa 48,68,228
TR 90,224,36864
Mvaa 52,64,240
TR 92,224,40960
Mvaa 52,68,244
WL 7
TR 94,224,45056
Mvaa 52,72,248
TR 64,245,49152
Mvaa 52,76,252
TR 66,245,53248
Mvaa 48,72,232
TR 68,245,57344
Mvaa 48,76,236
WL 7
TR 70,245,61440
Mvaa 56,80,192
TR 72,224,49152
Mvaa 56,84,196
TR 74,224,53248
Mvaa 60,80,208
TR 76,224,57344
Mvaa 60,84,212
WL 7
TR 78,224,61440
CV 48,48,49
Mvaa 60,88,216
TR 80,246,32768
CV 49,50,51
CV 50,52,53
Mvaa 60,92,220
TR 82,246,36864
CV 51,54,55
Mvaa 56,88,200
s_add_u32 s16, s30, 8391936
buffer_store_dwordx4 v[48:51], v234, s[72:75], s16 offen sc1
TR 84,246,40960
Mvaa 56,92,204
WL 7
TR 86,246,45056
Mvaa 56,64,224
TR 88,225,32768
Mvaa 56,68,228
TR 90,225,36864
Mvaa 60,64,240
TR 92,225,40960
Mvaa 60,68,244
WL 7
TR 94,225,45056
Mvaa 60,72,248
TR 64,246,49152
Mvaa 60,76,252
TR 66,246,53248
Mvaa 56,72,232
TR 68,246,57344
Mvaa 56,76,236
WL 7
TR 70,246,61440
Mvaa 64,80,192
TR 72,225,49152
Mvaa 64,84,196
TR 74,225,53248
Mvaa 68,80,208
TR 76,225,57344
Mvaa 68,84,212
WL 7
TR 78,225,61440
CV 56,56,57
Mvaa 68,88,216
TR 80,247,32768
CV 57,58,59
CV 58,60,61
Mvaa 68,92,220
TR 82,247,36864
CV 59,62,63
Mvaa 64,88,200
s_add_u32 s16, s30, 8393216
buffer_store_dwordx4 v[56:59], v234, s[72:75], s16 offen sc1
TR 84,247,40960
Mvaa 64,92,204
WL 7
TR 86,247,45056
Mvaa 64,64,224
TR 88,226,32768
Mvaa 64,68,228
TR 90,226,36864
Mvaa 68,64,240
TR 92,226,40960
Mvaa 68,68,244
WL 7
TR 94,226,45056
Mvaa 68,72,248
TR 64,247,49152
Mvaa 68,76,252
TR 66,247,53248
Mvaa 64,72,232
TR 68,247,57344
Mvaa 64,76,236
WL 7
TR 70,247,61440
Mvaa 0,80,192
TR 72,226,49152
Mvaa 0,84,196
TR 74,226,53248
Mvaa 4,80,208
TR 76,226,57344
Mvaa 4,84,212
WL 7
TR 78,226,61440
CV 64,64,65
Mvaa 4,88,216
TR 80,248,32768
CV 65,66,67
CV 66,68,69
Mvaa 4,92,220
TR 82,248,36864
CV 67,70,71
Mvaa 0,88,200
s_add_u32 s16, s30, 8394240
buffer_store_dwordx4 v[64:67], v234, s[72:75], s16 offen sc1
TR 84,248,40960
Mvaa 0,92,204
WL 7
TR 86,248,45056
Mvaa 0,64,224
TR 88,227,32768
Mvaa 0,68,228
TR 90,227,36864
Mvaa 4,64,240
TR 92,227,40960
Mvaa 4,68,244
WL 7
TR 94,227,45056
Mvaa 4,72,248
TR 64,248,49152
Mvaa 4,76,252
TR 66,248,53248
Mvaa 0,72,232
)"
R"(TR 68,248,57344
Mvaa 0,76,236
WL 7
TR 70,248,61440
Mvaa 8,80,192
TR 72,227,49152
Mvaa 8,84,196
TR 74,227,53248
Mvaa 12,80,208
TR 76,227,57344
Mvaa 12,84,212
WL 7
TR 78,227,61440
Mvaa 12,88,216
Mvaa 12,92,220
Mvaa 8,88,200
Mvaa 8,92,204
WL 4
Mvaa 8,64,224
Mvaa 8,68,228
Mvaa 12,64,240
Mvaa 12,68,244
WL 0
Mvaa 12,72,248
Mvaa 12,76,252
Mvaa 8,72,232
Mvaa 8,76,236
buffer_load_dwordx4 v[96:99], v234, s[72:75], s20 offen offset:0 sc1
buffer_load_dwordx4 v[100:103], v234, s[72:75], s20 offen offset:1024 sc1
buffer_load_dwordx2 v[104:105], v74, s[72:75], s20 offen offset:2048 sc1
buffer_load_dwordx4 v[106:109], v234, s[72:75], s21 offen offset:0 sc1
buffer_load_dwordx4 v[110:113], v234, s[72:75], s21 offen offset:1024 sc1
buffer_load_dwordx2 v[114:115], v74, s[72:75], s21 offen offset:2048 sc1
buffer_load_dwordx4 v[116:119], v234, s[72:75], s22 offen offset:0 sc1
buffer_load_dwordx4 v[120:123], v234, s[72:75], s22 offen offset:1024 sc1
buffer_load_dwordx2 v[124:125], v74, s[72:75], s22 offen offset:2048 sc1
s_waitcnt vmcnt(18)
s_branch Ltail_fused_block
Lslow9_fused_block:
s_add_u32 s98, s36, 265216
v_and_b32 v249, 3, v255
v_lshlrev_b32 v249, 7, v249
s_mov_b32 s99, 0x20000
Lpoll10_fused_block:
buffer_load_dwordx2 v[250:251], v249, s[72:75], s98 offen sc1
s_waitcnt vmcnt(0)
v_cmp_ne_u64 vcc, s[82:83], v[250:251]
s_sub_u32 s99, s99, 1
s_cbranch_scc1 Lpolld10_fused_block
s_nop 2
s_cbranch_vccnz Lpoll10_fused_block
Lpolld10_fused_block:
s_branch Lslowb9_fused_block
Lslow10_fused_block:
s_add_u32 s98, s36, 265728
v_and_b32 v249, 3, v255
v_lshlrev_b32 v249, 7, v249
s_mov_b32 s99, 0x20000
Lpoll11_fused_block:
buffer_load_dwordx2 v[250:251], v249, s[72:75], s98 offen sc1
s_waitcnt vmcnt(0)
v_cmp_ne_u64 vcc, s[82:83], v[250:251]
s_sub_u32 s99, s99, 1
s_cbranch_scc1 Lpolld11_fused_block
s_nop 2
s_cbranch_vccnz Lpoll11_fused_block
Lpolld11_fused_block:
s_branch Lslowb10_fused_block
Lslow11_fused_block:
s_add_u32 s98, s91, 393216
v_and_b32 v249, 3, v255
v_lshlrev_b32 v249, 7, v249
s_mov_b32 s99, 0x20000
Lpoll12_fused_block:
buffer_load_dwordx2 v[250:251], v249, s[72:75], s98 offen sc1
s_waitcnt vmcnt(0)
v_cmp_ne_u64 vcc, s[82:83], v[250:251]
s_sub_u32 s99, s99, 1
s_cbranch_scc1 Lpolld12_fused_block
s_nop 2
s_cbranch_vccnz Lpoll12_fused_block
Lpolld12_fused_block:
s_branch Lslowb11_fused_block
Lend_fused_block:
.purgem WL
.purgem DM
.purgem CV
.purgem GL
.purgem TR
.purgem Mvaa
.purgem Maaa
.purgem Mvav
)"
    :: "s"(ka), "s"(bid), "s"(wid), "s"((unsigned)(size_t)(__attribute__((address_space(3))) char*)lds)
    :       "v0","v1","v2","v3","v4","v5","v6","v7","v8","v9","v10","v11","v12","v13","v14","v15","v16","v17","v18","v19","v20","v21","v22","v23",
      "v24","v25","v26","v27","v28","v29","v30","v31","v32","v33","v34","v35","v36","v37","v38","v39","v40","v41","v42","v43","v44","v45","v46","v47",
      "v48","v49","v50","v51","v52","v53","v54","v55","v56","v57","v58","v59","v60","v61","v62","v63","v64","v65","v66","v67","v68","v69","v70","v71",
      "v72","v73","v74","v75","v76","v77","v78","v79","v80","v81","v82","v83","v84","v85","v86","v87","v88","v89","v90","v91","v92","v93","v94","v95",
      "v96","v97","v98","v99","v100","v101","v102","v103","v104","v105","v106","v107","v108","v109","v110","v111","v112","v113","v114","v115","v116","v117","v118","v119",
      "v120","v121","v122","v123","v124","v125","v126","v127","v128","v129","v130","v131","v132","v133","v134","v135","v136","v137","v138","v139","v140","v141","v142","v143",
      "v144","v145","v146","v147","v148","v149","v150","v151","v152","v153","v154","v155","v156","v157","v158","v159","v160","v161","v162","v163","v164","v165","v166","v167",
      "v168","v169","v170","v171","v172","v173","v174","v175","v176","v177","v178","v179","v180","v181","v182","v183","v184","v185","v186","v187","v188","v189","v190","v191",
      "v192","v193","v194","v195","v196","v197","v198","v199","v200","v201","v202","v203","v204","v205","v206","v207","v208","v209","v210","v211","v212","v213","v214","v215",
      "v216","v217","v218","v219","v220","v221","v222","v223","v224","v225","v226","v227","v228","v229","v230","v231","v232","v233","v234","v235","v236","v237","v238","v239",
      "v240","v241","v242","v243","v244","v245","v246","v247","v248","v249","v250","v251","v252","v253","v254","v255","a0","a1","a2","a3","a4","a5","a6","a7",
      "a8","a9","a10","a11","a12","a13","a14","a15","a16","a17","a18","a19","a20","a21","a22","a23","a24","a25","a26","a27","a28","a29","a30","a31",
      "a32","a33","a34","a35","a36","a37","a38","a39","a40","a41","a42","a43","a44","a45","a46","a47","a48","a49","a50","a51","a52","a53","a54","a55",
      "a56","a57","a58","a59","a60","a61","a62","a63","a64","a65","a66","a67","a68","a69","a70","a71","a72","a73","a74","a75","a76","a77","a78","a79",
      "a80","a81","a82","a83","a84","a85","a86","a87","a88","a89","a90","a91","a92","a93","a94","a95","a96","a97","a98","a99","a100","a101","a102","a103",
      "a104","a105","a106","a107","a108","a109","a110","a111","a112","a113","a114","a115","a116","a117","a118","a119","a120","a121","a122","a123","a124","a125","a126","a127",
      "a128","a129","a130","a131","a132","a133","a134","a135","a136","a137","a138","a139","a140","a141","a142","a143","a144","a145","a146","a147","a148","a149","a150","a151",
      "a152","a153","a154","a155","a156","a157","a158","a159","a160","a161","a162","a163","a164","a165","a166","a167","a168","a169","a170","a171","a172","a173","a174","a175",
      "a176","a177","a178","a179","a180","a181","a182","a183","a184","a185","a186","a187","a188","a189","a190","a191","a192","a193","a194","a195","a196","a197","a198","a199",
      "a200","a201","a202","a203","a204","a205","a206","a207","a208","a209","a210","a211","a212","a213","a214","a215","a216","a217","a218","a219","a220","a221","a222","a223",
      "a224","a225","a226","a227","a228","a229","a230","a231","a232","a233","a234","a235","a236","a237","a238","a239","a240","a241","a242","a243","a244","a245","a246","a247",
      "a248","a249","a250","a251","a252","a253","a254","a255","s6","s7","s8","s9","s10","s11","s12","s13","s14","s15","s16","s17","s18","s19","s20","s21",
      "s22","s23","s24","s25","s26","s27","s28","s29","s30","s31","s32","s33","s34","s35","s36","s37","s38","s39","s40","s41","s42","s43","s44","s45",
      "s46","s47","s48","s49","s50","s51","s52","s53","s54","s55","s56","s57","s58","s59","s60","s61","s62","s63","s64","s65","s66","s67","s68","s69",
      "s70","s71","s72","s73","s74","s75","s76","s77","s78","s79","s80","s81","s82","s83","s84","s85","s86","s87","s88","s89","s90","s91","s92","s93",
      "s94","s95","s96","s97","s98","s99","vcc","scc","memory");
}

extern "C" void kernel_launch(void* const* d_in, const int* in_sizes, int n_in, void* d_out, int out_size, void* d_ws, size_t ws_size,
                              hipStream_t stream) {
  hipLaunchKernelGGL(fused_block, dim3(256), dim3(256), 0, stream, (const void*)d_in[0], (const void*)d_in[1], (const void*)d_in[2],
                     (const void*)d_in[3], (const void*)d_in[4], (const void*)d_in[6], (const void*)d_in[7], d_out, d_ws);
}
```
